```python
import torch

def _softmax(x, axis=-1):
    return torch.softmax(x, dim=axis)

def _gelu(x):
    return torch.nn.functional.gelu(x, approximate='tanh')

def _silu(x):
    return torch.nn.functional.silu(x)

def _sigmoid(x):
    return torch.sigmoid(x)

def _layernorm(x, axis=-1, eps=1e-05):
    m = x.mean(dim=axis, keepdim=True)
    v = x.var(dim=axis, keepdim=True, unbiased=False)
    return (x - m) / torch.sqrt(v + eps)

def _scatter_add(tgt, idx, src, axis=0):
    return tgt.clone().index_add_(axis, idx, src)

def _segment_sum(src, seg, n):
    out = torch.zeros((n,) + src.shape[1:], dtype=src.dtype)
    return out.index_add_(0, seg, src)

def _pad(x, pw):
    flat = tuple((v for pair in reversed(pw) for v in pair))
    return torch.nn.functional.pad(x, flat)
kernel_name = 'recurrent_scan__sliding_local_dilated__mixer_gmlp__prompt'

def setup_inputs():

    _w = lambda a: (lambda t: t.to(torch.bfloat16) if t.is_floating_point() else t)(torch.as_tensor(a))

    def _A_setup():
        B, S, D = (1, 2048, 512)
        return {'x': _w((lambda *s: torch.randn(*s, dtype=torch.bfloat16) * 0.05)(B, S, D)), 'wqkv': _w((lambda *s: torch.randn(*s, dtype=torch.bfloat16) * 0.05)(D, 3 * D)), 'wo': _w((lambda *s: torch.randn(*s, dtype=torch.bfloat16) * 0.05)(D, D))}
    _A_in = _A_setup()

    def _B_setup():
        B, S, D, F = (1, 2048, 512, 1376)
        Fh = F // 2
        return {'x': _w((lambda *s: torch.randn(*s, dtype=torch.bfloat16) * 0.05)(B, S, D)), 'w_in': _w((lambda *s: torch.randn(*s, dtype=torch.bfloat16) * 0.4)(D, 2 * Fh)), 'w_sp': _w(0.01 * (lambda *s: torch.randn(*s, dtype=torch.bfloat16) * 0.4)(S, S)), 'b_sp': _w(1.0 + 0.01 * (lambda *s: torch.randn(*s, dtype=torch.bfloat16) * 0.05)(S)), 'w_out': _w((lambda *s: torch.randn(*s, dtype=torch.bfloat16) * 0.05)(Fh, D))}
    _B_in = _B_setup()
    return {'A_x': _A_in['x'], 'A_wqkv': _A_in['wqkv'], 'A_wo': _A_in['wo'], 'B_x': _B_in['x'], 'B_w_in': _B_in['w_in'], 'B_w_sp': _B_in['w_sp'], 'B_b_sp': _B_in['b_sp'], 'B_w_out': _B_in['w_out']}

def reference(A_x, A_wqkv, A_wo, B_x, B_w_in, B_w_sp, B_b_sp, B_w_out):

    def _A_ref(x, wqkv, wo):
        B, S, D = x.shape
        H = 32
        Dh = D // H
        W, dil = (8, 2)
        qkv = (lambda x, *d: x.permute(*d))((x @ wqkv).reshape(B, S, 3, H, Dh), 2, 0, 3, 1, 4)
        q, k, v = (qkv[0], qkv[1], qkv[2])
        scale = Dh ** (-0.5)
        pos = torch.arange(S)
        off = torch.arange(W) * dil
        idx = pos[:, None] - off[None, :]
        mask = idx >= 0
        idx_c = torch.where(mask, idx, 0).reshape(-1)
        kg = k[:, :, idx_c, :].reshape(B, H, S, W, Dh)
        vg = v[:, :, idx_c, :].reshape(B, H, S, W, Dh)
        logits = torch.einsum('bhsd,bhswd->bhsw', q, kg) * scale
        logits = torch.where(mask[None, None, :, :], logits, -1000000000.0)
        attn = _softmax(logits, axis=-1)
        o = torch.einsum('bhsw,bhswd->bhsd', attn, vg)
        return (lambda x, *d: x.permute(*d))(o, 0, 2, 1, 3).reshape(B, S, D) @ wo

    def _B_ref(x, w_in, w_sp, b_sp, w_out):
        B, S, D = x.shape
        F = w_in.shape[-1]
        Fh = F // 2
        z = _gelu(torch.einsum('bsd,df->bsf', x, w_in))
        z1 = z[..., :Fh].clone()
        z2 = _layernorm(z[..., Fh:], axis=-1)
        g = torch.einsum('ts,bsf->btf', w_sp, z2) + b_sp[None, :, None]
        return torch.einsum('bsf,fd->bsd', z1 * g, w_out)
    x = A_x
    xn = _layernorm(x, axis=-1)
    s = _A_ref(xn, A_wqkv, A_wo)
    g = _silu(_B_ref(xn, B_w_in, B_w_sp, B_b_sp, B_w_out))
    return x + s * g
```

```cpp
#include <hip/hip_runtime.h>
#include <hip/hip_bf16.h>
typedef unsigned short u16;
static __device__ __forceinline__ float b2f(u16 v){ return __uint_as_float(((unsigned)v)<<16); }
static __device__ __forceinline__ u16 f2b(float f){ unsigned u=__float_as_uint(f); u += 0x7fffu + ((u>>16)&1u); return (u16)(u>>16); }
static __device__ __forceinline__ float rb(float f){ return b2f(f2b(f)); }
constexpr int S=2048, D=512, F=1376, FH=688, NH=32, DH=16;

__global__ void k_ln(const u16* x, u16* xn, int cols, int ldx, int ldo){
  int row=blockIdx.x; int t=threadIdx.x; __shared__ float red[256];
  float s=0; for(int c=t;c<cols;c+=256) s+=b2f(x[row*ldx+c]);
  red[t]=s; __syncthreads(); for(int o=128;o>0;o>>=1){ if(t<o) red[t]+=red[t+o]; __syncthreads(); }
  float m=red[0]/cols; __syncthreads();
  float q=0; for(int c=t;c<cols;c+=256){ float d=b2f(x[row*ldx+c])-m; q+=d*d; }
  red[t]=q; __syncthreads(); for(int o=128;o>0;o>>=1){ if(t<o) red[t]+=red[t+o]; __syncthreads(); }
  float v=red[0]/cols; float r=1.0f/sqrtf(v+1e-5f);
  for(int c=t;c<cols;c+=256) xn[row*ldo+c]=f2b((b2f(x[row*ldx+c])-m)*r);
}
__global__ void k_gemm(const u16* A, int lda, const u16* B, int ldb, u16* C, int ldc, int M, int N, int K, int mode){
  int n=blockIdx.x*64+threadIdx.x%64; int m=blockIdx.y*4+threadIdx.x/64;
  if(n>=N||m>=M) return;
  float acc=0; for(int k=0;k<K;k++) acc+=b2f(A[m*lda+k])*b2f(B[k*ldb+n]);
  if(mode==1){ float v=rb(acc); float t=tanhf(0.7978845608028654f*(v+0.044715f*v*v*v)); acc=0.5f*v*(1.0f+t); }
  C[m*ldc+n]=f2b(acc);
}
__global__ void k_attn(const u16* qkv, u16* o){
  int t=blockIdx.x; int h=threadIdx.x; if(h>=NH) return;
  float q[16]; for(int d=0;d<16;d++) q[d]=b2f(qkv[t*1536+h*16+d]);
  float lg[8]; float mx=-1e30f;
  for(int w=0;w<8;w++){ int j=t-2*w; if(j>=0){ float a=0; for(int d=0;d<16;d++) a+=q[d]*b2f(qkv[j*1536+512+h*16+d]); lg[w]=a*0.25f; } else lg[w]=-1e9f; mx=fmaxf(mx,lg[w]); }
  float sum=0; for(int w=0;w<8;w++){ lg[w]=__expf(lg[w]-mx); sum+=lg[w]; }
  for(int d=0;d<16;d++){ float a=0; for(int w=0;w<8;w++){ int j=t-2*w; if(j<0) j=0; a+=lg[w]/sum*b2f(qkv[j*1536+1024+h*16+d]); } o[t*512+h*16+d]=f2b(a); }
}
__global__ void k_u(const u16* z, const u16* g, const u16* b, u16* u){
  int i=blockIdx.x*256+threadIdx.x; if(i>=S*FH) return; int t=i/FH, f=i%FH;
  float gg=rb(b2f(g[i])+b2f(b[t])); u[i]=f2b(b2f(z[t*F+f])*gg);
}
__global__ void k_fin(const u16* x, const u16* s, const u16* gb, u16* out){
  int i=blockIdx.x*256+threadIdx.x; if(i>=S*D) return;
  float v=b2f(gb[i]); float sl=rb(v/(1.0f+__expf(-v))); out[i]=f2b(b2f(x[i])+rb(b2f(s[i])*sl));
}
extern "C" void kernel_launch(void* const* d_in, const int* in_sizes, int n_in, void* d_out, int out_size, void* d_ws, size_t ws_size, hipStream_t stream){
  const u16* x=(const u16*)d_in[0]; const u16* wqkv=(const u16*)d_in[1]; const u16* wo=(const u16*)d_in[2];
  const u16* w_in=(const u16*)d_in[4]; const u16* w_sp=(const u16*)d_in[5]; const u16* b_sp=(const u16*)d_in[6]; const u16* w_out=(const u16*)d_in[7];
  u16* ws=(u16*)d_ws; u16* xn=ws; u16* qkv=xn+S*D; u16* z=qkv+S*1536; u16* z2n=z+S*F; u16* g=z2n+S*FH; u16* u=g+S*FH; u16* o=u+S*FH; u16* s=o+S*D; u16* gb=s+S*D;
  k_ln<<<S,256,0,stream>>>(x,xn,D,D,D);
  k_gemm<<<dim3(1536/64,S/4),256,0,stream>>>(xn,D,wqkv,1536,qkv,1536,S,1536,D,0);
  k_gemm<<<dim3((F+63)/64,S/4),256,0,stream>>>(xn,D,w_in,F,z,F,S,F,D,1);
  k_ln<<<S,256,0,stream>>>(z+FH,z2n,FH,F,FH);
  k_gemm<<<dim3((FH+63)/64,S/4),256,0,stream>>>(w_sp,S,z2n,FH,g,FH,S,FH,S,0);
  k_u<<<(S*FH+255)/256,256,0,stream>>>(z,g,b_sp,u);
  k_attn<<<S,64,0,stream>>>(qkv,o);
  k_gemm<<<dim3(D/64,S/4),256,0,stream>>>(o,D,wo,D,s,D,S,D,D,0);
  k_gemm<<<dim3(D/64,S/4),256,0,stream>>>(u,FH,w_out,D,gb,D,S,D,FH,0);
  k_fin<<<(S*D+255)/256,256,0,stream>>>(x,s,gb,(u16*)d_out);
}
```

```cpp
#include <hip/hip_runtime.h>

typedef unsigned short u16;
typedef unsigned long long u64;
typedef __bf16 bf16x8 __attribute__((ext_vector_type(8)));
typedef __bf16 bf16x2 __attribute__((ext_vector_type(2)));
typedef short s16x4 __attribute__((ext_vector_type(4)));
typedef float f32x4 __attribute__((ext_vector_type(4)));
typedef float f32x2 __attribute__((ext_vector_type(2)));
typedef unsigned u32x4 __attribute__((ext_vector_type(4)));
typedef unsigned u32x2 __attribute__((ext_vector_type(2)));
#define LAS __attribute__((address_space(3)))
#define GAS __attribute__((address_space(1)))
#define DEV __device__ __forceinline__

constexpr unsigned WS_EPOCH = 0;
constexpr unsigned WS_FZ2 = 4096;
constexpr unsigned WS_FZ1 = WS_FZ2 + 128 * 128;
constexpr unsigned WS_FO  = WS_FZ1 + 128 * 128;
constexpr unsigned WS_FU  = WS_FO + 128 * 128;
constexpr unsigned WS_XS  = 0x20000;
constexpr unsigned WS_ZS  = 0x30000;
constexpr unsigned WS_Z2N = 0x100000;
constexpr unsigned WS_Z1  = 0x400000;
constexpr unsigned WS_U   = 0x700000;
constexpr unsigned WS_O   = 0xA00000;
constexpr unsigned WS_DBG = 0xD00000;
constexpr unsigned WS_USED = 0xE00000;

constexpr unsigned LDS_BYTES = 160 * 1024;
constexpr unsigned A_SLOT = 43008, A_W = 18432;
constexpr unsigned Z_SLOT = 28672, Z_W = 16384;
constexpr unsigned G_SLOT = 20480, G_Z = 8192;
constexpr unsigned T_BASE = 143360;
constexpr unsigned T_EX2 = T_BASE;
constexpr unsigned T_XS = T_BASE + 8192;
constexpr unsigned T_CS = T_XS + 1280;
constexpr unsigned T_ZP = T_CS + 1024;
constexpr unsigned T_ZQ = T_ZP + 2048;
constexpr unsigned T_ZF = T_ZQ + 4096;
static_assert(T_ZF + 1024 <= LDS_BYTES, "lds");

constexpr unsigned SPIN_CAP = 200000;

#define STAMP(k) do {} while (0)

DEV const char* uni(const char* p) {
  u64 v = (u64)(size_t)p;
  unsigned lo = __builtin_amdgcn_readfirstlane((unsigned)v), hi = __builtin_amdgcn_readfirstlane((unsigned)(v >> 32));
  return (const char*)(size_t)(((u64)hi << 32) | lo);
}
DEV unsigned uni(unsigned v) { return __builtin_amdgcn_readfirstlane(v); }
DEV void dma16(unsigned voff, const char* sbase, unsigned lds) {
  asm volatile("s_mov_b32 m0, %2\n\ts_nop 0\n\tglobal_load_lds_dwordx4 %0, %1" ::"v"(voff), "s"(uni(sbase)), "s"(uni(lds)) : "memory");
}
DEV void dma16c(unsigned voff, const char* sbase, unsigned lds) {
  asm volatile("s_mov_b32 m0, %2\n\ts_nop 0\n\tglobal_load_lds_dwordx4 %0, %1 sc1" ::"v"(voff), "s"(uni(sbase)), "s"(uni(lds)) : "memory");
}
DEV void dma16m(unsigned voff, const char* sbase, unsigned lds, u64 mask) {
  asm volatile("s_mov_b32 m0, %2\n\ts_mov_b64 exec, %3\n\tglobal_load_lds_dwordx4 %0, %1\n\ts_mov_b64 exec, -1"
               ::"v"(voff), "s"(uni(sbase)), "s"(uni(lds)), "s"(mask) : "memory");
}
DEV void dma16mc(unsigned voff, const char* sbase, unsigned lds, u64 mask) {
  asm volatile("s_mov_b32 m0, %2\n\ts_mov_b64 exec, %3\n\tglobal_load_lds_dwordx4 %0, %1 sc1\n\ts_mov_b64 exec, -1"
               ::"v"(voff), "s"(uni(sbase)), "s"(uni(lds)), "s"(mask) : "memory");
}
template <int N> DEV void wait_vm() { asm volatile("s_waitcnt vmcnt(%0)" ::"n"(N) : "memory"); }
DEV void bar() { asm volatile("s_waitcnt lgkmcnt(0)\n\ts_barrier" ::: "memory"); }

DEV f32x4 mfma32(u32x4 a, u32x4 b, f32x4 c) {
  return __builtin_amdgcn_mfma_f32_16x16x32_bf16(__builtin_bit_cast(bf16x8, a), __builtin_bit_cast(bf16x8, b), c, 0, 0, 0);
}
DEV f32x4 mfma16(u32x2 a, u32x2 b, f32x4 c) {
  return __builtin_amdgcn_mfma_f32_16x16x16bf16_1k(__builtin_bit_cast(s16x4, a), __builtin_bit_cast(s16x4, b), c, 0, 0, 0);
}
DEV u32x4 lds128(LAS char* p) { return *(LAS u32x4*)p; }
DEV u32x2 lds64(LAS char* p) { return *(LAS u32x2*)p; }
DEV u32x4 trfrag(LAS char* p, int off4) {
  s16x4 a = __builtin_amdgcn_ds_read_tr16_b64_v4i16((LAS s16x4*)p);
  s16x4 b = __builtin_amdgcn_ds_read_tr16_b64_v4i16((LAS s16x4*)(p + off4));
  u32x2 ua = __builtin_bit_cast(u32x2, a), ub = __builtin_bit_cast(u32x2, b);
  u32x4 r = {ua.x, ua.y, ub.x, ub.y};
  return r;
}
DEV unsigned pk2(float a, float b) {
  f32x2 v = {a, b};
  bf16x2 h = __builtin_convertvector(v, bf16x2);
  return __builtin_bit_cast(unsigned, h);
}
DEV float blo(unsigned u) { return __uint_as_float(u << 16); }
DEV float bhi(unsigned u) { return __uint_as_float(u & 0xffff0000u); }
template <int CTRL> DEV float dpp(float v) {
  return __uint_as_float(__builtin_amdgcn_update_dpp(0u, __float_as_uint(v), CTRL, 0xf, 0xf, true));
}
DEV float sum16(float v) {
  v += dpp<0x128>(v); v += dpp<0x124>(v); v += dpp<0x122>(v); v += dpp<0x121>(v);
  return v;
}
DEV float sumq(float v) {
  unsigned u = __float_as_uint(v);
  auto a = __builtin_amdgcn_permlane16_swap(u, u, false, false);
  v = __uint_as_float(a[0]) + __uint_as_float(a[1]);
  u = __float_as_uint(v);
  auto b = __builtin_amdgcn_permlane32_swap(u, u, false, false);
  return __uint_as_float(b[0]) + __uint_as_float(b[1]);
}
DEV float maxq(float v) {
  unsigned u = __float_as_uint(v);
  auto a = __builtin_amdgcn_permlane16_swap(u, u, false, false);
  v = fmaxf(__uint_as_float(a[0]), __uint_as_float(a[1]));
  u = __float_as_uint(v);
  auto b = __builtin_amdgcn_permlane32_swap(u, u, false, false);
  return fmaxf(__uint_as_float(b[0]), __uint_as_float(b[1]));
}
DEV u32x4 pairswap(u32x2 b0, u32x2 b1) {
  auto s0 = __builtin_amdgcn_permlane16_swap(b0.x, b1.x, false, false);
  auto s1 = __builtin_amdgcn_permlane16_swap(b0.y, b1.y, false, false);
  u32x4 r = {s0[0], s1[0], s0[1], s1[1]};
  return r;
}
DEV float gelu_t(float v) {
  float e = __builtin_amdgcn_exp2f(v * __builtin_fmaf(v * v, -0.10294324f, -2.3022082f));
  return v * __builtin_amdgcn_rcpf(1.0f + e);
}
DEV u64 gload64(const char* p) { return __hip_atomic_load((GAS u64*)(size_t)p, __ATOMIC_RELAXED, __HIP_MEMORY_SCOPE_AGENT); }
DEV void gstore64(const char* p, u64 v) { __hip_atomic_store((GAS u64*)(size_t)p, v, __ATOMIC_RELAXED, __HIP_MEMORY_SCOPE_AGENT); }
DEV void poll_flags(const char* p, bool active, u64 T) {
  for (unsigned it = 0; it < SPIN_CAP; ++it) {
    bool ok = !active || gload64(p) == T;
    if (__all(ok)) break;
    __builtin_amdgcn_s_sleep(1);
  }
}
DEV u32x4 settle_pair(u32x4 g, __amdgpu_buffer_rsrc_t rs, unsigned off, bool active, unsigned T32) {
  for (unsigned it = 0; it < SPIN_CAP; ++it) {
    bool ok = !active || (g.y == T32 && g.w == T32);
    if (__all(ok)) break;
    __builtin_amdgcn_s_sleep(1);
    g = __builtin_amdgcn_raw_buffer_load_b128(rs, off, 0, 16);
  }
  return g;
}
DEV u32x4 aload128c(unsigned voff, const char* sbase) {
  u32x4 r;
  asm volatile("global_load_dwordx4 %0, %1, %2 sc1" : "=v"(r) : "v"(voff), "s"(uni(sbase)) : "memory");
  return r;
}
DEV u32x2 aload64c(unsigned voff, const char* sbase) {
  u32x2 r;
  asm volatile("global_load_dwordx2 %0, %1, %2 sc1" : "=v"(r) : "v"(voff), "s"(uni(sbase)) : "memory");
  return r;
}

struct Ctx {
  const char *x, *wqkv, *wo, *win, *wsp, *bsp, *wout;
  char *out, *ws;
  LAS char* L;
  unsigned lb;
  int tid, lane, w, bid;
  unsigned Tlo, Thi;
  __amdgpu_buffer_rsrc_t rs;
};
DEV u64 tagT(const Ctx& c) { return ((u64)c.Thi << 32) | c.Tlo; }

DEV void s0_issue(const Ctx& c, u32x4& xr, u32x2& ev, int row) {
  ev = aload64c(0, c.ws + WS_EPOCH);
  asm volatile("global_load_dwordx4 %0, %1, %2" : "=v"(xr) : "v"(c.lane * 16), "s"(uni(c.x + (size_t)row * 1024)) : "memory");
}
template <int P> DEV void s0_finish(Ctx& c, u32x4 xr, u32x2 ev, int row) {
  asm volatile("s_waitcnt vmcnt(%2) ; TIE %0 %1" : "+v"(xr), "+v"(ev) : "n"(P) : "memory");
  u64 e = (((u64)(unsigned)__builtin_amdgcn_readfirstlane(ev.y)) << 32) | (unsigned)__builtin_amdgcn_readfirstlane(ev.x);
  e += 1;
  c.Tlo = (unsigned)e; c.Thi = (unsigned)(e >> 32);
  unsigned x0 = xr.x, x1 = xr.y, x2 = xr.z, x3 = xr.w;
  float v[8] = {blo(x0), bhi(x0), blo(x1), bhi(x1), blo(x2), bhi(x2), blo(x3), bhi(x3)};
  float s = ((v[0] + v[1]) + (v[2] + v[3])) + ((v[4] + v[5]) + (v[6] + v[7]));
  s = sumq(sum16(s));
  float m = s * (1.0f / 512.0f), q = 0.f;
#pragma unroll
  for (int j = 0; j < 8; ++j) { float d = v[j] - m; q = __builtin_fmaf(d, d, q); }
  q = sumq(sum16(q));
  float r = 1.0f / sqrtf(q * (1.0f / 512.0f) + 1e-5f);
  if (c.lane < 2)
    gstore64(c.ws + WS_XS + row * 16 + c.lane * 8, ((u64)c.Tlo << 32) | __float_as_uint(c.lane ? r : m));
}

struct G3A { unsigned vw; const char* bw; };
DEV G3A g3_addr(const Ctx& c, int rt) {
  int r = 8 * c.w + (c.lane >> 3);
  G3A a;
  a.vw = r * 4096 + (((c.lane & 7) ^ ((r >> 1) & 7)) << 4);
  a.bw = c.wsp + (size_t)rt * 64 * 4096;
  return a;
}
DEV void g3_prefetch_w(const Ctx& c, const G3A& a) {
#pragma unroll
  for (int s = 0; s < 6; ++s) dma16(a.vw, a.bw + s * 128, c.lb + s * G_SLOT + c.w * 1024);
}

struct AAddr { unsigned vx0, vx1, vh, vw; const char *bx, *bh, *bw; u64 hmask; };
DEV void a_issue(const Ctx& c, const AAddr& a, int s, unsigned slot) {
  const unsigned l = c.lb + slot;
  dma16(a.vx0, a.bx + s * 128, l + c.w * 2048);
  dma16(a.vx1, a.bx + s * 128, l + c.w * 2048 + 1024);
  dma16m(a.vh, a.bh + s * 128, l + 128 * 128 + c.w * 256, a.hmask);
  const char* bw = a.bw + (size_t)s * 64 * 3072;
  dma16(a.vw, bw, l + A_W + c.w * 1024);
  dma16(a.vw, bw + 1024, l + A_W + 8192 + c.w * 1024);
  dma16(a.vw, bw + 2048, l + A_W + 16384 + c.w * 1024);
}

struct AAcc { f32x4 q[2][2], k[2][2], v[2][2], h, qc; };

template <bool HV> DEV void a_stage(const Ctx& c, AAcc& A, unsigned slot, unsigned xo, unsigned xh, unsigned wA, unsigned wB) {
  LAS char* L = c.L + slot;
#pragma unroll
  for (int kk = 0; kk < 2; ++kk) {
    u32x4 x0 = lds128(L + (xo ^ (kk * 64)));
    u32x4 x1 = lds128(L + (xo ^ (kk * 64)) + 2048);
    u32x4 hx = lds128(L + (xh ^ (kk * 64)));
    LAS char* pa = L + A_W + wA + kk * 4096;
    LAS char* pb = L + A_W + wB + kk * 4096;
    u32x4 qa = trfrag(pa, 512), qb = trfrag(pb, 512);
    u32x4 ka = trfrag(pa + 8192, 512), kb = trfrag(pb + 8192, 512);
    u32x4 va = trfrag(pa + 16384, 512), vb = trfrag(pb + 16384, 512);
    A.q[0][0] = mfma32(qa, x0, A.q[0][0]); A.q[1][0] = mfma32(qa, x1, A.q[1][0]);
    A.q[0][1] = mfma32(qb, x0, A.q[0][1]); A.q[1][1] = mfma32(qb, x1, A.q[1][1]);
    A.k[0][0] = mfma32(ka, x0, A.k[0][0]); A.k[1][0] = mfma32(ka, x1, A.k[1][0]);
    A.k[0][1] = mfma32(kb, x0, A.k[0][1]); A.k[1][1] = mfma32(kb, x1, A.k[1][1]);
    A.v[0][0] = mfma32(x0, va, A.v[0][0]); A.v[1][0] = mfma32(x1, va, A.v[1][0]);
    A.v[0][1] = mfma32(x0, vb, A.v[0][1]); A.v[1][1] = mfma32(x1, vb, A.v[1][1]);
    if (HV) {
      A.h = mfma32(hx, va, A.h);
    } else {
      A.h = mfma32(ka, hx, A.h);
      A.qc = mfma32(qa, hx, A.qc);
    }
  }
  asm volatile("" : "+v"(A.q[0][0]), "+v"(A.q[0][1]), "+v"(A.q[1][0]), "+v"(A.q[1][1]), "+v"(A.k[0][0]), "+v"(A.k[0][1]),
               "+v"(A.k[1][0]), "+v"(A.k[1][1]), "+v"(A.v[0][0]), "+v"(A.v[0][1]), "+v"(A.v[1][0]), "+v"(A.v[1][1]), "+v"(A.h), "+v"(A.qc));
}

template <bool HV> DEV void a_loop(const Ctx& c, AAcc& A, const AAddr& a, unsigned xo, unsigned xh, unsigned wA, unsigned wB,
                                   u32x4& g, unsigned gofs) {
  unsigned slot = 0, nslot = 2 * A_SLOT;
#pragma unroll 1
  for (int s = 0; s < 6; ++s) {
    wait_vm<6>();
    bar();
    a_issue(c, a, s + 2, nslot);
    a_stage<HV>(c, A, slot, xo, xh, wA, wB);
    slot = slot == 2 * A_SLOT ? 0 : slot + A_SLOT;
    nslot = nslot == 2 * A_SLOT ? 0 : nslot + A_SLOT;
  }
  g = aload128c(gofs, c.ws + WS_XS);
  wait_vm<7>();
  bar();
  a_stage<HV>(c, A, 0, xo, xh, wA, wB);
  asm volatile("s_waitcnt vmcnt(0) ; TIE %0" : "+v"(g) : : "memory");
  bar();
  a_stage<HV>(c, A, A_SLOT, xo, xh, wA, wB);
}

DEV void job_a(Ctx& c, int i, int hc, int s0row, const G3A& g3) {
  u32x4 xr; u32x2 ev;
  s0_issue(c, xr, ev, s0row);
  char* ws = c.ws;
  const int lane = c.lane, w = c.w, rg = w & 3, cg = w >> 2;
  const int t = lane & 15, q = lane >> 4;
  AAddr a;
  {
    int r0 = 16 * w + (lane >> 3);
    a.vx0 = r0 * 1024 + (((lane & 7) ^ ((r0 >> 1) & 7)) << 4);
    int r1 = r0 + 8;
    a.vx1 = r1 * 1024 + (((lane & 7) ^ ((r1 >> 1) & 7)) << 4);
    a.bx = c.x + (size_t)i * 128 * 1024;
    int rh = 2 * w + ((lane >> 3) & 1);
    a.vh = rh * 1024 + (((lane & 7) ^ w) << 4);
    a.bh = c.x + (size_t)(i > 0 ? i * 128 - 16 : 0) * 1024;
    a.hmask = w == 0 ? 0xff00ull : 0xffffull;
    int k = 8 * w + (lane >> 3);
    int f = ((k >> 1) & 1) | (((k >> 3) & 1) << 1);
    a.vw = k * 3072 + (((lane & 7) ^ (f << 1)) << 4);
    a.bw = c.wqkv + hc * 128;
  }
  if (c.tid < 24) {
    u32x4 ones = {0x3f803f80u, 0x3f803f80u, 0x3f803f80u, 0x3f803f80u};
    *(LAS u32x4*)(c.L + (c.tid >> 3) * A_SLOT + 128 * 128 + (c.tid & 7) * 16) = ones;
  }
  a_issue(c, a, 0, 0);
  a_issue(c, a, 1, A_SLOT);
  s0_finish<12>(c, xr, ev, s0row);

  const unsigned xlane = t * 128 + ((q ^ (t >> 1)) << 4);
  const unsigned xo = rg * 32 * 128 + xlane, xh = 128 * 128 + xlane;
  const int f = ((t >> 3) & 1) | ((q & 1) << 1);
  const int hA = 2 * cg + (rg & 1), hB = hA ^ 1;
  const unsigned wl = (8 * q + (t >> 2)) * 128 + (t & 3) * 8;
  const unsigned wA = wl + ((hA ^ f) << 5), wB = wl + ((hB ^ f) << 5);
  const int gj = c.tid < 144 ? c.tid : 143;
  const int gtok = gj < 128 ? i * 128 + gj : (i > 0 ? i * 128 - 16 + (gj - 128) : gj - 128);
  const unsigned gofs = gtok * 16;

  AAcc A;
  const f32x4 z4 = {0.f, 0.f, 0.f, 0.f};
#pragma unroll
  for (int x = 0; x < 2; ++x)
#pragma unroll
    for (int y = 0; y < 2; ++y) { A.q[x][y] = z4; A.k[x][y] = z4; A.v[x][y] = z4; }
  A.h = z4; A.qc = z4;
  u32x4 g = {0, 0, 0, 0};
  const bool hv = rg >= 2;
  if (hv) a_loop<true>(c, A, a, xo, xh, wA, wB, g, gofs);
  else a_loop<false>(c, A, a, xo, xh, wA, wB, g, gofs);
  STAMP(2);
  g = settle_pair(g, c.rs, WS_XS + gofs, true, c.Tlo);
  if (c.tid < 144) { u32x2 mr = {g.x, g.z}; *(LAS u32x2*)(c.L + T_XS + c.tid * 8) = mr; }
  if (!hv) {
    if (t == 0) {
      *(LAS f32x4*)(c.L + T_CS + (64 + hA * 16 + 4 * q) * 4) = A.h;
      *(LAS f32x4*)(c.L + T_CS + (hA * 16 + 4 * q) * 4) = A.qc;
    }
  } else if (lane < 16) {
    *(LAS float*)(c.L + T_CS + (128 + hA * 16 + lane) * 4) = A.h[0];
  }
  bar();
  g3_prefetch_w(c, g3);

  u32x2 Q[2][2], K[2][2], V[2][2], H;
  {
    f32x4 csq[2], csk[2]; float csv[2];
#pragma unroll
    for (int y = 0; y < 2; ++y) {
      int hh = y ? hB : hA;
      csq[y] = *(LAS f32x4*)(c.L + T_CS + (hh * 16 + 4 * q) * 4);
      csk[y] = *(LAS f32x4*)(c.L + T_CS + (64 + hh * 16 + 4 * q) * 4);
      csv[y] = *(LAS float*)(c.L + T_CS + (128 + hh * 16 + t) * 4);
    }
#pragma unroll
    for (int x = 0; x < 2; ++x) {
      f32x2 mr = *(LAS f32x2*)(c.L + T_XS + (rg * 32 + x * 16 + t) * 8);
      f32x4 m01 = *(LAS f32x4*)(c.L + T_XS + (rg * 32 + x * 16 + 4 * q) * 8);
      f32x4 m23 = *(LAS f32x4*)(c.L + T_XS + (rg * 32 + x * 16 + 4 * q + 2) * 8);
      const float rq = mr.y * 0.25f;
#pragma unroll
      for (int y = 0; y < 2; ++y) {
        f32x4 aq = A.q[x][y], ak = A.k[x][y], av = A.v[x][y];
        Q[x][y].x = pk2(rq * (aq[0] - mr.x * csq[y][0]), rq * (aq[1] - mr.x * csq[y][1]));
        Q[x][y].y = pk2(rq * (aq[2] - mr.x * csq[y][2]), rq * (aq[3] - mr.x * csq[y][3]));
        K[x][y].x = pk2(mr.y * (ak[0] - mr.x * csk[y][0]), mr.y * (ak[1] - mr.x * csk[y][1]));
        K[x][y].y = pk2(mr.y * (ak[2] - mr.x * csk[y][2]), mr.y * (ak[3] - mr.x * csk[y][3]));
        V[x][y].x = pk2(m01[1] * (av[0] - m01[0] * csv[y]), m01[3] * (av[1] - m01[2] * csv[y]));
        V[x][y].y = pk2(m23[1] * (av[2] - m23[0] * csv[y]), m23[3] * (av[3] - m23[2] * csv[y]));
      }
    }
    if (!hv) {
      f32x2 mr = *(LAS f32x2*)(c.L + T_XS + (128 + t) * 8);
      H.x = pk2(mr.y * (A.h[0] - mr.x * csk[0][0]), mr.y * (A.h[1] - mr.x * csk[0][1]));
      H.y = pk2(mr.y * (A.h[2] - mr.x * csk[0][2]), mr.y * (A.h[3] - mr.x * csk[0][3]));
    } else {
      f32x4 m01 = *(LAS f32x4*)(c.L + T_XS + (128 + 4 * q) * 8);
      f32x4 m23 = *(LAS f32x4*)(c.L + T_XS + (128 + 4 * q + 2) * 8);
      H.x = pk2(m01[1] * (A.h[0] - m01[0] * csv[0]), m01[3] * (A.h[1] - m01[2] * csv[0]));
      H.y = pk2(m23[1] * (A.h[2] - m23[0] * csv[0]), m23[3] * (A.h[3] - m23[2] * csv[0]));
    }
  }
  const unsigned KX = G_Z, VX = G_SLOT + G_Z;
  {
    *(LAS u32x2*)(c.L + (hv ? VX : KX) + hA * 512 + lane * 8) = H;
    if (rg < 3) {
      *(LAS u32x2*)(c.L + KX + ((rg + 1) * 4 + hA) * 512 + lane * 8) = K[1][0];
      *(LAS u32x2*)(c.L + KX + ((rg + 1) * 4 + hB) * 512 + lane * 8) = K[1][1];
      *(LAS u32x2*)(c.L + VX + ((rg + 1) * 4 + hA) * 512 + lane * 8) = V[1][0];
      *(LAS u32x2*)(c.L + VX + ((rg + 1) * 4 + hB) * 512 + lane * 8) = V[1][1];
    }
  }
  bar();
  u32x2 Kp[2], Vp[2];
  Kp[0] = lds64(c.L + KX + (rg * 4 + hA) * 512 + lane * 8);
  Kp[1] = lds64(c.L + KX + (rg * 4 + hB) * 512 + lane * 8);
  Vp[0] = lds64(c.L + VX + (rg * 4 + hA) * 512 + lane * 8);
  Vp[1] = lds64(c.L + VX + (rg * 4 + hB) * 512 + lane * 8);

  const float NEG = -1e30f, LOG2E = 1.4426950408889634f;
  float bc[4], bp[4];
#pragma unroll
  for (int j = 0; j < 4; ++j) {
    int d = t - (4 * q + j);
    bc[j] = (d >= 0 && !(d & 1)) ? 0.f : NEG;
    int dp = d + 16;
    bp[j] = (dp <= 14 && !(dp & 1)) ? 0.f : NEG;
  }
  const bool first = (i == 0 && rg == 0);
#pragma unroll
  for (int x = 1; x >= 0; --x) {
    u32x2 O[2];
#pragma unroll
    for (int y = 0; y < 2; ++y) {
      u32x2 kp = x ? K[0][y] : Kp[y], vp = x ? V[0][y] : Vp[y];
      f32x4 sp = mfma16(kp, Q[x][y], z4), sc = mfma16(K[x][y], Q[x][y], z4);
      float e[8];
#pragma unroll
      for (int j = 0; j < 4; ++j) {
        e[j] = __builtin_fmaf(sp[j], LOG2E, (x == 0 && first) ? NEG : bp[j]);
        e[4 + j] = __builtin_fmaf(sc[j], LOG2E, bc[j]);
      }
      float mx = fmaxf(fmaxf(fmaxf(e[0], e[1]), fmaxf(e[2], e[3])), fmaxf(fmaxf(e[4], e[5]), fmaxf(e[6], e[7])));
      mx = maxq(mx);
      float sum = 0.f;
#pragma unroll
      for (int j = 0; j < 8; ++j) { e[j] = __builtin_amdgcn_exp2f(e[j] - mx); sum += e[j]; }
      float inv = __builtin_amdgcn_rcpf(sumq(sum));
      u32x4 P = {pk2(e[0] * inv, e[1] * inv), pk2(e[2] * inv, e[3] * inv), pk2(e[4] * inv, e[5] * inv), pk2(e[6] * inv, e[7] * inv)};
      u32x4 VV = {vp.x, vp.y, V[x][y].x, V[x][y].y};
      f32x4 o = mfma32(VV, P, z4);
      O[y].x = pk2(o[0], o[1]); O[y].y = pk2(o[2], o[3]);
    }
    u32x4 fr = pairswap(O[0], O[1]);
    int qp = (((q & 1) << 1) | (q >> 1)) ^ ((rg & 1) << 1);
    unsigned off = WS_O + ((i * 8 + rg * 2 + x) * 16 + 2 * hc + cg) * 1024 + qp * 256 + t * 16;
    __builtin_amdgcn_raw_buffer_store_b128(fr, c.rs, off, 0, 16);
  }
  wait_vm<0>();
  bar();
  if (c.tid == 0) gstore64(ws + WS_FO + (i * 8 + hc) * 128, tagT(c));
  STAMP(3);
}

struct ZAddr { unsigned vx0, vx1, vw; const char *bx, *bw; };
template <int S> DEV void z_issue(const Ctx& c, const ZAddr& a) {
  if (S >= 16) return;
  const unsigned l = c.lb + (S % 5) * Z_SLOT;
  if (S < 8) {
    dma16(a.vx0, a.bx + S * 128, l + c.w * 2048);
    dma16(a.vx1, a.bx + S * 128, l + c.w * 2048 + 1024);
  }
  const char* bw = a.bw + (size_t)(S & 7) * 64 * 2752 + (S < 8 ? 1376 : 0);
  dma16m(a.vw, bw, l + Z_W + c.w * 1536, 0xffffffffffffull);
  dma16m(a.vw, bw + 4 * 2752, l + Z_W + c.w * 1536 + 768, 0xffffffffffffull);
}
constexpr int z_n(int s) { return s >= 16 ? 0 : (s < 8 ? 4 : 2); }
constexpr int z_wait(int s) { return z_n(s + 1) + z_n(s + 2) + z_n(s + 3); }

template <int S> DEV void z_stage_a(const Ctx& c, const ZAddr& a, u32x4 (&xf)[2][16], f32x4 (&acc)[2][3], f32x4& cs, unsigned xo,
                                    const unsigned (&wb)[3], u32x4& g, unsigned gofs) {
  if (S == 7) asm volatile("s_waitcnt vmcnt(4) ; TIE %0" : "+v"(g) : : "memory");
  else wait_vm<z_wait(S)>();
  bar();
  z_issue<S + 4>(c, a);
  if (S == 4) g = aload128c(gofs, c.ws + WS_XS);
  LAS char* L = c.L + (S % 5) * Z_SLOT;
  const u32x4 ones = {0x3f803f80u, 0x3f803f80u, 0x3f803f80u, 0x3f803f80u};
#pragma unroll
  for (int kk = 0; kk < 2; ++kk) {
    xf[0][2 * S + kk] = lds128(L + (xo ^ (kk * 64)));
    xf[1][2 * S + kk] = lds128(L + (xo ^ (kk * 64)) + 2048);
    u32x4 wf[3];
#pragma unroll
    for (int j = 0; j < 3; ++j) wf[j] = trfrag(L + Z_W + wb[j] + kk * 6144, 768);
#pragma unroll
    for (int j = 0; j < 3; ++j) {
      acc[0][j] = mfma32(xf[0][2 * S + kk], wf[j], acc[0][j]);
      acc[1][j] = mfma32(xf[1][2 * S + kk], wf[j], acc[1][j]);
    }
    cs = mfma32(ones, wf[0], cs);
  }
  asm volatile("" : "+v"(acc[0][0]), "+v"(acc[0][1]), "+v"(acc[0][2]), "+v"(acc[1][0]), "+v"(acc[1][1]), "+v"(acc[1][2]), "+v"(cs));
}
template <int S> DEV void z_stage_b(const Ctx& c, const ZAddr& a, const u32x4 (&xf)[2][16], f32x4 (&acc)[2][3], f32x4& cs,
                                    const unsigned (&wb)[3], u32x4 (&g)[2], unsigned gofs, const char* gbase) {
  if (S == 15) asm volatile("s_waitcnt vmcnt(0) ; TIE %0 %1" : "+v"(g[0]), "+v"(g[1]) : : "memory");
  else wait_vm<z_wait(S)>();
  bar();
  z_issue<S + 4>(c, a);
  if (S == 13) { g[0] = aload128c(gofs, gbase); g[1] = aload128c(gofs, gbase + 2048); }
  LAS char* L = c.L + (S % 5) * Z_SLOT;
  const u32x4 ones = {0x3f803f80u, 0x3f803f80u, 0x3f803f80u, 0x3f803f80u};
#pragma unroll
  for (int kk = 0; kk < 2; ++kk) {
    u32x4 wf[3];
#pragma unroll
    for (int j = 0; j < 3; ++j) wf[j] = trfrag(L + Z_W + wb[j] + kk * 6144, 768);
#pragma unroll
    for (int j = 0; j < 3; ++j) {
      acc[0][j] = mfma32(wf[j], xf[0][2 * (S - 8) + kk], acc[0][j]);
      acc[1][j] = mfma32(wf[j], xf[1][2 * (S - 8) + kk], acc[1][j]);
    }
    cs = mfma32(wf[0], ones, cs);
  }
  asm volatile("" : "+v"(acc[0][0]), "+v"(acc[0][1]), "+v"(acc[0][2]), "+v"(acc[1][0]), "+v"(acc[1][1]), "+v"(acc[1][2]), "+v"(cs));
}

DEV int fb_start(int c) { return c < 3 ? 6 * c : 18 + 5 * (c - 3); }

DEV void job_z(Ctx& c, int i, int zc, int s0row, const G3A& g3) {
  u32x4 xr; u32x2 ev;
  s0_issue(c, xr, ev, s0row);
  char* ws = c.ws;
  const int lane = c.lane, w = c.w, rg = w & 3, cg = w >> 2;
  const int t = lane & 15, q = lane >> 4;
  const int fb0 = fb_start(zc), nb = zc < 3 ? 6 : 5;
  ZAddr a;
  {
    int r0 = 16 * w + (lane >> 3);
    a.vx0 = r0 * 1024 + (((lane & 7) ^ ((r0 >> 1) & 7)) << 4);
    int r1 = r0 + 8;
    a.vx1 = r1 * 1024 + (((lane & 7) ^ ((r1 >> 1) & 7)) << 4);
    a.bx = c.x + (size_t)i * 128 * 1024;
    int l48 = lane < 48 ? lane : 47;
    int kr = l48 / 12, cc = l48 % 12;
    int b = (cc >> 1) ^ (w & 1);
    int fbg = fb0 + b; fbg = fbg > 42 ? 42 : fbg;
    a.vw = (8 * w + kr) * 2752 + fbg * 32 + (cc & 1) * 16;
    a.bw = c.win;
  }
  z_issue<0>(c, a); z_issue<1>(c, a); z_issue<2>(c, a); z_issue<3>(c, a);
  s0_finish<16>(c, xr, ev, s0row);

  const unsigned xo = rg * 32 * 128 + t * 128 + ((q ^ (t >> 1)) << 4);
  unsigned wb[3]; int lb3[3];
#pragma unroll
  for (int j = 0; j < 3; ++j) {
    int jr = j + (rg == 3 ? 0 : rg); jr = jr >= 3 ? jr - 3 : jr;
    lb3[j] = 3 * cg + jr;
    wb[j] = (8 * q + (t >> 2)) * 192 + ((lb3[j] ^ (q & 1)) << 5) + (t & 3) * 8;
  }
  const int gj = c.tid < 128 ? c.tid : 127;
  const unsigned gofs = (i * 128 + gj) * 16;
  const f32x4 z4 = {0.f, 0.f, 0.f, 0.f};
  u32x4 xf[2][16];
  f32x4 acc[2][3], cs = z4;
#pragma unroll
  for (int j = 0; j < 3; ++j) { acc[0][j] = z4; acc[1][j] = z4; }
  u32x4 g = {0, 0, 0, 0};
  z_stage_a<0>(c, a, xf, acc, cs, xo, wb, g, gofs); z_stage_a<1>(c, a, xf, acc, cs, xo, wb, g, gofs);
  z_stage_a<2>(c, a, xf, acc, cs, xo, wb, g, gofs); z_stage_a<3>(c, a, xf, acc, cs, xo, wb, g, gofs);
  z_stage_a<4>(c, a, xf, acc, cs, xo, wb, g, gofs); z_stage_a<5>(c, a, xf, acc, cs, xo, wb, g, gofs);
  z_stage_a<6>(c, a, xf, acc, cs, xo, wb, g, gofs); z_stage_a<7>(c, a, xf, acc, cs, xo, wb, g, gofs);
  STAMP(2);
  g = settle_pair(g, c.rs, WS_XS + gofs, true, c.Tlo);
  if (c.tid < 128) { u32x2 mr = {g.x, g.z}; *(LAS u32x2*)(c.L + T_XS + c.tid * 8) = mr; }
  if (rg < 3 && lane < 16) *(LAS float*)(c.L + T_CS + (lb3[0] * 16 + lane) * 4) = cs[0];
  bar();
  u32x2 Z2[2][3];
  {
    float csf[3];
#pragma unroll
    for (int j = 0; j < 3; ++j) csf[j] = *(LAS float*)(c.L + T_CS + (lb3[j] * 16 + t) * 4);
#pragma unroll
    for (int x = 0; x < 2; ++x) {
      f32x4 m01 = *(LAS f32x4*)(c.L + T_XS + (rg * 32 + x * 16 + 4 * q) * 8);
      f32x4 m23 = *(LAS f32x4*)(c.L + T_XS + (rg * 32 + x * 16 + 4 * q + 2) * 8);
      float s1[4] = {0.f, 0.f, 0.f, 0.f}, s2[4] = {0.f, 0.f, 0.f, 0.f};
#pragma unroll
      for (int j = 0; j < 3; ++j) {
        f32x4 v = acc[x][j];
        float y0 = gelu_t(m01[1] * (v[0] - m01[0] * csf[j])), y1 = gelu_t(m01[3] * (v[1] - m01[2] * csf[j]));
        float y2 = gelu_t(m23[1] * (v[2] - m23[0] * csf[j])), y3 = gelu_t(m23[3] * (v[3] - m23[2] * csf[j]));
        unsigned p0 = pk2(y0, y1), p1 = pk2(y2, y3);
        Z2[x][j].x = p0; Z2[x][j].y = p1;
        if (lb3[j] < nb) {
          y0 = blo(p0); y1 = bhi(p0); y2 = blo(p1); y3 = bhi(p1);
          s1[0] += y0; s1[1] += y1; s1[2] += y2; s1[3] += y3;
          s2[0] = __builtin_fmaf(y0, y0, s2[0]); s2[1] = __builtin_fmaf(y1, y1, s2[1]);
          s2[2] = __builtin_fmaf(y2, y2, s2[2]); s2[3] = __builtin_fmaf(y3, y3, s2[3]);
        }
      }
#pragma unroll
      for (int r = 0; r < 4; ++r) { s1[r] = sum16(s1[r]); s2[r] = sum16(s2[r]); }
      if (t == 0) {
        f32x4 o0 = {s1[0], s2[0], s1[1], s2[1]}, o1 = {s1[2], s2[2], s1[3], s2[3]};
        LAS char* p = c.L + T_ZP + (cg * 128 + rg * 32 + x * 16 + 4 * q) * 8;
        *(LAS f32x4*)p = o0; *(LAS f32x4*)(p + 16) = o1;
      }
    }
  }
  bar();
  if (c.tid < 256) {
    int tok = c.tid >> 1, st = c.tid & 1;
    float v = *(LAS float*)(c.L + T_ZP + tok * 8 + st * 4) + *(LAS float*)(c.L + T_ZP + (128 + tok) * 8 + st * 4);
    gstore64(ws + WS_ZS + ((i * 8 + zc) * 128 + tok) * 16 + st * 8, ((u64)c.Tlo << 32) | __float_as_uint(v));
  }
  STAMP(3);
  cs = z4;
#pragma unroll
  for (int j = 0; j < 3; ++j) { acc[0][j] = z4; acc[1][j] = z4; }
  u32x4 gs[2] = {{0, 0, 0, 0}, {0, 0, 0, 0}};
  const int stok = c.tid & 127, pg = c.tid >> 7;
  const unsigned sofs = ((2 * pg) * 128 + stok) * 16;
  const char* sbase = ws + WS_ZS + (size_t)i * 8 * 128 * 16;
  z_stage_b<8>(c, a, xf, acc, cs, wb, gs, sofs, sbase);  z_stage_b<9>(c, a, xf, acc, cs, wb, gs, sofs, sbase);
  z_stage_b<10>(c, a, xf, acc, cs, wb, gs, sofs, sbase); z_stage_b<11>(c, a, xf, acc, cs, wb, gs, sofs, sbase);
  z_stage_b<12>(c, a, xf, acc, cs, wb, gs, sofs, sbase); z_stage_b<13>(c, a, xf, acc, cs, wb, gs, sofs, sbase);
  z_stage_b<14>(c, a, xf, acc, cs, wb, gs, sofs, sbase); z_stage_b<15>(c, a, xf, acc, cs, wb, gs, sofs, sbase);
  STAMP(4);
  {
    const unsigned so = WS_ZS + i * 8 * 128 * 16 + sofs;
    gs[0] = settle_pair(gs[0], c.rs, so, true, c.Tlo);
    gs[1] = settle_pair(gs[1], c.rs, so + 2048, true, c.Tlo);
    f32x2 p = {__uint_as_float(gs[0].x) + __uint_as_float(gs[1].x), __uint_as_float(gs[0].z) + __uint_as_float(gs[1].z)};
    *(LAS f32x2*)(c.L + T_ZQ + (pg * 128 + stok) * 8) = p;
  }
  if (rg < 3 && t == 0) *(LAS f32x4*)(c.L + T_CS + 512 + (lb3[0] * 16 + 4 * q) * 4) = cs;
  bar();
  g3_prefetch_w(c, g3);
  if (c.tid < 128) {
    f32x2 p0 = *(LAS f32x2*)(c.L + T_ZQ + c.tid * 8), p1 = *(LAS f32x2*)(c.L + T_ZQ + (128 + c.tid) * 8);
    f32x2 p2 = *(LAS f32x2*)(c.L + T_ZQ + (256 + c.tid) * 8), p3 = *(LAS f32x2*)(c.L + T_ZQ + (384 + c.tid) * 8);
    float s = (p0.x + p1.x) + (p2.x + p3.x), ss = (p0.y + p1.y) + (p2.y + p3.y);
    float m = s * (1.0f / 688.0f);
    float var = fmaxf(ss * (1.0f / 688.0f) - m * m, 0.f);
    f32x2 o = {m, 1.0f / sqrtf(var + 1e-5f)};
    *(LAS f32x2*)(c.L + T_ZF + c.tid * 8) = o;
  }
  bar();
  {
    const int qp = ((q & 1) << 1) | (q >> 1);
    f32x4 mr[2][2];
#pragma unroll
    for (int x = 0; x < 2; ++x) {
      mr[x][0] = *(LAS f32x4*)(c.L + T_ZF + (rg * 32 + x * 16 + 4 * q) * 8);
      mr[x][1] = *(LAS f32x4*)(c.L + T_ZF + (rg * 32 + x * 16 + 4 * q + 2) * 8);
    }
#pragma unroll
    for (int j = 0; j < 3; ++j) {
      u32x2 n[2];
#pragma unroll
      for (int x = 0; x < 2; ++x) {
        unsigned p0 = Z2[x][j].x, p1 = Z2[x][j].y;
        n[x].x = pk2((blo(p0) - mr[x][0][0]) * mr[x][0][1], (bhi(p0) - mr[x][0][2]) * mr[x][0][3]);
        n[x].y = pk2((blo(p1) - mr[x][1][0]) * mr[x][1][1], (bhi(p1) - mr[x][1][2]) * mr[x][1][3]);
      }
      u32x4 fr = pairswap(n[0], n[1]);
      if (lb3[j] < nb) {
        unsigned off = WS_Z2N + ((i * 4 + rg) * 44 + fb0 + lb3[j]) * 1024 + qp * 256 + t * 16;
        __builtin_amdgcn_raw_buffer_store_b128(fr, c.rs, off, 0, 16);
      }
    }
  }
  wait_vm<0>();
  bar();
  if (c.tid == 0) gstore64(ws + WS_FZ2 + (zc * 16 + i) * 128, tagT(c));
  STAMP(5);
  {
    f32x2 mr[2];
#pragma unroll
    for (int x = 0; x < 2; ++x) mr[x] = *(LAS f32x2*)(c.L + T_XS + (rg * 32 + x * 16 + t) * 8);
#pragma unroll
    for (int j = 0; j < 3; ++j) {
      f32x4 c4 = *(LAS f32x4*)(c.L + T_CS + 512 + (lb3[j] * 16 + 4 * q) * 4);
      u32x2 n[2];
#pragma unroll
      for (int x = 0; x < 2; ++x) {
        f32x4 v = acc[x][j];
        n[x].x = pk2(gelu_t(mr[x].y * (v[0] - mr[x].x * c4[0])), gelu_t(mr[x].y * (v[1] - mr[x].x * c4[1])));
        n[x].y = pk2(gelu_t(mr[x].y * (v[2] - mr[x].x * c4[2])), gelu_t(mr[x].y * (v[3] - mr[x].x * c4[3])));
      }
      u32x4 fr = pairswap(n[0], n[1]);
      if (lb3[j] < nb) {
        int fb = fb0 + lb3[j];
        unsigned off = WS_Z1 + ((i * 8 + rg * 2 + (q & 1)) * 22 + (fb >> 1)) * 1024 + (2 * (fb & 1) + (q >> 1)) * 256 + t * 16;
        __builtin_amdgcn_raw_buffer_store_b128(fr, c.rs, off, 0, 16);
      }
    }
  }
  wait_vm<0>();
  bar();
  if (c.tid == 0) gstore64(ws + WS_FZ1 + (i * 8 + zc) * 128, tagT(c));
  STAMP(6);
}

struct HAddr { unsigned va, vw; const char *bo, *bu, *bwo, *bwout; };
template <int T> DEV void h_issue_w(const Ctx& c, const HAddr& a) {
  const unsigned l = c.lb + ((32 + T) % 7) * G_SLOT + 8192 + c.w * 1024;
  if (T < 8) dma16(a.vw, a.bwo + (size_t)T * 64 * 1024, l);
  else if (T < 18) dma16(a.vw, a.bwout + (size_t)(T - 8) * 64 * 1024, l);
  else {
    int k = 8 * c.w + (c.lane >> 3);
    unsigned v = k > 47 ? a.vw - (k - 47) * 1024 : a.vw;
    dma16(v, a.bwout + (size_t)640 * 1024, l);
  }
}
template <int T> DEV void h_issue_a(const Ctx& c, const HAddr& a) {
  const unsigned l = c.lb + ((32 + T) % 7) * G_SLOT + c.w * 1024;
  if (T < 8) dma16c(a.va, a.bo + T * 2048, l);
  else dma16c(a.va, a.bu + (T - 8) * 2048, l);
}
template <int T> DEV void h_stage(const Ctx& c, f32x4 (&acc)[2], unsigned ao, unsigned w0, unsigned w1) {
  LAS char* L = c.L + ((32 + T) % 7) * G_SLOT;
#pragma unroll
  for (int kk = 0; kk < 2; ++kk) {
    u32x4 b = lds128(L + ao + kk * 4096);
    if (T == 18 && kk == 1) {
      if (c.lane >= 32) { u32x4 z = {0, 0, 0, 0}; b = z; }
    }
    u32x4 f0 = trfrag(L + 8192 + w0 + kk * 4096, 512), f1 = trfrag(L + 8192 + w1 + kk * 4096, 512);
    acc[0] = mfma32(f0, b, acc[0]);
    acc[1] = mfma32(f1, b, acc[1]);
  }
  asm volatile("" : "+v"(acc[0]), "+v"(acc[1]));
}

DEV void job_g(Ctx& c, int rt, int gc, const G3A& g3, bool epoch_wg) {
  char* ws = c.ws;
  const int lane = c.lane, w = c.w;
  const int t = lane & 15, q = lane >> 4;
  const int rg = w & 1, cg = (w >> 1) & 1, kh = w >> 2;
  const int fb0 = fb_start(gc), nb = gc < 3 ? 6 : 5;
  const u64 T = tagT(c);
  float bias = blo(*(const GAS u16*)(size_t)(c.bsp + ((rt * 4 + rg * 2 + kh) * 16 + t) * 2));
  if (w == 0) poll_flags(ws + WS_FZ2 + (gc * 16 + (lane & 15)) * 128, lane < 16, T);
  asm volatile("" : "+v"(bias));
  bar();
  STAMP(8);
  const char* zb = ws + WS_Z2N + (size_t)fb0 * 1024;
  const unsigned vz1 = ((w / 6) * 44 + (w % 6)) * 1024 + lane * 16;
  const int u2 = 8 + (w >> 1);
  const unsigned vz2 = (44 + (u2 - 6)) * 1024 + (w & 1) * 512 + (lane & 31) * 16;
  const unsigned lz1 = c.lb + G_Z + w * 1024, lz2 = c.lb + G_Z + u2 * 1024 + (w & 1) * 512;
#pragma unroll
  for (int s = 0; s < 6; ++s) {
    dma16c(vz1, zb + (size_t)s * 88 * 1024, lz1 + s * G_SLOT);
    dma16mc(vz2, zb + (size_t)s * 88 * 1024, lz2 + s * G_SLOT, 0xffffffffull);
  }
  HAddr h;
  {
    h.va = ((w & 3) * 16 + (w >> 2)) * 1024 + lane * 16;
    int k = 8 * w + (lane >> 3);
    int f = ((k >> 1) & 1) | (((k >> 3) & 1) << 1);
    h.vw = k * 1024 + (((lane & 7) ^ (f << 1)) << 4);
    h.bo = ws + WS_O + (size_t)rt * 4 * 16 * 1024;
    h.bu = ws + WS_U + (size_t)rt * 4 * 22 * 1024;
    h.bwo = c.wo + gc * 128;
    h.bwout = c.wout + gc * 128;
  }
  const unsigned vau = ((w & 3) * 22 + (w >> 2)) * 1024 + lane * 16;
  const unsigned bo_ = rg * 32 * 128 + t * 128 + (((4 * kh + q) ^ (t >> 1)) << 4);
  const unsigned ao_ = G_Z + (kh * 6 + 3 * cg) * 1024 + lane * 16;
  const f32x4 z4 = {0.f, 0.f, 0.f, 0.f};
  f32x4 acc[2][3];
#pragma unroll
  for (int j = 0; j < 3; ++j) { acc[0][j] = z4; acc[1][j] = z4; }
  const int tbk = rt * 4 + rg * 2 + kh;
  const unsigned flofs = lane < 8 ? WS_FO + ((rt >> 1) * 8 + lane) * 128 : WS_FZ1 + ((rt >> 1) * 8 + gc) * 128;
  unsigned z1ofs[3];
#pragma unroll
  for (int j = 0; j < 3; ++j) {
    int fb = fb0 + 3 * cg + j; fb = fb > 42 ? 42 : fb;
    z1ofs[j] = (tbk * 22 + (fb >> 1)) * 1024 + (2 * (fb & 1) + (q >> 1)) * 256 + t * 16 + (q & 1) * 8;
  }
  unsigned slot = 0, nslot = 6 * G_SLOT;
  auto stage = [&](int s) {
    bar();
    if (s < 26) {
      dma16(g3.vw, g3.bw + (s + 6) * 128, c.lb + nslot + w * 1024);
      dma16c(vz1, zb + (size_t)(s + 6) * 88 * 1024, lz1 + nslot);
      dma16mc(vz2, zb + (size_t)(s + 6) * 88 * 1024, lz2 + nslot, 0xffffffffull);
    } else {
      const unsigned l = c.lb + nslot + w * 1024;
      dma16c(h.va, h.bo + (s - 26) * 2048, l);
      dma16(h.vw, h.bwo + (size_t)(s - 26) * 64 * 1024, l + 8192);
    }
    LAS char* L = c.L + slot;
    u32x4 b0 = lds128(L + bo_), b1 = lds128(L + bo_ + 2048);
    u32x4 a0 = lds128(L + ao_), a1 = lds128(L + ao_ + 1024), a2 = lds128(L + ao_ + 2048);
    acc[0][0] = mfma32(a0, b0, acc[0][0]); acc[1][0] = mfma32(a0, b1, acc[1][0]);
    acc[0][1] = mfma32(a1, b0, acc[0][1]); acc[1][1] = mfma32(a1, b1, acc[1][1]);
    acc[0][2] = mfma32(a2, b0, acc[0][2]); acc[1][2] = mfma32(a2, b1, acc[1][2]);
    slot = slot == 6 * G_SLOT ? 0 : slot + G_SLOT;
    nslot = nslot == 6 * G_SLOT ? 0 : nslot + G_SLOT;
  };
#pragma unroll 1
  for (int s = 0; s < 5; ++s) {
    if (s == 0) wait_vm<10>(); else if (s == 1) wait_vm<11>(); else if (s == 2) wait_vm<12>(); else if (s == 3) wait_vm<13>(); else wait_vm<14>();
    stage(s);
  }
#pragma unroll 1
  for (int s = 5; s < 9; ++s) { wait_vm<15>(); stage(s); }
  u32x2 fl = aload64c(flofs, ws);
#pragma unroll 1
  for (int s = 9; s < 16; ++s) { wait_vm<15>(); stage(s); }
  asm volatile("; TIE %0" : "+v"(fl));
  if (!__all(fl.x == c.Tlo && fl.y == c.Thi)) poll_flags(ws + flofs, true, T);
  u32x2 z1v[3];
  z1v[0] = aload64c(z1ofs[0], ws + WS_Z1);
  z1v[1] = aload64c(z1ofs[1], ws + WS_Z1);
  z1v[2] = aload64c(z1ofs[2], ws + WS_Z1);
#pragma unroll 1
  for (int s = 16; s < 27; ++s) { wait_vm<15>(); stage(s); }
#pragma unroll 1
  for (int s = 27; s < 32; ++s) {
    if (s == 27) wait_vm<14>(); else if (s == 28) wait_vm<13>(); else if (s == 29) wait_vm<12>(); else if (s == 30) wait_vm<11>(); else wait_vm<10>();
    stage(s);
  }
  asm volatile("; TIE %0 %1 %2" : "+v"(z1v[0]), "+v"(z1v[1]), "+v"(z1v[2]));
  STAMP(9);
  bar();
  {
    const unsigned e01 = 3 * G_SLOT + w * 2048 + lane * 16, e2 = T_EX2 + w * 1024 + lane * 16;
    const unsigned p01 = 3 * G_SLOT + (w ^ 4) * 2048 + lane * 16, p2 = T_EX2 + (w ^ 4) * 1024 + lane * 16;
    f32x4 keep[3], send[3];
#pragma unroll
    for (int j = 0; j < 3; ++j) { keep[j] = kh ? acc[1][j] : acc[0][j]; send[j] = kh ? acc[0][j] : acc[1][j]; }
    *(LAS f32x4*)(c.L + e01) = send[0]; *(LAS f32x4*)(c.L + e01 + 1024) = send[1]; *(LAS f32x4*)(c.L + e2) = send[2];
    bar();
    keep[0] += *(LAS f32x4*)(c.L + p01); keep[1] += *(LAS f32x4*)(c.L + p01 + 1024); keep[2] += *(LAS f32x4*)(c.L + p2);
#pragma unroll
    for (int j = 0; j < 3; ++j) {
      unsigned g0 = pk2(keep[j][0] + bias, keep[j][1] + bias), g1 = pk2(keep[j][2] + bias, keep[j][3] + bias);
      unsigned a0 = z1v[j].x, a1 = z1v[j].y;
      u32x2 u;
      u.x = pk2(blo(a0) * blo(g0), bhi(a0) * bhi(g0));
      u.y = pk2(blo(a1) * blo(g1), bhi(a1) * bhi(g1));
      if (3 * cg + j < nb) __builtin_amdgcn_raw_buffer_store_b64(u, c.rs, WS_U + z1ofs[j], 0, 16);
    }
  }
  wait_vm<0>();
  bar();
  if (c.tid == 0) gstore64(ws + WS_FU + (rt * 8 + gc) * 128, T);
  STAMP(10);

  const int tb = w & 3, np = w >> 2;
  const unsigned ha = tb * 1024 + lane * 16;
  const int fw = ((t >> 3) & 1) | ((q & 1) << 1);
  const unsigned wl = (8 * q + (t >> 2)) * 128 + (t & 3) * 8;
  const unsigned hw0 = wl + (((2 * np) ^ fw) << 5), hw1 = wl + (((2 * np + 1) ^ fw) << 5);
  f32x4 sa[2] = {z4, z4};
  bar(); h_issue_a<6>(c, h); h_issue_w<6>(c, h); h_stage<0>(c, sa, ha, hw0, hw1);
  bar(); h_issue_a<7>(c, h); h_issue_w<7>(c, h); h_stage<1>(c, sa, ha, hw0, hw1);
  bar(); h_issue_w<8>(c, h);  h_stage<2>(c, sa, ha, hw0, hw1);
  bar(); h_issue_w<9>(c, h);  h_stage<3>(c, sa, ha, hw0, hw1);
  bar(); h_issue_w<10>(c, h); h_stage<4>(c, sa, ha, hw0, hw1);
  bar(); h_issue_w<11>(c, h); h_stage<5>(c, sa, ha, hw0, hw1);
  wait_vm<6>(); bar(); h_issue_w<12>(c, h); h_stage<6>(c, sa, ha, hw0, hw1);
  wait_vm<5>(); bar(); h_issue_w<13>(c, h); h_stage<7>(c, sa, ha, hw0, hw1);
  STAMP(11);
  h.va = vau;
  if (w == 0) poll_flags(ws + WS_FU + (rt * 8 + (lane & 7)) * 128, lane < 8, T);
  if (epoch_wg && w == 1) {
#pragma unroll
    for (int j = 0; j < 2; ++j) {
      poll_flags(ws + WS_FZ2 + (j * 64 + lane) * 128, true, T);
      poll_flags(ws + WS_FO + (j * 64 + lane) * 128, true, T);
    }
    if (lane == 0) gstore64(ws + WS_EPOCH, T);
  }
  bar();
  STAMP(12);
  h_issue_a<8>(c, h); h_issue_a<9>(c, h); h_issue_a<10>(c, h); h_issue_a<11>(c, h); h_issue_a<12>(c, h); h_issue_a<13>(c, h);
  const int orow = rt * 64 + tb * 16 + t;
  const unsigned oofs = orow * 1024 + gc * 128 + np * 64 + (q & 1) * 32 + (q >> 1) * 16;
  f32x4 ga[2] = {z4, z4};
  wait_vm<5>();  bar(); h_issue_a<14>(c, h); h_issue_w<14>(c, h); h_stage<8>(c, ga, ha, hw0, hw1);
  wait_vm<6>();  bar(); h_issue_a<15>(c, h); h_issue_w<15>(c, h); h_stage<9>(c, ga, ha, hw0, hw1);
  wait_vm<7>();  bar(); h_issue_a<16>(c, h); h_issue_w<16>(c, h); h_stage<10>(c, ga, ha, hw0, hw1);
  wait_vm<8>();  bar(); h_issue_a<17>(c, h); h_issue_w<17>(c, h); h_stage<11>(c, ga, ha, hw0, hw1);
  wait_vm<9>();  bar(); h_issue_a<18>(c, h); h_issue_w<18>(c, h); h_stage<12>(c, ga, ha, hw0, hw1);
  u32x4 xres;
  asm volatile("global_load_dwordx4 %0, %1, %2" : "=v"(xres) : "v"(oofs), "s"(uni(c.x)) : "memory");
  wait_vm<11>(); bar(); h_stage<13>(c, ga, ha, hw0, hw1);
  wait_vm<9>();  bar(); h_stage<14>(c, ga, ha, hw0, hw1);
  wait_vm<7>();  bar(); h_stage<15>(c, ga, ha, hw0, hw1);
  wait_vm<5>();  bar(); h_stage<16>(c, ga, ha, hw0, hw1);
  wait_vm<3>();  bar(); h_stage<17>(c, ga, ha, hw0, hw1);
  wait_vm<1>();  bar(); h_stage<18>(c, ga, ha, hw0, hw1);
  asm volatile("s_waitcnt vmcnt(0) ; TIE %0" : "+v"(xres) : : "memory");
  STAMP(13);
  {
    u32x2 S[2], G[2];
#pragma unroll
    for (int b = 0; b < 2; ++b) {
      S[b].x = pk2(sa[b][0], sa[b][1]); S[b].y = pk2(sa[b][2], sa[b][3]);
      G[b].x = pk2(ga[b][0], ga[b][1]); G[b].y = pk2(ga[b][2], ga[b][3]);
    }
    u32x4 s8 = pairswap(S[0], S[1]), g8 = pairswap(G[0], G[1]);
    u32x4 o;
#pragma unroll
    for (int j = 0; j < 4; ++j) {
      unsigned su = s8[j], gu = g8[j], xu = xres[j];
      float g0 = blo(gu), g1 = bhi(gu);
      float e0 = g0 * __builtin_amdgcn_rcpf(1.0f + __builtin_amdgcn_exp2f(g0 * -1.4426950408889634f));
      float e1 = g1 * __builtin_amdgcn_rcpf(1.0f + __builtin_amdgcn_exp2f(g1 * -1.4426950408889634f));
      o[j] = pk2(__builtin_fmaf(blo(su), e0, blo(xu)), __builtin_fmaf(bhi(su), e1, bhi(xu)));
    }
    *(GAS u32x4*)(size_t)(c.out + oofs) = o;
  }
  STAMP(14);
}

__global__ __launch_bounds__(512, 2) void fused_kernel(const char* x, const char* wqkv, const char* wo, const char* win, const char* wsp,
                                                       const char* bsp, const char* wout, char* out, char* ws) {
  extern __shared__ __attribute__((aligned(16))) char smem[];
  Ctx c;
  c.x = x; c.wqkv = wqkv; c.wo = wo; c.win = win; c.wsp = wsp; c.bsp = bsp; c.wout = wout; c.out = out; c.ws = ws;
  c.L = (LAS char*)smem;
  c.lb = (unsigned)(size_t)c.L;
  c.tid = threadIdx.x; c.lane = c.tid & 63; c.w = __builtin_amdgcn_readfirstlane(c.tid >> 6);
  c.rs = __builtin_amdgcn_make_buffer_rsrc((void*)ws, 0, WS_USED, 0x00020000);
  c.Tlo = 0; c.Thi = 0;
  c.bid = blockIdx.x;
  STAMP(0);
  const int bid = blockIdx.x, xcd = bid & 7, sl = bid >> 3;
  const int i = (xcd & 1) * 8 + (sl >> 2), rsel = sl & 3, col = 2 * (xcd >> 1) + (rsel & 1);
  const int r16 = (xcd >> 1) * 4 + rsel;
  const int rt = (xcd >> 1) * 8 + (sl >> 2), gc = (xcd & 1) * 4 + (sl & 3);
  const int s0row = i * 128 + r16 * 8 + c.w;
  const G3A g3 = g3_addr(c, rt);
  if (rsel < 2) job_a(c, i, col, s0row, g3);
  else job_z(c, i, col, s0row, g3);
  STAMP(7);
  job_g(c, rt, gc, g3, bid == 0);
}

extern "C" void kernel_launch(void* const* d_in, const int* in_sizes, int n_in, void* d_out, int out_size, void* d_ws, size_t ws_size,
                              hipStream_t stream) {
  (void)in_sizes; (void)n_in; (void)out_size; (void)ws_size;
  (void)hipFuncSetAttribute((const void*)fused_kernel, hipFuncAttributeMaxDynamicSharedMemorySize, LDS_BYTES);
  fused_kernel<<<256, 512, LDS_BYTES, stream>>>((const char*)d_in[0], (const char*)d_in[1], (const char*)d_in[2], (const char*)d_in[4],
                                                (const char*)d_in[5], (const char*)d_in[6], (const char*)d_in[7], (char*)d_out, (char*)d_ws);
}
```

```cpp
#include <hip/hip_runtime.h>
#define EXP 0

typedef unsigned short u16;
typedef unsigned long long u64;
typedef __bf16 bf16x8 __attribute__((ext_vector_type(8)));
typedef __bf16 bf16x2 __attribute__((ext_vector_type(2)));
typedef short s16x4 __attribute__((ext_vector_type(4)));
typedef float f32x4 __attribute__((ext_vector_type(4)));
typedef float f32x2 __attribute__((ext_vector_type(2)));
typedef unsigned u32x4 __attribute__((ext_vector_type(4)));
typedef unsigned u32x2 __attribute__((ext_vector_type(2)));
#define LAS __attribute__((address_space(3)))
#define GAS __attribute__((address_space(1)))
#define DEV __device__ __forceinline__

constexpr unsigned WS_EPOCH = 0;
constexpr unsigned WS_FZ2 = 4096;
constexpr unsigned WS_FZ1 = WS_FZ2 + 128 * 128;
constexpr unsigned WS_FO  = WS_FZ1 + 128 * 128;
constexpr unsigned WS_FU  = WS_FO + 128 * 128;
constexpr unsigned WS_XS  = 0x20000;
constexpr unsigned WS_ZS  = 0x30000;
constexpr unsigned WS_Z2N = 0x100000;
constexpr unsigned WS_Z1  = 0x400000;
constexpr unsigned WS_U   = 0x700000;
constexpr unsigned WS_O   = 0xA00000;
constexpr unsigned WS_DBG = 0xD00000;
constexpr unsigned WS_USED = 0xE00000;

constexpr unsigned LDS_BYTES = 160 * 1024;
constexpr unsigned A_SLOT = 43008, A_W = 18432;
constexpr unsigned Z_SLOT = 28672, Z_W = 16384;
constexpr unsigned G_SLOT = 20480, G_Z = 8192;
constexpr unsigned T_BASE = 143360;
constexpr unsigned T_EX2 = T_BASE;
constexpr unsigned T_XS = T_BASE + 8192;
constexpr unsigned T_CS = T_XS + 1280;
constexpr unsigned T_ZP = T_CS + 1024;
constexpr unsigned T_ZQ = T_ZP + 2048;
constexpr unsigned T_ZF = T_ZQ + 4096;
static_assert(T_ZF + 1024 <= LDS_BYTES, "lds");

constexpr unsigned SPIN_CAP = 200000;

#define STAMP1(k) do {} while (0)
#define STAMPC(k) do {} while (0)
#define STAMP(k) do {} while (0)

DEV const char* uni(const char* p) {
  u64 v = (u64)(size_t)p;
  unsigned lo = __builtin_amdgcn_readfirstlane((unsigned)v), hi = __builtin_amdgcn_readfirstlane((unsigned)(v >> 32));
  return (const char*)(size_t)(((u64)hi << 32) | lo);
}
DEV unsigned uni(unsigned v) { return __builtin_amdgcn_readfirstlane(v); }
DEV void dma16(unsigned voff, const char* sbase, unsigned lds) {
  asm volatile("s_mov_b32 m0, %2\n\ts_nop 0\n\tglobal_load_lds_dwordx4 %0, %1" ::"v"(voff), "s"(uni(sbase)), "s"(uni(lds)) : "memory");
}
DEV void dma16c(unsigned voff, const char* sbase, unsigned lds) {
  asm volatile("s_mov_b32 m0, %2\n\ts_nop 0\n\tglobal_load_lds_dwordx4 %0, %1 sc1" ::"v"(voff), "s"(uni(sbase)), "s"(uni(lds)) : "memory");
}
DEV void dma16m(unsigned voff, const char* sbase, unsigned lds, u64 mask) {
  asm volatile("s_mov_b32 m0, %2\n\ts_mov_b64 exec, %3\n\tglobal_load_lds_dwordx4 %0, %1\n\ts_mov_b64 exec, -1"
               ::"v"(voff), "s"(uni(sbase)), "s"(uni(lds)), "s"(mask) : "memory");
}
DEV void dma16mc(unsigned voff, const char* sbase, unsigned lds, u64 mask) {
  asm volatile("s_mov_b32 m0, %2\n\ts_mov_b64 exec, %3\n\tglobal_load_lds_dwordx4 %0, %1 sc1\n\ts_mov_b64 exec, -1"
               ::"v"(voff), "s"(uni(sbase)), "s"(uni(lds)), "s"(mask) : "memory");
}
template <int N> DEV void wait_vm() { asm volatile("s_waitcnt vmcnt(%0)" ::"n"(N) : "memory"); }
DEV void sb() { __builtin_amdgcn_sched_barrier(0); }
DEV void bar() { asm volatile("s_waitcnt lgkmcnt(0)\n\ts_barrier" ::: "memory"); }

template <int V = 0> DEV f32x4 mfmaX(u32x4 a, u32x4 b, f32x4 c) {
  return __builtin_amdgcn_mfma_f32_16x16x32_bf16(__builtin_bit_cast(bf16x8, a), __builtin_bit_cast(bf16x8, b), c, 0, 0, 0);
}
DEV f32x4 mfma32(u32x4 a, u32x4 b, f32x4 c) {
  return __builtin_amdgcn_mfma_f32_16x16x32_bf16(__builtin_bit_cast(bf16x8, a), __builtin_bit_cast(bf16x8, b), c, 0, 0, 0);
}
DEV f32x4 mfma16(u32x2 a, u32x2 b, f32x4 c) {
  return __builtin_amdgcn_mfma_f32_16x16x16bf16_1k(__builtin_bit_cast(s16x4, a), __builtin_bit_cast(s16x4, b), c, 0, 0, 0);
}
DEV u32x4 lds128(LAS char* p) { return *(LAS u32x4*)p; }
DEV u32x2 lds64(LAS char* p) { return *(LAS u32x2*)p; }
DEV u32x4 trfrag(LAS char* p, int off4) {
  s16x4 a = __builtin_amdgcn_ds_read_tr16_b64_v4i16((LAS s16x4*)p);
  s16x4 b = __builtin_amdgcn_ds_read_tr16_b64_v4i16((LAS s16x4*)(p + off4));
  u32x2 ua = __builtin_bit_cast(u32x2, a), ub = __builtin_bit_cast(u32x2, b);
  u32x4 r = {ua.x, ua.y, ub.x, ub.y};
  return r;
}
DEV unsigned pk2(float a, float b) {
  f32x2 v = {a, b};
  bf16x2 h = __builtin_convertvector(v, bf16x2);
  return __builtin_bit_cast(unsigned, h);
}
DEV float blo(unsigned u) { return __uint_as_float(u << 16); }
DEV float bhi(unsigned u) { return __uint_as_float(u & 0xffff0000u); }
template <int CTRL> DEV float dpp(float v) {
  return __uint_as_float(__builtin_amdgcn_update_dpp(0u, __float_as_uint(v), CTRL, 0xf, 0xf, true));
}
DEV float sum16(float v) {
  v += dpp<0x128>(v); v += dpp<0x124>(v); v += dpp<0x122>(v); v += dpp<0x121>(v);
  return v;
}
DEV float sumq(float v) {
  unsigned u = __float_as_uint(v);
  auto a = __builtin_amdgcn_permlane16_swap(u, u, false, false);
  v = __uint_as_float(a[0]) + __uint_as_float(a[1]);
  u = __float_as_uint(v);
  auto b = __builtin_amdgcn_permlane32_swap(u, u, false, false);
  return __uint_as_float(b[0]) + __uint_as_float(b[1]);
}
DEV float maxq(float v) {
  unsigned u = __float_as_uint(v);
  auto a = __builtin_amdgcn_permlane16_swap(u, u, false, false);
  v = fmaxf(__uint_as_float(a[0]), __uint_as_float(a[1]));
  u = __float_as_uint(v);
  auto b = __builtin_amdgcn_permlane32_swap(u, u, false, false);
  return fmaxf(__uint_as_float(b[0]), __uint_as_float(b[1]));
}
DEV u32x4 pairswap(u32x2 b0, u32x2 b1) {
  auto s0 = __builtin_amdgcn_permlane16_swap(b0.x, b1.x, false, false);
  auto s1 = __builtin_amdgcn_permlane16_swap(b0.y, b1.y, false, false);
  u32x4 r = {s0[0], s1[0], s0[1], s1[1]};
  return r;
}
DEV float gelu_t(float v) {
  float e = __builtin_amdgcn_exp2f(v * __builtin_fmaf(v * v, -0.10294324f, -2.3022082f));
  return v * __builtin_amdgcn_rcpf(1.0f + e);
}
DEV u64 gload64(const char* p) { return __hip_atomic_load((GAS u64*)(size_t)p, __ATOMIC_RELAXED, __HIP_MEMORY_SCOPE_AGENT); }
DEV void gstore64(const char* p, u64 v) { __hip_atomic_store((GAS u64*)(size_t)p, v, __ATOMIC_RELAXED, __HIP_MEMORY_SCOPE_AGENT); }
DEV void poll_flags(const char* p, bool active, u64 T) {
  for (unsigned it = 0; it < SPIN_CAP; ++it) {
    bool ok = !active || gload64(p) == T;
    if (__all(ok)) break;
    __builtin_amdgcn_s_sleep(1);
  }
}
DEV u32x4 settle_pair(u32x4 g, __amdgpu_buffer_rsrc_t rs, unsigned off, bool active, unsigned T32) {
  for (unsigned it = 0; it < SPIN_CAP; ++it) {
    bool ok = !active || (g.y == T32 && g.w == T32);
    if (__all(ok)) break;
    __builtin_amdgcn_s_sleep(1);
    g = __builtin_amdgcn_raw_buffer_load_b128(rs, off, 0, 16);
  }
  return g;
}
DEV u32x4 aload128c(unsigned voff, const char* sbase) {
  u32x4 r;
  asm volatile("global_load_dwordx4 %0, %1, %2 sc1" : "=v"(r) : "v"(voff), "s"(uni(sbase)) : "memory");
  return r;
}
DEV u32x2 aload64c(unsigned voff, const char* sbase) {
  u32x2 r;
  asm volatile("global_load_dwordx2 %0, %1, %2 sc1" : "=v"(r) : "v"(voff), "s"(uni(sbase)) : "memory");
  return r;
}

struct Ctx {
  const char *x, *wqkv, *wo, *win, *wsp, *bsp, *wout;
  char *out, *ws;
  LAS char* L;
  unsigned lb;
  int tid, lane, w, bid;
  unsigned Tlo, Thi;
  __amdgpu_buffer_rsrc_t rs;
};
DEV u64 tagT(const Ctx& c) { return ((u64)c.Thi << 32) | c.Tlo; }

DEV void s0_issue(const Ctx& c, u32x4& xr, u32x2& ev, int row) {
  ev = aload64c(0, c.ws + WS_EPOCH);
  asm volatile("global_load_dwordx4 %0, %1, %2" : "=v"(xr) : "v"(c.lane * 16), "s"(uni(c.x + (size_t)row * 1024)) : "memory");
}
template <int P> DEV void s0_finish(Ctx& c, u32x4 xr, u32x2 ev, int row) {
  asm volatile("s_waitcnt vmcnt(%2) ; TIE %0 %1" : "+v"(xr), "+v"(ev) : "n"(P) : "memory");
  u64 e = (((u64)(unsigned)__builtin_amdgcn_readfirstlane(ev.y)) << 32) | (unsigned)__builtin_amdgcn_readfirstlane(ev.x);
  e += 1;
  c.Tlo = (unsigned)e; c.Thi = (unsigned)(e >> 32);
  unsigned x0 = xr.x, x1 = xr.y, x2 = xr.z, x3 = xr.w;
  float v[8] = {blo(x0), bhi(x0), blo(x1), bhi(x1), blo(x2), bhi(x2), blo(x3), bhi(x3)};
  float s = ((v[0] + v[1]) + (v[2] + v[3])) + ((v[4] + v[5]) + (v[6] + v[7]));
  s = sumq(sum16(s));
  float m = s * (1.0f / 512.0f), q = 0.f;
#pragma unroll
  for (int j = 0; j < 8; ++j) { float d = v[j] - m; q = __builtin_fmaf(d, d, q); }
  q = sumq(sum16(q));
  float r = 1.0f / sqrtf(q * (1.0f / 512.0f) + 1e-5f);
  if (c.lane < 2)
    gstore64(c.ws + WS_XS + row * 16 + c.lane * 8, ((u64)c.Tlo << 32) | __float_as_uint(c.lane ? r : m));
}

struct G3A { unsigned vw; const char* bw; };
DEV G3A g3_addr(const Ctx& c, int rt) {
  int r = 8 * c.w + (c.lane >> 3);
  G3A a;
  a.vw = r * 4096 + (((c.lane & 7) ^ ((r >> 1) & 7)) << 4);
  a.bw = c.wsp + (size_t)rt * 64 * 4096;
  return a;
}
DEV void g3_prefetch_w(const Ctx& c, const G3A& a) {
#pragma unroll
  for (int s = 0; s < 7; ++s) dma16(a.vw, a.bw + s * 128, c.lb + s * G_SLOT + c.w * 1024);
}

struct AAddr { unsigned vx0, vx1, vh, vw; const char *bx, *bh, *bw; u64 hmask; };
template <int H, int P> DEV void a_dma(const Ctx& c, const AAddr& a, int s, unsigned slot) {
  const unsigned l = c.lb + slot;
  if (H == 0) {
    if (P == 0) dma16(a.vx0, a.bx + s * 128, l + c.w * 2048);
    if (P == 1) dma16(a.vx1, a.bx + s * 128, l + c.w * 2048 + 1024);
    if (P == 2) dma16m(a.vh, a.bh + s * 128, l + 128 * 128 + c.w * 256, a.hmask);
  } else {
    dma16(a.vw, a.bw + (size_t)s * 64 * 3072 + P * 1024, l + A_W + P * 8192 + c.w * 1024);
  }
}
template <int H> DEV void a_half(const Ctx& c, const AAddr& a, int s, unsigned slot) {
  a_dma<H, 0>(c, a, s, slot); a_dma<H, 1>(c, a, s, slot); a_dma<H, 2>(c, a, s, slot);
}

struct AAcc { f32x4 q[2][2], k[2][2], v[2][2], h, qc; };
struct AF { u32x4 x0, x1, hx, qa, qb, ka, kb, va, vb; };
struct AOfs { unsigned xo, xh, wA, wB; };

DEV void a_read(const Ctx& c, AF& F, unsigned slot, int kk, const AOfs& o) {
  LAS char* L = c.L + slot;
  LAS char* pa = L + A_W + o.wA + kk * 4096;
  LAS char* pb = L + A_W + o.wB + kk * 4096;
  F.x0 = lds128(L + (o.xo ^ (kk * 64)));
  F.qa = trfrag(pa, 512);
  F.x1 = lds128(L + (o.xo ^ (kk * 64)) + 2048);
  F.qb = trfrag(pb, 512);
  F.ka = trfrag(pa + 8192, 512); F.kb = trfrag(pb + 8192, 512);
  F.va = trfrag(pa + 16384, 512); F.vb = trfrag(pb + 16384, 512);
  F.hx = lds128(L + (o.xh ^ (kk * 64)));
  sb();
}
template <bool HV, int H> DEV void a_mma(const Ctx& c, AAcc& A, const AF& F, const AAddr& a, int sn, unsigned slot) {
  A.q[0][0] = mfmaX(F.qa, F.x0, A.q[0][0]); A.q[1][0] = mfmaX(F.qa, F.x1, A.q[1][0]);
  A.q[0][1] = mfmaX(F.qb, F.x0, A.q[0][1]); A.q[1][1] = mfmaX(F.qb, F.x1, A.q[1][1]);
  sb();
  if (H >= 0) a_dma<(H < 0 ? 0 : H), 0>(c, a, sn, slot);
  sb();
  A.k[0][0] = mfmaX(F.ka, F.x0, A.k[0][0]); A.k[1][0] = mfmaX(F.ka, F.x1, A.k[1][0]);
  A.k[0][1] = mfmaX(F.kb, F.x0, A.k[0][1]); A.k[1][1] = mfmaX(F.kb, F.x1, A.k[1][1]);
  A.v[0][0] = mfmaX(F.x0, F.va, A.v[0][0]);
  sb();
  if (H >= 0) a_dma<(H < 0 ? 0 : H), 1>(c, a, sn, slot);
  sb();
  A.v[1][0] = mfmaX(F.x1, F.va, A.v[1][0]);
  A.v[0][1] = mfmaX(F.x0, F.vb, A.v[0][1]); A.v[1][1] = mfmaX(F.x1, F.vb, A.v[1][1]);
  if (HV) {
    A.h = mfmaX(F.hx, F.va, A.h);
  } else {
    A.h = mfmaX(F.ka, F.hx, A.h);
    A.qc = mfmaX(F.qa, F.hx, A.qc);
  }
  sb();
  if (H >= 0) a_dma<(H < 0 ? 0 : H), 2>(c, a, sn, slot);
  sb();
}

template <bool HV> DEV void a_loop(const Ctx& c, AAcc& A, const AAddr& a, const AOfs& o, u32x4& g, unsigned gofs) {
  AF F0, F1;
  wait_vm<9>();
  bar();
  a_read(c, F0, 0, 0, o);
  unsigned s0 = 0, s1 = A_SLOT, s2 = 2 * A_SLOT;
#pragma unroll 1
  for (int s = 0; s < 5; ++s) {
    a_read(c, F1, s0, 1, o);
    a_mma<HV, 1>(c, A, F0, a, s + 2, s2);
    wait_vm<6>();
    bar();
    STAMP1(16 + s);
    a_read(c, F0, s1, 0, o);
    a_mma<HV, 0>(c, A, F1, a, s + 3, s0);
    unsigned t = s0; s0 = s1; s1 = s2; s2 = t;
  }
  a_read(c, F1, 2 * A_SLOT, 1, o);
  a_mma<HV, 1>(c, A, F0, a, 7, A_SLOT);
  wait_vm<6>();
  bar();
  g = aload128c(gofs, c.ws + WS_XS);
  a_read(c, F0, 0, 0, o);
  a_mma<HV, -1>(c, A, F1, a, 0, 0);
  a_read(c, F1, 0, 1, o);
  a_mma<HV, -1>(c, A, F0, a, 0, 0);
  asm volatile("s_waitcnt vmcnt(0) ; TIE %0" : "+v"(g) : : "memory");
  bar();
  a_read(c, F0, A_SLOT, 0, o);
  a_mma<HV, -1>(c, A, F1, a, 0, 0);
  a_read(c, F1, A_SLOT, 1, o);
  a_mma<HV, -1>(c, A, F0, a, 0, 0);
  a_mma<HV, -1>(c, A, F1, a, 0, 0);
}

DEV void job_a(Ctx& c, int i, int hc, int s0row, const G3A& g3) {
  u32x4 xr; u32x2 ev;
  s0_issue(c, xr, ev, s0row);
  char* ws = c.ws;
  const int lane = c.lane, w = c.w, rg = w & 3, cg = w >> 2;
  const int t = lane & 15, q = lane >> 4;
  AAddr a;
  {
    int r0 = 16 * w + (lane >> 3);
    a.vx0 = r0 * 1024 + (((lane & 7) ^ ((r0 >> 1) & 7)) << 4);
    int r1 = r0 + 8;
    a.vx1 = r1 * 1024 + (((lane & 7) ^ ((r1 >> 1) & 7)) << 4);
    a.bx = c.x + (size_t)i * 128 * 1024;
    int rh = 2 * w + ((lane >> 3) & 1);
    a.vh = rh * 1024 + (((lane & 7) ^ w) << 4);
    a.bh = c.x + (size_t)(i > 0 ? i * 128 - 16 : 0) * 1024;
    a.hmask = w == 0 ? 0xff00ull : 0xffffull;
    int k = 8 * w + (lane >> 3);
    int f = ((k >> 1) & 1) | (((k >> 3) & 1) << 1);
    a.vw = k * 3072 + (((lane & 7) ^ (f << 1)) << 4);
    a.bw = c.wqkv + hc * 128;
  }
  if (c.tid < 24) {
    u32x4 ones = {0x3f803f80u, 0x3f803f80u, 0x3f803f80u, 0x3f803f80u};
    *(LAS u32x4*)(c.L + (c.tid >> 3) * A_SLOT + 128 * 128 + (c.tid & 7) * 16) = ones;
  }
  a_half<0>(c, a, 0, 0); a_half<1>(c, a, 0, 0);
  a_half<0>(c, a, 1, A_SLOT); a_half<1>(c, a, 1, A_SLOT);
  s0_finish<12>(c, xr, ev, s0row);
  STAMP(15);
  a_half<0>(c, a, 2, 2 * A_SLOT);

  const unsigned xlane = t * 128 + ((q ^ (t >> 1)) << 4);
  const unsigned xo = rg * 32 * 128 + xlane, xh = 128 * 128 + xlane;
  const int f = ((t >> 3) & 1) | ((q & 1) << 1);
  const int hA = 2 * cg + (rg & 1), hB = hA ^ 1;
  const unsigned wl = (8 * q + (t >> 2)) * 128 + (t & 3) * 8;
  const unsigned wA = wl + ((hA ^ f) << 5), wB = wl + ((hB ^ f) << 5);
  const int gj = c.tid < 144 ? c.tid : 143;
  const int gtok = gj < 128 ? i * 128 + gj : (i > 0 ? i * 128 - 16 + (gj - 128) : gj - 128);
  const unsigned gofs = gtok * 16;

  AAcc A;
  const f32x4 z4 = {0.f, 0.f, 0.f, 0.f};
#pragma unroll
  for (int x = 0; x < 2; ++x)
#pragma unroll
    for (int y = 0; y < 2; ++y) { A.q[x][y] = z4; A.k[x][y] = z4; A.v[x][y] = z4; }
  A.h = z4; A.qc = z4;
  u32x4 g = {0, 0, 0, 0};
  const bool hv = rg >= 2;
  const AOfs ao = {xo, xh, wA, wB};
  if (hv) a_loop<true>(c, A, a, ao, g, gofs);
  else a_loop<false>(c, A, a, ao, g, gofs);
  STAMP(2);
  g = settle_pair(g, c.rs, WS_XS + gofs, true, c.Tlo);
  if (c.tid < 144) { u32x2 mr = {g.x, g.z}; *(LAS u32x2*)(c.L + T_XS + c.tid * 8) = mr; }
  if (!hv) {
    if (t == 0) {
      *(LAS f32x4*)(c.L + T_CS + (64 + hA * 16 + 4 * q) * 4) = A.h;
      *(LAS f32x4*)(c.L + T_CS + (hA * 16 + 4 * q) * 4) = A.qc;
    }
  } else if (lane < 16) {
    *(LAS float*)(c.L + T_CS + (128 + hA * 16 + lane) * 4) = A.h[0];
  }
  bar();
  g3_prefetch_w(c, g3);

  u32x2 Q[2][2], K[2][2], V[2][2], H;
  {
    f32x4 csq[2], csk[2]; float csv[2];
#pragma unroll
    for (int y = 0; y < 2; ++y) {
      int hh = y ? hB : hA;
      csq[y] = *(LAS f32x4*)(c.L + T_CS + (hh * 16 + 4 * q) * 4);
      csk[y] = *(LAS f32x4*)(c.L + T_CS + (64 + hh * 16 + 4 * q) * 4);
      csv[y] = *(LAS float*)(c.L + T_CS + (128 + hh * 16 + t) * 4);
    }
#pragma unroll
    for (int x = 0; x < 2; ++x) {
      f32x2 mr = *(LAS f32x2*)(c.L + T_XS + (rg * 32 + x * 16 + t) * 8);
      f32x4 m01 = *(LAS f32x4*)(c.L + T_XS + (rg * 32 + x * 16 + 4 * q) * 8);
      f32x4 m23 = *(LAS f32x4*)(c.L + T_XS + (rg * 32 + x * 16 + 4 * q + 2) * 8);
      const float rq = mr.y * 0.25f;
#pragma unroll
      for (int y = 0; y < 2; ++y) {
        f32x4 aq = A.q[x][y], ak = A.k[x][y], av = A.v[x][y];
        Q[x][y].x = pk2(rq * (aq[0] - mr.x * csq[y][0]), rq * (aq[1] - mr.x * csq[y][1]));
        Q[x][y].y = pk2(rq * (aq[2] - mr.x * csq[y][2]), rq * (aq[3] - mr.x * csq[y][3]));
        K[x][y].x = pk2(mr.y * (ak[0] - mr.x * csk[y][0]), mr.y * (ak[1] - mr.x * csk[y][1]));
        K[x][y].y = pk2(mr.y * (ak[2] - mr.x * csk[y][2]), mr.y * (ak[3] - mr.x * csk[y][3]));
        V[x][y].x = pk2(m01[1] * (av[0] - m01[0] * csv[y]), m01[3] * (av[1] - m01[2] * csv[y]));
        V[x][y].y = pk2(m23[1] * (av[2] - m23[0] * csv[y]), m23[3] * (av[3] - m23[2] * csv[y]));
      }
    }
    if (!hv) {
      f32x2 mr = *(LAS f32x2*)(c.L + T_XS + (128 + t) * 8);
      H.x = pk2(mr.y * (A.h[0] - mr.x * csk[0][0]), mr.y * (A.h[1] - mr.x * csk[0][1]));
      H.y = pk2(mr.y * (A.h[2] - mr.x * csk[0][2]), mr.y * (A.h[3] - mr.x * csk[0][3]));
    } else {
      f32x4 m01 = *(LAS f32x4*)(c.L + T_XS + (128 + 4 * q) * 8);
      f32x4 m23 = *(LAS f32x4*)(c.L + T_XS + (128 + 4 * q + 2) * 8);
      H.x = pk2(m01[1] * (A.h[0] - m01[0] * csv[0]), m01[3] * (A.h[1] - m01[2] * csv[0]));
      H.y = pk2(m23[1] * (A.h[2] - m23[0] * csv[0]), m23[3] * (A.h[3] - m23[2] * csv[0]));
    }
  }
  const unsigned KX = G_Z, VX = G_SLOT + G_Z;
  {
    *(LAS u32x2*)(c.L + (hv ? VX : KX) + hA * 512 + lane * 8) = H;
    if (rg < 3) {
      *(LAS u32x2*)(c.L + KX + ((rg + 1) * 4 + hA) * 512 + lane * 8) = K[1][0];
      *(LAS u32x2*)(c.L + KX + ((rg + 1) * 4 + hB) * 512 + lane * 8) = K[1][1];
      *(LAS u32x2*)(c.L + VX + ((rg + 1) * 4 + hA) * 512 + lane * 8) = V[1][0];
      *(LAS u32x2*)(c.L + VX + ((rg + 1) * 4 + hB) * 512 + lane * 8) = V[1][1];
    }
  }
  bar();
  u32x2 Kp[2], Vp[2];
  Kp[0] = lds64(c.L + KX + (rg * 4 + hA) * 512 + lane * 8);
  Kp[1] = lds64(c.L + KX + (rg * 4 + hB) * 512 + lane * 8);
  Vp[0] = lds64(c.L + VX + (rg * 4 + hA) * 512 + lane * 8);
  Vp[1] = lds64(c.L + VX + (rg * 4 + hB) * 512 + lane * 8);

  const float NEG = -1e30f, LOG2E = 1.4426950408889634f;
  float bc[4], bp[4];
#pragma unroll
  for (int j = 0; j < 4; ++j) {
    int d = t - (4 * q + j);
    bc[j] = (d >= 0 && !(d & 1)) ? 0.f : NEG;
    int dp = d + 16;
    bp[j] = (dp <= 14 && !(dp & 1)) ? 0.f : NEG;
  }
  const bool first = (i == 0 && rg == 0);
#pragma unroll
  for (int x = 1; x >= 0; --x) {
    u32x2 O[2];
#pragma unroll
    for (int y = 0; y < 2; ++y) {
      u32x2 kp = x ? K[0][y] : Kp[y], vp = x ? V[0][y] : Vp[y];
      f32x4 sp = mfma16(kp, Q[x][y], z4), sc = mfma16(K[x][y], Q[x][y], z4);
      float e[8];
#pragma unroll
      for (int j = 0; j < 4; ++j) {
        e[j] = __builtin_fmaf(sp[j], LOG2E, (x == 0 && first) ? NEG : bp[j]);
        e[4 + j] = __builtin_fmaf(sc[j], LOG2E, bc[j]);
      }
      float mx = fmaxf(fmaxf(fmaxf(e[0], e[1]), fmaxf(e[2], e[3])), fmaxf(fmaxf(e[4], e[5]), fmaxf(e[6], e[7])));
      mx = maxq(mx);
      float sum = 0.f;
#pragma unroll
      for (int j = 0; j < 8; ++j) { e[j] = __builtin_amdgcn_exp2f(e[j] - mx); sum += e[j]; }
      float inv = __builtin_amdgcn_rcpf(sumq(sum));
      u32x4 P = {pk2(e[0] * inv, e[1] * inv), pk2(e[2] * inv, e[3] * inv), pk2(e[4] * inv, e[5] * inv), pk2(e[6] * inv, e[7] * inv)};
      u32x4 VV = {vp.x, vp.y, V[x][y].x, V[x][y].y};
      f32x4 o = mfma32(VV, P, z4);
      O[y].x = pk2(o[0], o[1]); O[y].y = pk2(o[2], o[3]);
    }
    u32x4 fr = pairswap(O[0], O[1]);
    int qp = (((q & 1) << 1) | (q >> 1)) ^ ((rg & 1) << 1);
    unsigned off = WS_O + ((i * 8 + rg * 2 + x) * 16 + 2 * hc + cg) * 1024 + qp * 256 + t * 16;
    __builtin_amdgcn_raw_buffer_store_b128(fr, c.rs, off, 0, 16);
  }
  wait_vm<0>();
  bar();
  if (c.tid == 0) gstore64(ws + WS_FO + (i * 8 + hc) * 128, tagT(c));
  STAMP(3);
}

struct ZAddr { unsigned vx0, vx1, vw; const char *bx, *bw; };
template <int S, int H, int P> DEV void z_dma(const Ctx& c, const ZAddr& a) {
  if (S >= 16 || S < 0) return;
  const unsigned l = c.lb + (S % 5) * Z_SLOT;
  const char* bw = a.bw + (size_t)(S & 7) * 64 * 2752 + (S < 8 ? 1376 : 0);
  if (S < 8) {
    if (H == 0) dma16(P ? a.vx1 : a.vx0, a.bx + S * 128, l + c.w * 2048 + P * 1024);
    else dma16m(a.vw, bw + P * 4 * 2752, l + Z_W + c.w * 1536 + P * 768, 0xffffffffffffull);
  } else if (P == 0) {
    dma16m(a.vw, bw + H * 4 * 2752, l + Z_W + c.w * 1536 + H * 768, 0xffffffffffffull);
  }
}
template <int S, int H> DEV void z_half(const Ctx& c, const ZAddr& a) { z_dma<S, H, 0>(c, a); z_dma<S, H, 1>(c, a); }
constexpr int z_n(int s) { return s >= 16 ? 0 : (s < 8 ? 4 : 2); }
constexpr int z_wait(int s) { return z_n(s + 1) + z_n(s + 2) + z_n(s + 3); }

struct ZW { u32x4 f[3]; };
template <int S, int KK> DEV void z_read_w(const Ctx& c, ZW& F, const unsigned (&wb)[3]) {
  LAS char* L = c.L + (S % 5) * Z_SLOT;
#pragma unroll
  for (int j = 0; j < 3; ++j) F.f[j] = trfrag(L + Z_W + wb[j] + KK * 6144, 768);
}
template <int S, int KK> DEV void z_read_x(const Ctx& c, u32x4 (&xf)[2][16], unsigned xo) {
  LAS char* L = c.L + (S % 5) * Z_SLOT;
  xf[0][2 * S + KK] = lds128(L + (xo ^ (KK * 64)));
  xf[1][2 * S + KK] = lds128(L + (xo ^ (KK * 64)) + 2048);
}
template <bool SWAP, int J, int SN, int H> DEV void z_mma(const Ctx& c, const ZAddr& a, const u32x4 (&xf)[2][16], const ZW& F,
                                                          f32x4 (&acc)[2][3], f32x4& cs) {
  const u32x4 ones = {0x3f803f80u, 0x3f803f80u, 0x3f803f80u, 0x3f803f80u};
  sb();
  if (!SWAP) {
    acc[0][0] = mfmaX(xf[0][J], F.f[0], acc[0][0]); acc[1][0] = mfmaX(xf[1][J], F.f[0], acc[1][0]);
    acc[0][1] = mfmaX(xf[0][J], F.f[1], acc[0][1]);
  } else {
    acc[0][0] = mfmaX(F.f[0], xf[0][J], acc[0][0]); acc[1][0] = mfmaX(F.f[0], xf[1][J], acc[1][0]);
    acc[0][1] = mfmaX(F.f[1], xf[0][J], acc[0][1]);
  }
  sb();
  z_dma<SN, H, 0>(c, a);
  sb();
  if (!SWAP) {
    acc[1][1] = mfmaX(xf[1][J], F.f[1], acc[1][1]);
    acc[0][2] = mfmaX(xf[0][J], F.f[2], acc[0][2]); acc[1][2] = mfmaX(xf[1][J], F.f[2], acc[1][2]);
    cs = mfmaX(ones, F.f[0], cs);
  } else {
    acc[1][1] = mfmaX(F.f[1], xf[1][J], acc[1][1]);
    acc[0][2] = mfmaX(F.f[2], xf[0][J], acc[0][2]); acc[1][2] = mfmaX(F.f[2], xf[1][J], acc[1][2]);
    cs = mfmaX(F.f[0], ones, cs);
  }
  sb();
  z_dma<SN, H, 1>(c, a);
  sb();
}
template <int S> DEV void z_stage_a(const Ctx& c, const ZAddr& a, u32x4 (&xf)[2][16], ZW& F0, ZW& F1, f32x4 (&acc)[2][3], f32x4& cs,
                                    unsigned xo, const unsigned (&wb)[3], u32x4& g, unsigned gofs) {
  z_read_x<S, 1>(c, xf, xo); z_read_w<S, 1>(c, F1, wb);
  z_mma<false, 2 * S, (S == 0 ? -1 : S + 4), 1>(c, a, xf, F0, acc, cs);
  if (S == 7) asm volatile("s_waitcnt vmcnt(4) ; TIE %0" : "+v"(g) : : "memory");
  else wait_vm<z_wait(S + 1)>();
  bar();
  STAMP1(16 + S);
  if (S == 5) g = aload128c(gofs, c.ws + WS_XS);
  if (S < 7) { z_read_x<(S < 7 ? S + 1 : 0), 0>(c, xf, xo); z_read_w<S + 1, 0>(c, F0, wb); }
  z_mma<false, 2 * S + 1, S + 5, 0>(c, a, xf, F1, acc, cs);
}
template <int S> DEV void z_stage_b(const Ctx& c, const ZAddr& a, const u32x4 (&xf)[2][16], ZW& F0, ZW& F1, f32x4 (&acc)[2][3], f32x4& cs,
                                    const unsigned (&wb)[3], u32x4 (&g)[2], unsigned gofs, const char* gbase) {
  z_read_w<S, 1>(c, F1, wb);
  z_mma<true, 2 * (S - 8), S + 4, 1>(c, a, xf, F0, acc, cs);
  if (S < 15) {
    wait_vm<z_wait(S + 1)>();
    bar();
    if (S == 14) { g[0] = aload128c(gofs, gbase); g[1] = aload128c(gofs, gbase + 2048); }
    z_read_w<S + 1, 0>(c, F0, wb);
  }
  z_mma<true, 2 * (S - 8) + 1, S + 5, 0>(c, a, xf, F1, acc, cs);
  if (S == 15) asm volatile("s_waitcnt vmcnt(0) ; TIE %0 %1" : "+v"(g[0]), "+v"(g[1]) : : "memory");
}

DEV int fb_start(int c) { return c < 3 ? 6 * c : 18 + 5 * (c - 3); }

DEV void job_z(Ctx& c, int i, int zc, int s0row, const G3A& g3) {
  u32x4 xr; u32x2 ev;
  s0_issue(c, xr, ev, s0row);
  char* ws = c.ws;
  const int lane = c.lane, w = c.w, rg = w & 3, cg = w >> 2;
  const int t = lane & 15, q = lane >> 4;
  const int fb0 = fb_start(zc), nb = zc < 3 ? 6 : 5;
  ZAddr a;
  {
    int r0 = 16 * w + (lane >> 3);
    a.vx0 = r0 * 1024 + (((lane & 7) ^ ((r0 >> 1) & 7)) << 4);
    int r1 = r0 + 8;
    a.vx1 = r1 * 1024 + (((lane & 7) ^ ((r1 >> 1) & 7)) << 4);
    a.bx = c.x + (size_t)i * 128 * 1024;
    int l48 = lane < 48 ? lane : 47;
    int kr = l48 / 12, cc = l48 % 12;
    int b = (cc >> 1) ^ (w & 1);
    int fbg = fb0 + b; fbg = fbg > 42 ? 42 : fbg;
    a.vw = (8 * w + kr) * 2752 + fbg * 32 + (cc & 1) * 16;
    a.bw = c.win;
  }
  z_half<0, 0>(c, a); z_half<0, 1>(c, a); z_half<1, 0>(c, a); z_half<1, 1>(c, a); z_half<2, 0>(c, a); z_half<2, 1>(c, a);
  s0_finish<12>(c, xr, ev, s0row);
  STAMP(15);
  z_half<3, 0>(c, a); z_half<3, 1>(c, a); z_half<4, 0>(c, a); z_half<4, 1>(c, a);

  const unsigned xo = rg * 32 * 128 + t * 128 + ((q ^ (t >> 1)) << 4);
  unsigned wb[3]; int lb3[3];
#pragma unroll
  for (int j = 0; j < 3; ++j) {
    int jr = j + (rg == 3 ? 0 : rg); jr = jr >= 3 ? jr - 3 : jr;
    lb3[j] = 3 * cg + jr;
    wb[j] = (8 * q + (t >> 2)) * 192 + ((lb3[j] ^ (q & 1)) << 5) + (t & 3) * 8;
  }
  const int gj = c.tid < 128 ? c.tid : 127;
  const unsigned gofs = (i * 128 + gj) * 16;
  const f32x4 z4 = {0.f, 0.f, 0.f, 0.f};
  u32x4 xf[2][16];
  f32x4 acc[2][3], cs = z4;
#pragma unroll
  for (int j = 0; j < 3; ++j) { acc[0][j] = z4; acc[1][j] = z4; }
  u32x4 g = {0, 0, 0, 0};
  ZW F0, F1;
  wait_vm<16>();
  bar();
  z_read_x<0, 0>(c, xf, xo); z_read_w<0, 0>(c, F0, wb);
  z_stage_a<0>(c, a, xf, F0, F1, acc, cs, xo, wb, g, gofs); z_stage_a<1>(c, a, xf, F0, F1, acc, cs, xo, wb, g, gofs);
  z_stage_a<2>(c, a, xf, F0, F1, acc, cs, xo, wb, g, gofs); z_stage_a<3>(c, a, xf, F0, F1, acc, cs, xo, wb, g, gofs);
  z_stage_a<4>(c, a, xf, F0, F1, acc, cs, xo, wb, g, gofs); z_stage_a<5>(c, a, xf, F0, F1, acc, cs, xo, wb, g, gofs);
  z_stage_a<6>(c, a, xf, F0, F1, acc, cs, xo, wb, g, gofs); z_stage_a<7>(c, a, xf, F0, F1, acc, cs, xo, wb, g, gofs);
  asm volatile("" : "+v"(acc[0][0]), "+v"(acc[0][1]), "+v"(acc[0][2]), "+v"(acc[1][0]), "+v"(acc[1][1]), "+v"(acc[1][2]), "+v"(cs));
  STAMP(2);
  g = settle_pair(g, c.rs, WS_XS + gofs, true, c.Tlo);
  if (c.tid < 128) { u32x2 mr = {g.x, g.z}; *(LAS u32x2*)(c.L + T_XS + c.tid * 8) = mr; }
  if (rg < 3 && lane < 16) *(LAS float*)(c.L + T_CS + (lb3[0] * 16 + lane) * 4) = cs[0];
  bar();
  u32x2 Z2[2][3];
  {
    float csf[3];
#pragma unroll
    for (int j = 0; j < 3; ++j) csf[j] = *(LAS float*)(c.L + T_CS + (lb3[j] * 16 + t) * 4);
#pragma unroll
    for (int x = 0; x < 2; ++x) {
      f32x4 m01 = *(LAS f32x4*)(c.L + T_XS + (rg * 32 + x * 16 + 4 * q) * 8);
      f32x4 m23 = *(LAS f32x4*)(c.L + T_XS + (rg * 32 + x * 16 + 4 * q + 2) * 8);
      float s1[4] = {0.f, 0.f, 0.f, 0.f}, s2[4] = {0.f, 0.f, 0.f, 0.f};
#pragma unroll
      for (int j = 0; j < 3; ++j) {
        f32x4 v = acc[x][j];
        float y0 = gelu_t(m01[1] * (v[0] - m01[0] * csf[j])), y1 = gelu_t(m01[3] * (v[1] - m01[2] * csf[j]));
        float y2 = gelu_t(m23[1] * (v[2] - m23[0] * csf[j])), y3 = gelu_t(m23[3] * (v[3] - m23[2] * csf[j]));
        unsigned p0 = pk2(y0, y1), p1 = pk2(y2, y3);
        Z2[x][j].x = p0; Z2[x][j].y = p1;
        if (lb3[j] < nb) {
          y0 = blo(p0); y1 = bhi(p0); y2 = blo(p1); y3 = bhi(p1);
          s1[0] += y0; s1[1] += y1; s1[2] += y2; s1[3] += y3;
          s2[0] = __builtin_fmaf(y0, y0, s2[0]); s2[1] = __builtin_fmaf(y1, y1, s2[1]);
          s2[2] = __builtin_fmaf(y2, y2, s2[2]); s2[3] = __builtin_fmaf(y3, y3, s2[3]);
        }
      }
#pragma unroll
      for (int r = 0; r < 4; ++r) { s1[r] = sum16(s1[r]); s2[r] = sum16(s2[r]); }
      if (t == 0) {
        f32x4 o0 = {s1[0], s2[0], s1[1], s2[1]}, o1 = {s1[2], s2[2], s1[3], s2[3]};
        LAS char* p = c.L + T_ZP + (cg * 128 + rg * 32 + x * 16 + 4 * q) * 8;
        *(LAS f32x4*)p = o0; *(LAS f32x4*)(p + 16) = o1;
      }
    }
  }
  bar();
  if (c.tid < 256) {
    int tok = c.tid >> 1, st = c.tid & 1;
    float v = *(LAS float*)(c.L + T_ZP + tok * 8 + st * 4) + *(LAS float*)(c.L + T_ZP + (128 + tok) * 8 + st * 4);
    gstore64(ws + WS_ZS + ((i * 8 + zc) * 128 + tok) * 16 + st * 8, ((u64)c.Tlo << 32) | __float_as_uint(v));
  }
  STAMP(3);
  cs = z4;
#pragma unroll
  for (int j = 0; j < 3; ++j) { acc[0][j] = z4; acc[1][j] = z4; }
  u32x4 gs[2] = {{0, 0, 0, 0}, {0, 0, 0, 0}};
  const int stok = c.tid & 127, pg = c.tid >> 7;
  const unsigned sofs = ((2 * pg) * 128 + stok) * 16;
  const char* sbase = ws + WS_ZS + (size_t)i * 8 * 128 * 16;
  z_read_w<8, 0>(c, F0, wb);
  z_stage_b<8>(c, a, xf, F0, F1, acc, cs, wb, gs, sofs, sbase);  z_stage_b<9>(c, a, xf, F0, F1, acc, cs, wb, gs, sofs, sbase);
  z_stage_b<10>(c, a, xf, F0, F1, acc, cs, wb, gs, sofs, sbase); z_stage_b<11>(c, a, xf, F0, F1, acc, cs, wb, gs, sofs, sbase);
  z_stage_b<12>(c, a, xf, F0, F1, acc, cs, wb, gs, sofs, sbase); z_stage_b<13>(c, a, xf, F0, F1, acc, cs, wb, gs, sofs, sbase);
  z_stage_b<14>(c, a, xf, F0, F1, acc, cs, wb, gs, sofs, sbase); z_stage_b<15>(c, a, xf, F0, F1, acc, cs, wb, gs, sofs, sbase);
  asm volatile("" : "+v"(acc[0][0]), "+v"(acc[0][1]), "+v"(acc[0][2]), "+v"(acc[1][0]), "+v"(acc[1][1]), "+v"(acc[1][2]), "+v"(cs));
  STAMP(4);
  {
    const unsigned so = WS_ZS + i * 8 * 128 * 16 + sofs;
    gs[0] = settle_pair(gs[0], c.rs, so, true, c.Tlo);
    gs[1] = settle_pair(gs[1], c.rs, so + 2048, true, c.Tlo);
    f32x2 p = {__uint_as_float(gs[0].x) + __uint_as_float(gs[1].x), __uint_as_float(gs[0].z) + __uint_as_float(gs[1].z)};
    *(LAS f32x2*)(c.L + T_ZQ + (pg * 128 + stok) * 8) = p;
  }
  if (rg < 3 && t == 0) *(LAS f32x4*)(c.L + T_CS + 512 + (lb3[0] * 16 + 4 * q) * 4) = cs;
  bar();
  g3_prefetch_w(c, g3);
  if (c.tid < 128) {
    f32x2 p0 = *(LAS f32x2*)(c.L + T_ZQ + c.tid * 8), p1 = *(LAS f32x2*)(c.L + T_ZQ + (128 + c.tid) * 8);
    f32x2 p2 = *(LAS f32x2*)(c.L + T_ZQ + (256 + c.tid) * 8), p3 = *(LAS f32x2*)(c.L + T_ZQ + (384 + c.tid) * 8);
    float s = (p0.x + p1.x) + (p2.x + p3.x), ss = (p0.y + p1.y) + (p2.y + p3.y);
    float m = s * (1.0f / 688.0f);
    float var = fmaxf(ss * (1.0f / 688.0f) - m * m, 0.f);
    f32x2 o = {m, 1.0f / sqrtf(var + 1e-5f)};
    *(LAS f32x2*)(c.L + T_ZF + c.tid * 8) = o;
  }
  bar();
  {
    const int qp = ((q & 1) << 1) | (q >> 1);
    f32x4 mr[2][2];
#pragma unroll
    for (int x = 0; x < 2; ++x) {
      mr[x][0] = *(LAS f32x4*)(c.L + T_ZF + (rg * 32 + x * 16 + 4 * q) * 8);
      mr[x][1] = *(LAS f32x4*)(c.L + T_ZF + (rg * 32 + x * 16 + 4 * q + 2) * 8);
    }
#pragma unroll
    for (int j = 0; j < 3; ++j) {
      u32x2 n[2];
#pragma unroll
      for (int x = 0; x < 2; ++x) {
        unsigned p0 = Z2[x][j].x, p1 = Z2[x][j].y;
        n[x].x = pk2((blo(p0) - mr[x][0][0]) * mr[x][0][1], (bhi(p0) - mr[x][0][2]) * mr[x][0][3]);
        n[x].y = pk2((blo(p1) - mr[x][1][0]) * mr[x][1][1], (bhi(p1) - mr[x][1][2]) * mr[x][1][3]);
      }
      u32x4 fr = pairswap(n[0], n[1]);
      if (lb3[j] < nb) {
        unsigned off = WS_Z2N + ((i * 4 + rg) * 44 + fb0 + lb3[j]) * 1024 + qp * 256 + t * 16;
        __builtin_amdgcn_raw_buffer_store_b128(fr, c.rs, off, 0, 16);
      }
    }
  }
  wait_vm<0>();
  bar();
  if (c.tid == 0) gstore64(ws + WS_FZ2 + (zc * 16 + i) * 128, tagT(c));
  STAMP(5);
  {
    f32x2 mr[2];
#pragma unroll
    for (int x = 0; x < 2; ++x) mr[x] = *(LAS f32x2*)(c.L + T_XS + (rg * 32 + x * 16 + t) * 8);
#pragma unroll
    for (int j = 0; j < 3; ++j) {
      f32x4 c4 = *(LAS f32x4*)(c.L + T_CS + 512 + (lb3[j] * 16 + 4 * q) * 4);
      u32x2 n[2];
#pragma unroll
      for (int x = 0; x < 2; ++x) {
        f32x4 v = acc[x][j];
        n[x].x = pk2(gelu_t(mr[x].y * (v[0] - mr[x].x * c4[0])), gelu_t(mr[x].y * (v[1] - mr[x].x * c4[1])));
        n[x].y = pk2(gelu_t(mr[x].y * (v[2] - mr[x].x * c4[2])), gelu_t(mr[x].y * (v[3] - mr[x].x * c4[3])));
      }
      u32x4 fr = pairswap(n[0], n[1]);
      if (lb3[j] < nb) {
        int fb = fb0 + lb3[j];
        unsigned off = WS_Z1 + ((i * 8 + rg * 2 + (q & 1)) * 22 + (fb >> 1)) * 1024 + (2 * (fb & 1) + (q >> 1)) * 256 + t * 16;
        __builtin_amdgcn_raw_buffer_store_b128(fr, c.rs, off, 0, 16);
      }
    }
  }
  wait_vm<0>();
  bar();
  if (c.tid == 0) gstore64(ws + WS_FZ1 + (i * 8 + zc) * 128, tagT(c));
  STAMP(6);
}

struct HAddr { unsigned va, vw; const char *bo, *bu, *bwo, *bwout; };
template <int T> DEV void h_issue_w(const Ctx& c, const HAddr& a) {
  const unsigned l = c.lb + ((32 + T) % 7) * G_SLOT + 8192 + c.w * 1024;
  if (T < 8) dma16(a.vw, a.bwo + (size_t)T * 64 * 1024, l);
  else if (T < 18) dma16(a.vw, a.bwout + (size_t)(T - 8) * 64 * 1024, l);
  else {
    int k = 8 * c.w + (c.lane >> 3);
    unsigned v = k > 47 ? a.vw - (k - 47) * 1024 : a.vw;
    dma16(v, a.bwout + (size_t)640 * 1024, l);
  }
}
template <int T> DEV void h_issue_a(const Ctx& c, const HAddr& a) {
  const unsigned l = c.lb + ((32 + T) % 7) * G_SLOT + c.w * 1024;
  if (T < 8) dma16c(a.va, a.bo + T * 2048, l);
  else dma16c(a.va, a.bu + (T - 8) * 2048, l);
}
struct HF { u32x4 b[2], f0[2], f1[2]; };
template <int T> DEV void h_read(const Ctx& c, HF& F, unsigned ao, unsigned w0, unsigned w1) {
  LAS char* L = c.L + ((32 + T) % 7) * G_SLOT;
#pragma unroll
  for (int kk = 0; kk < 2; ++kk) {
    F.b[kk] = lds128(L + ao + kk * 4096);
    F.f0[kk] = trfrag(L + 8192 + w0 + kk * 4096, 512);
    F.f1[kk] = trfrag(L + 8192 + w1 + kk * 4096, 512);
  }
  sb();
}
template <int T, int IA, int IW> DEV void h_mma(const Ctx& c, const HAddr& h, const HF& F, f32x4 (&acc)[2]) {
  if (IA >= 0) h_issue_a<(IA < 0 ? 0 : IA)>(c, h);
  sb();
  acc[0] = mfma32(F.f0[0], F.b[0], acc[0]);
  acc[1] = mfma32(F.f1[0], F.b[0], acc[1]);
  sb();
  if (IW >= 0) h_issue_w<(IW < 0 ? 0 : IW)>(c, h);
  sb();
  u32x4 b = F.b[1];
  if (T == 18) {
    if (c.lane >= 32) { u32x4 z = {0, 0, 0, 0}; b = z; }
  }
  acc[0] = mfma32(F.f0[1], b, acc[0]);
  acc[1] = mfma32(F.f1[1], b, acc[1]);
  sb();
}
template <int T, int IA, int IW, bool NEXT> DEV void h_it(const Ctx& c, const HAddr& h, HF& Fc, HF& Fn, f32x4 (&acc)[2],
                                                         unsigned ao, unsigned w0, unsigned w1) {
  bar();
  if (NEXT) h_read<T + 1>(c, Fn, ao, w0, w1);
  h_mma<T, IA, IW>(c, h, Fc, acc);
}

template <int V> struct IC { static constexpr int v = V; };
struct GF { u32x4 b0, b1, a0, a1, a2; };

DEV void job_g(Ctx& c, int rt, int gc, const G3A& g3, bool epoch_wg) {
  char* ws = c.ws;
  const int lane = c.lane, w = c.w;
  const int t = lane & 15, q = lane >> 4;
  const int rg = w & 1, cg = (w >> 1) & 1, kh = w >> 2;
  const int fb0 = fb_start(gc), nb = gc < 3 ? 6 : 5;
  const u64 T = tagT(c);
  float bias = blo(*(const GAS u16*)(size_t)(c.bsp + ((rt * 4 + rg * 2 + kh) * 16 + t) * 2));
  if (w == 0) poll_flags(ws + WS_FZ2 + (gc * 16 + (lane & 15)) * 128, lane < 16, T);
  asm volatile("" : "+v"(bias));
  bar();
  STAMP(8);
  const char* zb = ws + WS_Z2N + (size_t)fb0 * 1024;
  const unsigned vz1 = ((w / 6) * 44 + (w % 6)) * 1024 + lane * 16;
  const int u2 = 8 + (w >> 1);
  const unsigned vz2 = (44 + (u2 - 6)) * 1024 + (w & 1) * 512 + (lane & 31) * 16;
  const unsigned lz1 = c.lb + G_Z + w * 1024, lz2 = c.lb + G_Z + u2 * 1024 + (w & 1) * 512;
#pragma unroll
  for (int s = 0; s < 7; ++s) {
    dma16c(vz1, zb + (size_t)s * 88 * 1024, lz1 + s * G_SLOT);
    dma16mc(vz2, zb + (size_t)s * 88 * 1024, lz2 + s * G_SLOT, 0xffffffffull);
  }
  HAddr h;
  {
    h.va = ((w & 3) * 16 + (w >> 2)) * 1024 + lane * 16;
    int k = 8 * w + (lane >> 3);
    int f = ((k >> 1) & 1) | (((k >> 3) & 1) << 1);
    h.vw = k * 1024 + (((lane & 7) ^ (f << 1)) << 4);
    h.bo = ws + WS_O + (size_t)rt * 4 * 16 * 1024;
    h.bu = ws + WS_U + (size_t)rt * 4 * 22 * 1024;
    h.bwo = c.wo + gc * 128;
    h.bwout = c.wout + gc * 128;
  }
  const unsigned vau = ((w & 3) * 22 + (w >> 2)) * 1024 + lane * 16;
  const unsigned bo_ = rg * 32 * 128 + t * 128 + (((4 * kh + q) ^ (t >> 1)) << 4);
  const unsigned ao_ = G_Z + (kh * 6 + 3 * cg) * 1024 + lane * 16;
  const f32x4 z4 = {0.f, 0.f, 0.f, 0.f};
  f32x4 acc[2][3];
#pragma unroll
  for (int j = 0; j < 3; ++j) { acc[0][j] = z4; acc[1][j] = z4; }
  const int tbk = rt * 4 + rg * 2 + kh;
  const unsigned flofs = lane < 8 ? WS_FO + ((rt >> 1) * 8 + lane) * 128 : WS_FZ1 + ((rt >> 1) * 8 + gc) * 128;
  unsigned z1ofs[3];
#pragma unroll
  for (int j = 0; j < 3; ++j) {
    int fb = fb0 + 3 * cg + j; fb = fb > 42 ? 42 : fb;
    z1ofs[j] = (tbk * 22 + (fb >> 1)) * 1024 + (2 * (fb & 1) + (q >> 1)) * 256 + t * 16 + (q & 1) * 8;
  }
  unsigned slot = 0;
  const char* pw = g3.bw + 7 * 128;
  const char* pz = zb + (size_t)7 * 88 * 1024;
  auto rd = [&](GF& F, unsigned sl) {
    LAS char* L = c.L + sl;
    F.a0 = lds128(L + ao_); F.b0 = lds128(L + bo_); F.b1 = lds128(L + bo_ + 2048);
    F.a1 = lds128(L + ao_ + 1024); F.a2 = lds128(L + ao_ + 2048);
    sb();
  };
  auto it = [&](auto kind, auto ht, GF& Fc, GF& Fn, bool next) {
    constexpr int KIND = decltype(kind)::v, HT = decltype(ht)::v;
    bar();
    const unsigned nsl = slot == 6 * G_SLOT ? 0 : slot + G_SLOT;
    if (KIND == 0) dma16(g3.vw, pw, c.lb + slot + w * 1024);
    if (KIND == 1) h_issue_a<HT>(c, h);
    sb();
    if (next) rd(Fn, nsl);
    acc[0][0] = mfma32(Fc.a0, Fc.b0, acc[0][0]); acc[1][0] = mfma32(Fc.a0, Fc.b1, acc[1][0]);
    acc[0][1] = mfma32(Fc.a1, Fc.b0, acc[0][1]);
    sb();
    if (KIND == 0) dma16c(vz1, pz, lz1 + slot);
    if (KIND == 1) h_issue_w<HT>(c, h);
    sb();
    acc[1][1] = mfma32(Fc.a1, Fc.b1, acc[1][1]);
    acc[0][2] = mfma32(Fc.a2, Fc.b0, acc[0][2]); acc[1][2] = mfma32(Fc.a2, Fc.b1, acc[1][2]);
    sb();
    if (KIND == 0) { dma16mc(vz2, pz, lz2 + slot, 0xffffffffull); pw += 128; pz += 88 * 1024; }
    sb();
    slot = nsl;
  };
  const IC<0> K0; const IC<1> K1; const IC<2> K2;
  GF F0, F1;
  wait_vm<12>();
  bar();
  rd(F0, 0);
  wait_vm<10>(); it(K0, K0, F0, F1, true);
  wait_vm<11>(); it(K0, K0, F1, F0, true);
  wait_vm<12>(); it(K0, K0, F0, F1, true);
  wait_vm<13>(); it(K0, K0, F1, F0, true);
  wait_vm<14>(); it(K0, K0, F0, F1, true);
  wait_vm<15>(); it(K0, K0, F1, F0, true);
#pragma unroll 1
  for (int s = 6; s < 10; s += 2) { wait_vm<15>(); it(K0, K0, F0, F1, true); wait_vm<15>(); it(K0, K0, F1, F0, true); }
  u32x2 fl = aload64c(flofs, ws);
#pragma unroll 1
  for (int s = 10; s < 16; s += 2) { wait_vm<15>(); it(K0, K0, F0, F1, true); wait_vm<15>(); it(K0, K0, F1, F0, true); }
  asm volatile("; TIE %0" : "+v"(fl));
  if (!__all(fl.x == c.Tlo && fl.y == c.Thi)) poll_flags(ws + flofs, true, T);
  u32x2 z1v[3];
  z1v[0] = aload64c(z1ofs[0], ws + WS_Z1);
  z1v[1] = aload64c(z1ofs[1], ws + WS_Z1);
  z1v[2] = aload64c(z1ofs[2], ws + WS_Z1);
#pragma unroll 1
  for (int s = 16; s < 24; s += 2) { wait_vm<15>(); it(K0, K0, F0, F1, true); wait_vm<15>(); it(K0, K0, F1, F0, true); }
  wait_vm<15>(); it(K0, K0, F0, F1, true);
  wait_vm<15>(); it(K1, IC<0>(), F1, F0, true);
  wait_vm<14>(); it(K1, IC<1>(), F0, F1, true);
  wait_vm<13>(); it(K1, IC<2>(), F1, F0, true);
  wait_vm<12>(); it(K1, IC<3>(), F0, F1, true);
  wait_vm<11>(); it(K1, IC<4>(), F1, F0, true);
  wait_vm<10>(); it(K1, IC<5>(), F0, F1, true);
  it(K2, K0, F1, F0, false);
  asm volatile("; TIE %0 %1 %2" : "+v"(z1v[0]), "+v"(z1v[1]), "+v"(z1v[2]));
  STAMP(9);
  {
    const unsigned e01 = 3 * G_SLOT + w * 2048 + lane * 16, e2 = T_EX2 + w * 1024 + lane * 16;
    const unsigned p01 = 3 * G_SLOT + (w ^ 4) * 2048 + lane * 16, p2 = T_EX2 + (w ^ 4) * 1024 + lane * 16;
    f32x4 keep[3], send[3];
#pragma unroll
    for (int j = 0; j < 3; ++j) { keep[j] = kh ? acc[1][j] : acc[0][j]; send[j] = kh ? acc[0][j] : acc[1][j]; }
    *(LAS f32x4*)(c.L + e01) = send[0]; *(LAS f32x4*)(c.L + e01 + 1024) = send[1]; *(LAS f32x4*)(c.L + e2) = send[2];
    bar();
    keep[0] += *(LAS f32x4*)(c.L + p01); keep[1] += *(LAS f32x4*)(c.L + p01 + 1024); keep[2] += *(LAS f32x4*)(c.L + p2);
#pragma unroll
    for (int j = 0; j < 3; ++j) {
      unsigned g0 = pk2(keep[j][0] + bias, keep[j][1] + bias), g1 = pk2(keep[j][2] + bias, keep[j][3] + bias);
      unsigned a0 = z1v[j].x, a1 = z1v[j].y;
      u32x2 u;
      u.x = pk2(blo(a0) * blo(g0), bhi(a0) * bhi(g0));
      u.y = pk2(blo(a1) * blo(g1), bhi(a1) * bhi(g1));
      if (3 * cg + j < nb) __builtin_amdgcn_raw_buffer_store_b64(u, c.rs, WS_U + z1ofs[j], 0, 16);
    }
  }
  wait_vm<0>();
  bar();
  if (c.tid == 0) gstore64(ws + WS_FU + (rt * 8 + gc) * 128, T);
  STAMP(10);

  const int tb = w & 3, np = w >> 2;
  const unsigned ha = tb * 1024 + lane * 16;
  const int fw = ((t >> 3) & 1) | ((q & 1) << 1);
  const unsigned wl = (8 * q + (t >> 2)) * 128 + (t & 3) * 8;
  const unsigned hw0 = wl + (((2 * np) ^ fw) << 5), hw1 = wl + (((2 * np + 1) ^ fw) << 5);
  f32x4 sa[2] = {z4, z4};
  HF H0, H1;
  h_issue_a<6>(c, h); h_issue_w<6>(c, h);
  h_read<0>(c, H0, ha, hw0, hw1);
  h_it<0, 7, 7, true>(c, h, H0, H1, sa, ha, hw0, hw1);
  h_it<1, -1, 8, true>(c, h, H1, H0, sa, ha, hw0, hw1);
  h_it<2, -1, 9, true>(c, h, H0, H1, sa, ha, hw0, hw1);
  h_it<3, -1, 10, true>(c, h, H1, H0, sa, ha, hw0, hw1);
  h_it<4, -1, 11, true>(c, h, H0, H1, sa, ha, hw0, hw1);
  wait_vm<6>(); h_it<5, -1, 12, true>(c, h, H1, H0, sa, ha, hw0, hw1);
  wait_vm<5>(); h_it<6, -1, 13, true>(c, h, H0, H1, sa, ha, hw0, hw1);
  h_it<7, -1, 14, false>(c, h, H1, H0, sa, ha, hw0, hw1);
  asm volatile("" : "+v"(sa[0]), "+v"(sa[1]));
  STAMP(11);
  h.va = vau;
  if (w == 0) poll_flags(ws + WS_FU + (rt * 8 + (lane & 7)) * 128, lane < 8, T);
  if (epoch_wg && w == 1) {
#pragma unroll
    for (int j = 0; j < 2; ++j) {
      poll_flags(ws + WS_FZ2 + (j * 64 + lane) * 128, true, T);
      poll_flags(ws + WS_FO + (j * 64 + lane) * 128, true, T);
    }
    if (lane == 0) gstore64(ws + WS_EPOCH, T);
  }
  bar();
  STAMP(12);
  h_issue_a<8>(c, h); h_issue_a<9>(c, h); h_issue_a<10>(c, h); h_issue_a<11>(c, h); h_issue_a<12>(c, h); h_issue_a<13>(c, h);
  h_issue_a<14>(c, h);
  const int orow = rt * 64 + tb * 16 + t;
  const unsigned oofs = orow * 1024 + gc * 128 + np * 64 + (q & 1) * 32 + (q >> 1) * 16;
  f32x4 ga[2] = {z4, z4};
  wait_vm<6>();
  bar();
  h_read<8>(c, H0, ha, hw0, hw1);
  wait_vm<5>(); h_it<8, 15, 15, true>(c, h, H0, H1, ga, ha, hw0, hw1);
  wait_vm<6>(); h_it<9, 16, 16, true>(c, h, H1, H0, ga, ha, hw0, hw1);
  wait_vm<7>(); h_it<10, 17, 17, true>(c, h, H0, H1, ga, ha, hw0, hw1);
  wait_vm<8>(); h_it<11, 18, 18, true>(c, h, H1, H0, ga, ha, hw0, hw1);
  wait_vm<9>(); h_it<12, -1, -1, true>(c, h, H0, H1, ga, ha, hw0, hw1);
  u32x4 xres;
  asm volatile("global_load_dwordx4 %0, %1, %2" : "=v"(xres) : "v"(oofs), "s"(uni(c.x)) : "memory");
  wait_vm<9>(); h_it<13, -1, -1, true>(c, h, H1, H0, ga, ha, hw0, hw1);
  wait_vm<7>(); h_it<14, -1, -1, true>(c, h, H0, H1, ga, ha, hw0, hw1);
  wait_vm<5>(); h_it<15, -1, -1, true>(c, h, H1, H0, ga, ha, hw0, hw1);
  wait_vm<3>(); h_it<16, -1, -1, true>(c, h, H0, H1, ga, ha, hw0, hw1);
  wait_vm<1>(); h_it<17, -1, -1, true>(c, h, H1, H0, ga, ha, hw0, hw1);
  h_mma<18, -1, -1>(c, h, H0, ga);
  asm volatile("s_waitcnt vmcnt(0) ; TIE %0" : "+v"(xres) : : "memory");
  STAMP(13);
  {
    u32x2 S[2], G[2];
#pragma unroll
    for (int b = 0; b < 2; ++b) {
      S[b].x = pk2(sa[b][0], sa[b][1]); S[b].y = pk2(sa[b][2], sa[b][3]);
      G[b].x = pk2(ga[b][0], ga[b][1]); G[b].y = pk2(ga[b][2], ga[b][3]);
    }
    u32x4 s8 = pairswap(S[0], S[1]), g8 = pairswap(G[0], G[1]);
    u32x4 o;
#pragma unroll
    for (int j = 0; j < 4; ++j) {
      unsigned su = s8[j], gu = g8[j], xu = xres[j];
      float g0 = blo(gu), g1 = bhi(gu);
      float e0 = g0 * __builtin_amdgcn_rcpf(1.0f + __builtin_amdgcn_exp2f(g0 * -1.4426950408889634f));
      float e1 = g1 * __builtin_amdgcn_rcpf(1.0f + __builtin_amdgcn_exp2f(g1 * -1.4426950408889634f));
      o[j] = pk2(__builtin_fmaf(blo(su), e0, blo(xu)), __builtin_fmaf(bhi(su), e1, bhi(xu)));
    }
    *(GAS u32x4*)(size_t)(c.out + oofs) = o;
  }
  STAMP(14); STAMPC(31);
}

__global__ __launch_bounds__(512, 2) void fused_kernel(const char* x, const char* wqkv, const char* wo, const char* win, const char* wsp,
                                                       const char* bsp, const char* wout, char* out, char* ws) {
  extern __shared__ __attribute__((aligned(16))) char smem[];
  Ctx c;
  c.x = x; c.wqkv = wqkv; c.wo = wo; c.win = win; c.wsp = wsp; c.bsp = bsp; c.wout = wout; c.out = out; c.ws = ws;
  c.L = (LAS char*)smem;
  c.lb = (unsigned)(size_t)c.L;
  c.tid = threadIdx.x; c.lane = c.tid & 63; c.w = __builtin_amdgcn_readfirstlane(c.tid >> 6);
  c.rs = __builtin_amdgcn_make_buffer_rsrc((void*)ws, 0, WS_USED, 0x00020000);
  c.Tlo = 0; c.Thi = 0;
  c.bid = blockIdx.x;
  STAMP(0); STAMPC(30);
  const int bid = blockIdx.x, xcd = bid & 7, sl = bid >> 3;
  const int i = (xcd & 1) * 8 + (sl >> 2), rsel = sl & 3, col = 2 * (xcd >> 1) + (rsel & 1);
  const int r16 = (xcd >> 1) * 4 + rsel;
  const int rt = (xcd >> 1) * 8 + (sl >> 2), gc = (xcd & 1) * 4 + (sl & 3);
  const int s0row = i * 128 + r16 * 8 + c.w;
  const G3A g3 = g3_addr(c, rt);
  if (rsel < 2) job_a(c, i, col, s0row, g3);
  else job_z(c, i, col, s0row, g3);
  STAMP(7);
  job_g(c, rt, gc, g3, bid == 0);
}

extern "C" void kernel_launch(void* const* d_in, const int* in_sizes, int n_in, void* d_out, int out_size, void* d_ws, size_t ws_size,
                              hipStream_t stream) {
  (void)in_sizes; (void)n_in; (void)out_size; (void)ws_size;
  (void)hipFuncSetAttribute((const void*)fused_kernel, hipFuncAttributeMaxDynamicSharedMemorySize, LDS_BYTES);
  fused_kernel<<<256, 512, LDS_BYTES, stream>>>((const char*)d_in[0], (const char*)d_in[1], (const char*)d_in[2], (const char*)d_in[4],
                                                (const char*)d_in[5], (const char*)d_in[6], (const char*)d_in[7], (char*)d_out, (char*)d_ws);
}
```

```cpp
#define PF 3
#define OUTSC 3
#include <hip/hip_runtime.h>
#define EXP 0

typedef unsigned short u16;
typedef unsigned long long u64;
typedef __bf16 bf16x8 __attribute__((ext_vector_type(8)));
typedef __bf16 bf16x2 __attribute__((ext_vector_type(2)));
typedef short s16x4 __attribute__((ext_vector_type(4)));
typedef float f32x4 __attribute__((ext_vector_type(4)));
typedef float f32x2 __attribute__((ext_vector_type(2)));
typedef unsigned u32x4 __attribute__((ext_vector_type(4)));
typedef unsigned u32x2 __attribute__((ext_vector_type(2)));
#define LAS __attribute__((address_space(3)))
#define GAS __attribute__((address_space(1)))
#define DEV __device__ __forceinline__

constexpr unsigned WS_EPOCH = 0;
constexpr unsigned WS_FZ2 = 4096;
constexpr unsigned WS_FZ1 = WS_FZ2 + 128 * 128;
constexpr unsigned WS_FO  = WS_FZ1 + 128 * 128;
constexpr unsigned WS_FU  = WS_FO + 128 * 128;
constexpr unsigned WS_XS  = 0x20000;
constexpr unsigned WS_ZS  = 0x30000;
constexpr unsigned WS_Z2N = 0x100000;
constexpr unsigned WS_Z1  = 0x400000;
constexpr unsigned WS_U   = 0x700000;
constexpr unsigned WS_O   = 0xA00000;
constexpr unsigned WS_DBG = 0xD00000;
constexpr unsigned WS_USED = 0xE00000;

constexpr unsigned LDS_BYTES = 160 * 1024;
constexpr unsigned A_SLOT = 43008, A_W = 18432;
constexpr unsigned Z_SLOT = 28672, Z_W = 16384;
constexpr unsigned G_SLOT = 20480, G_Z = 8192;
constexpr unsigned T_BASE = 143360;
constexpr unsigned T_EX2 = T_BASE;
constexpr unsigned T_XS = T_BASE + 8192;
constexpr unsigned T_CS = T_XS + 1280;
constexpr unsigned T_ZP = T_CS + 1024;
constexpr unsigned T_ZQ = T_ZP + 2048;
constexpr unsigned T_ZF = T_ZQ + 4096;
constexpr unsigned T_DUMMY = T_ZF + 1024;
static_assert(T_DUMMY + 256 <= LDS_BYTES, "lds");

constexpr unsigned SPIN_CAP = 200000;

#define STAMP1(k) do {} while (0)
#define STAMPC(k) do {} while (0)
#define STAMP(k) do {} while (0)

DEV const char* uni(const char* p) {
  u64 v = (u64)(size_t)p;
  unsigned lo = __builtin_amdgcn_readfirstlane((unsigned)v), hi = __builtin_amdgcn_readfirstlane((unsigned)(v >> 32));
  return (const char*)(size_t)(((u64)hi << 32) | lo);
}
DEV unsigned uni(unsigned v) { return __builtin_amdgcn_readfirstlane(v); }
struct Ctx0 { unsigned sink; };
DEV void dma16(unsigned voff, const char* sbase, unsigned lds) {
  asm volatile("s_mov_b32 m0, %2\n\ts_nop 0\n\tglobal_load_lds_dwordx4 %0, %1" ::"v"(voff), "s"(uni(sbase)), "s"(uni(lds)) : "memory");
}
DEV void dma16c(unsigned voff, const char* sbase, unsigned lds) {
  asm volatile("s_mov_b32 m0, %2\n\ts_nop 0\n\tglobal_load_lds_dwordx4 %0, %1 sc1" ::"v"(voff), "s"(uni(sbase)), "s"(uni(lds)) : "memory");
}
DEV void dma16m(unsigned voff, const char* sbase, unsigned lds, u64 mask) {
  asm volatile("s_mov_b32 m0, %2\n\ts_mov_b64 exec, %3\n\tglobal_load_lds_dwordx4 %0, %1\n\ts_mov_b64 exec, -1"
               ::"v"(voff), "s"(uni(sbase)), "s"(uni(lds)), "s"(mask) : "memory");
}
DEV void dma16mc(unsigned voff, const char* sbase, unsigned lds, u64 mask) {
  asm volatile("s_mov_b32 m0, %2\n\ts_mov_b64 exec, %3\n\tglobal_load_lds_dwordx4 %0, %1 sc1\n\ts_mov_b64 exec, -1"
               ::"v"(voff), "s"(uni(sbase)), "s"(uni(lds)), "s"(mask) : "memory");
}
DEV void touch(const Ctx0& c, const char* p) {
  asm volatile("s_mov_b32 m0, %1\n\ts_nop 0\n\tglobal_load_lds_dword %0, off" ::"v"(p), "s"(uni(c.sink)) : "memory");
}
template <int N> DEV void wait_vm() { asm volatile("s_waitcnt vmcnt(%0)" ::"n"(N) : "memory"); }
DEV void sb() { __builtin_amdgcn_sched_barrier(0); }
DEV void bar() { asm volatile("s_waitcnt lgkmcnt(0)\n\ts_barrier" ::: "memory"); }

template <int V = 0> DEV f32x4 mfmaX(u32x4 a, u32x4 b, f32x4 c) {
  return __builtin_amdgcn_mfma_f32_16x16x32_bf16(__builtin_bit_cast(bf16x8, a), __builtin_bit_cast(bf16x8, b), c, 0, 0, 0);
}
DEV f32x4 mfma32(u32x4 a, u32x4 b, f32x4 c) {
  return __builtin_amdgcn_mfma_f32_16x16x32_bf16(__builtin_bit_cast(bf16x8, a), __builtin_bit_cast(bf16x8, b), c, 0, 0, 0);
}
DEV f32x4 mfma16(u32x2 a, u32x2 b, f32x4 c) {
  return __builtin_amdgcn_mfma_f32_16x16x16bf16_1k(__builtin_bit_cast(s16x4, a), __builtin_bit_cast(s16x4, b), c, 0, 0, 0);
}
DEV u32x4 lds128(LAS char* p) { return *(LAS u32x4*)p; }
DEV u32x2 lds64(LAS char* p) { return *(LAS u32x2*)p; }
DEV u32x4 trfrag(LAS char* p, int off4) {
  s16x4 a = __builtin_amdgcn_ds_read_tr16_b64_v4i16((LAS s16x4*)p);
  s16x4 b = __builtin_amdgcn_ds_read_tr16_b64_v4i16((LAS s16x4*)(p + off4));
  u32x2 ua = __builtin_bit_cast(u32x2, a), ub = __builtin_bit_cast(u32x2, b);
  u32x4 r = {ua.x, ua.y, ub.x, ub.y};
  return r;
}
DEV unsigned pk2(float a, float b) {
  f32x2 v = {a, b};
  bf16x2 h = __builtin_convertvector(v, bf16x2);
  return __builtin_bit_cast(unsigned, h);
}
DEV float blo(unsigned u) { return __uint_as_float(u << 16); }
DEV float bhi(unsigned u) { return __uint_as_float(u & 0xffff0000u); }
template <int CTRL> DEV float dpp(float v) {
  return __uint_as_float(__builtin_amdgcn_update_dpp(0u, __float_as_uint(v), CTRL, 0xf, 0xf, true));
}
DEV float sum16(float v) {
  v += dpp<0x128>(v); v += dpp<0x124>(v); v += dpp<0x122>(v); v += dpp<0x121>(v);
  return v;
}
DEV float sumq(float v) {
  unsigned u = __float_as_uint(v);
  auto a = __builtin_amdgcn_permlane16_swap(u, u, false, false);
  v = __uint_as_float(a[0]) + __uint_as_float(a[1]);
  u = __float_as_uint(v);
  auto b = __builtin_amdgcn_permlane32_swap(u, u, false, false);
  return __uint_as_float(b[0]) + __uint_as_float(b[1]);
}
DEV float maxq(float v) {
  unsigned u = __float_as_uint(v);
  auto a = __builtin_amdgcn_permlane16_swap(u, u, false, false);
  v = fmaxf(__uint_as_float(a[0]), __uint_as_float(a[1]));
  u = __float_as_uint(v);
  auto b = __builtin_amdgcn_permlane32_swap(u, u, false, false);
  return fmaxf(__uint_as_float(b[0]), __uint_as_float(b[1]));
}
DEV u32x4 pairswap(u32x2 b0, u32x2 b1) {
  auto s0 = __builtin_amdgcn_permlane16_swap(b0.x, b1.x, false, false);
  auto s1 = __builtin_amdgcn_permlane16_swap(b0.y, b1.y, false, false);
  u32x4 r = {s0[0], s1[0], s0[1], s1[1]};
  return r;
}
DEV float gelu_t(float v) {
  float e = __builtin_amdgcn_exp2f(v * __builtin_fmaf(v * v, -0.10294324f, -2.3022082f));
  return v * __builtin_amdgcn_rcpf(1.0f + e);
}
DEV u64 gload64(const char* p) { return __hip_atomic_load((GAS u64*)(size_t)p, __ATOMIC_RELAXED, __HIP_MEMORY_SCOPE_AGENT); }
DEV void gstore64(const char* p, u64 v) { __hip_atomic_store((GAS u64*)(size_t)p, v, __ATOMIC_RELAXED, __HIP_MEMORY_SCOPE_AGENT); }
DEV void poll_flags(const char* p, bool active, u64 T) {
  for (unsigned it = 0; it < SPIN_CAP; ++it) {
    bool ok = !active || gload64(p) == T;
    if (__all(ok)) break;
    __builtin_amdgcn_s_sleep(1);
  }
}
DEV u32x4 settle_pair(u32x4 g, __amdgpu_buffer_rsrc_t rs, unsigned off, bool active, unsigned T32) {
  for (unsigned it = 0; it < SPIN_CAP; ++it) {
    bool ok = !active || (g.y == T32 && g.w == T32);
    if (__all(ok)) break;
    __builtin_amdgcn_s_sleep(1);
    g = __builtin_amdgcn_raw_buffer_load_b128(rs, off, 0, 16);
  }
  return g;
}
DEV u32x4 aload128c(unsigned voff, const char* sbase) {
  u32x4 r;
  asm volatile("global_load_dwordx4 %0, %1, %2 sc1" : "=v"(r) : "v"(voff), "s"(uni(sbase)) : "memory");
  return r;
}
DEV u32x2 aload64c(unsigned voff, const char* sbase) {
  u32x2 r;
  asm volatile("global_load_dwordx2 %0, %1, %2 sc1" : "=v"(r) : "v"(voff), "s"(uni(sbase)) : "memory");
  return r;
}

struct Ctx {
  const char *x, *wqkv, *wo, *win, *wsp, *bsp, *wout;
  char *out, *ws;
  LAS char* L;
  unsigned lb;
  int tid, lane, w, bid;
  unsigned Tlo, Thi;
  __amdgpu_buffer_rsrc_t rs;
};
DEV u64 tagT(const Ctx& c) { return ((u64)c.Thi << 32) | c.Tlo; }

DEV void s0_issue(const Ctx& c, u32x4& xr, u32x2& ev, int row) {
  ev = aload64c(0, c.ws + WS_EPOCH);
  asm volatile("global_load_dwordx4 %0, %1, %2" : "=v"(xr) : "v"(c.lane * 16), "s"(uni(c.x + (size_t)row * 1024)) : "memory");
}
template <int P> DEV void s0_finish(Ctx& c, u32x4 xr, u32x2 ev, int row) {
  asm volatile("s_waitcnt vmcnt(%2) ; TIE %0 %1" : "+v"(xr), "+v"(ev) : "n"(P) : "memory");
  u64 e = (((u64)(unsigned)__builtin_amdgcn_readfirstlane(ev.y)) << 32) | (unsigned)__builtin_amdgcn_readfirstlane(ev.x);
  e += 1;
  c.Tlo = (unsigned)e; c.Thi = (unsigned)(e >> 32);
  unsigned x0 = xr.x, x1 = xr.y, x2 = xr.z, x3 = xr.w;
  float v[8] = {blo(x0), bhi(x0), blo(x1), bhi(x1), blo(x2), bhi(x2), blo(x3), bhi(x3)};
  float s = ((v[0] + v[1]) + (v[2] + v[3])) + ((v[4] + v[5]) + (v[6] + v[7]));
  s = sumq(sum16(s));
  float m = s * (1.0f / 512.0f), q = 0.f;
#pragma unroll
  for (int j = 0; j < 8; ++j) { float d = v[j] - m; q = __builtin_fmaf(d, d, q); }
  q = sumq(sum16(q));
  float r = 1.0f / sqrtf(q * (1.0f / 512.0f) + 1e-5f);
  if (c.lane < 2)
    gstore64(c.ws + WS_XS + row * 16 + c.lane * 8, ((u64)c.Tlo << 32) | __float_as_uint(c.lane ? r : m));
}

struct G3A { unsigned vw; const char* bw; };
DEV G3A g3_addr(const Ctx& c, int rt) {
  int r = 8 * c.w + (c.lane >> 3);
  G3A a;
  a.vw = r * 4096 + (((c.lane & 7) ^ ((r >> 1) & 7)) << 4);
  a.bw = c.wsp + (size_t)rt * 64 * 4096;
  return a;
}
DEV void g3_prefetch_w(const Ctx& c, const G3A& a) {
#pragma unroll
  for (int s = 0; s < 7; ++s) dma16(a.vw, a.bw + s * 128, c.lb + s * G_SLOT + c.w * 1024);
}

DEV void tlb_touch(const Ctx& c) {
  const int n = c.tid;
  const char* p = c.ws + (size_t)n * 65536;
  if (n >= 224) p = c.wsp + (size_t)(n - 224) * 65536;
  if (n >= 352) p = c.out + (size_t)(n - 352) * 65536;
  if (n >= 384) p = c.wo + (size_t)(n - 384) * 65536;
  if (n >= 392) p = c.wout + (size_t)(n - 392) * 65536;
  if (n >= 403) p = c.bsp;
  Ctx0 k = {c.lb + T_DUMMY};
  touch(k, p);
}
DEV void l2_warm(const Ctx& c, int rt, int a16, int half, int xcol) {
  Ctx0 k = {c.lb + T_DUMMY};
#pragma unroll
  for (int j = 0; j < 2; ++j) {
    int row = rt * 64 + half * 32 + 4 * c.w + 2 * j + (c.lane >> 5);
    touch(k, c.wsp + (size_t)row * 4096 + (c.lane & 31) * 128);
  }
  int n = c.w * 64 + c.lane; n = n < 300 ? n : 299;
  int row = a16 * 75 + (n >> 2);
  const char* p = row < 512 ? c.wo + (size_t)row * 1024 : c.wout + (size_t)(row - 512) * 1024;
  if (c.w < 5) touch(k, p + xcol * 512 + (n & 3) * 128);
}

struct AAddr { unsigned vx0, vx1, vh, vw; const char *bx, *bh, *bw; u64 hmask; };
template <int H, int P> DEV void a_dma(const Ctx& c, const AAddr& a, int s, unsigned slot) {
  const unsigned l = c.lb + slot;
  if (H == 0) {
    if (P == 0) dma16(a.vx0, a.bx + s * 128, l + c.w * 2048);
    if (P == 1) dma16(a.vx1, a.bx + s * 128, l + c.w * 2048 + 1024);
    if (P == 2) dma16m(a.vh, a.bh + s * 128, l + 128 * 128 + c.w * 256, a.hmask);
  } else {
    dma16(a.vw, a.bw + (size_t)s * 64 * 3072 + P * 1024, l + A_W + P * 8192 + c.w * 1024);
  }
}
template <int H> DEV void a_half(const Ctx& c, const AAddr& a, int s, unsigned slot) {
  a_dma<H, 0>(c, a, s, slot); a_dma<H, 1>(c, a, s, slot); a_dma<H, 2>(c, a, s, slot);
}

struct AAcc { f32x4 q[2][2], k[2][2], v[2][2], h, qc; };
struct AF { u32x4 x0, x1, hx, qa, qb, ka, kb, va, vb; };
struct AOfs { unsigned xo, xh, wA, wB; };

DEV void a_read(const Ctx& c, AF& F, unsigned slot, int kk, const AOfs& o) {
  LAS char* L = c.L + slot;
  LAS char* pa = L + A_W + o.wA + kk * 4096;
  LAS char* pb = L + A_W + o.wB + kk * 4096;
  F.qa = trfrag(pa, 512);
  F.x0 = lds128(L + (o.xo ^ (kk * 64)));
  F.x1 = lds128(L + (o.xo ^ (kk * 64)) + 2048);
  F.qb = trfrag(pb, 512);
  F.ka = trfrag(pa + 8192, 512); F.kb = trfrag(pb + 8192, 512);
  F.va = trfrag(pa + 16384, 512); F.vb = trfrag(pb + 16384, 512);
  F.hx = lds128(L + (o.xh ^ (kk * 64)));
  sb();
}
template <bool HV, int H, bool NEXT> DEV void a_step(const Ctx& c, AAcc& A, const AF& F, AF& N, unsigned rslot, int kk, const AOfs& o,
                                                     const AAddr& a, int sn, unsigned slot) {
  LAS char* L = c.L + rslot;
  LAS char* pa = L + A_W + o.wA + kk * 4096;
  LAS char* pb = L + A_W + o.wB + kk * 4096;
  LAS char* px = L + (o.xo ^ (kk * 64));
  sb();
  A.q[0][0] = mfmaX(F.qa, F.x0, A.q[0][0]); sb();
  if (NEXT) N.qa = trfrag(pa, 512);
  sb();
  A.q[1][0] = mfmaX(F.qa, F.x1, A.q[1][0]); sb();
  if (NEXT) { N.x0 = lds128(px); N.x1 = lds128(px + 2048); }
  sb();
  A.q[0][1] = mfmaX(F.qb, F.x0, A.q[0][1]); sb();
  if (NEXT) N.qb = trfrag(pb, 512);
  sb();
  A.q[1][1] = mfmaX(F.qb, F.x1, A.q[1][1]); sb();
  if (NEXT) N.ka = trfrag(pa + 8192, 512);
  sb();
  A.k[0][0] = mfmaX(F.ka, F.x0, A.k[0][0]); sb();
  if (NEXT) N.kb = trfrag(pb + 8192, 512);
  sb();
  A.k[1][0] = mfmaX(F.ka, F.x1, A.k[1][0]); sb();
  if (NEXT) N.va = trfrag(pa + 16384, 512);
  sb();
  A.k[0][1] = mfmaX(F.kb, F.x0, A.k[0][1]); sb();
  if (NEXT) N.vb = trfrag(pb + 16384, 512);
  sb();
  A.k[1][1] = mfmaX(F.kb, F.x1, A.k[1][1]); sb();
  if (NEXT) N.hx = lds128(L + (o.xh ^ (kk * 64)));
  sb();
  A.v[0][0] = mfmaX(F.x0, F.va, A.v[0][0]); sb();
  if (H >= 0) a_dma<(H < 0 ? 0 : H), 0>(c, a, sn, slot);
  sb();
  A.v[1][0] = mfmaX(F.x1, F.va, A.v[1][0]);
  A.v[0][1] = mfmaX(F.x0, F.vb, A.v[0][1]); sb();
  if (H >= 0) a_dma<(H < 0 ? 0 : H), 1>(c, a, sn, slot);
  sb();
  A.v[1][1] = mfmaX(F.x1, F.vb, A.v[1][1]);
  if (HV) {
    A.h = mfmaX(F.hx, F.va, A.h);
  } else {
    A.h = mfmaX(F.ka, F.hx, A.h);
  }
  sb();
  if (H >= 0) a_dma<(H < 0 ? 0 : H), 2>(c, a, sn, slot);
  sb();
  if (!HV) A.qc = mfmaX(F.qa, F.hx, A.qc);
  sb();
}

template <bool HV> DEV void a_loop(const Ctx& c, AAcc& A, const AAddr& a, const AOfs& o, u32x4& g, unsigned gofs) {
  AF F0, F1;
  wait_vm<9 + (PF & 1)>();
  bar();
  a_read(c, F0, 0, 0, o);
  unsigned s0 = 0, s1 = A_SLOT, s2 = 2 * A_SLOT;
#pragma unroll 1
  for (int s = 0; s < 5; ++s) {
    a_step<HV, 1, true>(c, A, F0, F1, s0, 1, o, a, s + 2, s2);
    wait_vm<6>();
    bar();
    STAMP1(16 + s);
    a_step<HV, 0, true>(c, A, F1, F0, s1, 0, o, a, s + 3, s0);
    unsigned t = s0; s0 = s1; s1 = s2; s2 = t;
  }
  a_step<HV, 1, true>(c, A, F0, F1, 2 * A_SLOT, 1, o, a, 7, A_SLOT);
  wait_vm<6>();
  bar();
  g = aload128c(gofs, c.ws + WS_XS);
  a_step<HV, -1, true>(c, A, F1, F0, 0, 0, o, a, 0, 0);
  a_step<HV, -1, true>(c, A, F0, F1, 0, 1, o, a, 0, 0);
  asm volatile("s_waitcnt vmcnt(0) ; TIE %0" : "+v"(g) : : "memory");
  bar();
  a_step<HV, -1, true>(c, A, F1, F0, A_SLOT, 0, o, a, 0, 0);
  a_step<HV, -1, true>(c, A, F0, F1, A_SLOT, 1, o, a, 0, 0);
  a_step<HV, -1, false>(c, A, F1, F0, 0, 0, o, a, 0, 0);
}

DEV void job_a(Ctx& c, int i, int hc, int s0row, const G3A& g3, int rt, int half, int xcol) {
  u32x4 xr; u32x2 ev;
  s0_issue(c, xr, ev, s0row);
  char* ws = c.ws;
  const int lane = c.lane, w = c.w, rg = w & 3, cg = w >> 2;
  const int t = lane & 15, q = lane >> 4;
  AAddr a;
  {
    int r0 = 16 * w + (lane >> 3);
    a.vx0 = r0 * 1024 + (((lane & 7) ^ ((r0 >> 1) & 7)) << 4);
    int r1 = r0 + 8;
    a.vx1 = r1 * 1024 + (((lane & 7) ^ ((r1 >> 1) & 7)) << 4);
    a.bx = c.x + (size_t)i * 128 * 1024;
    int rh = 2 * w + ((lane >> 3) & 1);
    a.vh = rh * 1024 + (((lane & 7) ^ w) << 4);
    a.bh = c.x + (size_t)(i > 0 ? i * 128 - 16 : 0) * 1024;
    a.hmask = w == 0 ? 0xff00ull : 0xffffull;
    int k = 8 * w + (lane >> 3);
    int f = ((k >> 1) & 1) | (((k >> 3) & 1) << 1);
    a.vw = k * 3072 + (((lane & 7) ^ (f << 1)) << 4);
    a.bw = c.wqkv + hc * 128;
  }
  if (c.tid < 24) {
    u32x4 ones = {0x3f803f80u, 0x3f803f80u, 0x3f803f80u, 0x3f803f80u};
    *(LAS u32x4*)(c.L + (c.tid >> 3) * A_SLOT + 128 * 128 + (c.tid & 7) * 16) = ones;
  }
  a_half<0>(c, a, 0, 0); a_half<1>(c, a, 0, 0);
  a_half<0>(c, a, 1, A_SLOT); a_half<1>(c, a, 1, A_SLOT);
  s0_finish<12>(c, xr, ev, s0row);
  STAMP(15);
  a_half<0>(c, a, 2, 2 * A_SLOT);
  if (PF & 1) tlb_touch(c);

  const unsigned xlane = t * 128 + ((q ^ (t >> 1)) << 4);
  const unsigned xo = rg * 32 * 128 + xlane, xh = 128 * 128 + xlane;
  const int f = ((t >> 3) & 1) | ((q & 1) << 1);
  const int hA = 2 * cg + (rg & 1), hB = hA ^ 1;
  const unsigned wl = (8 * q + (t >> 2)) * 128 + (t & 3) * 8;
  const unsigned wA = wl + ((hA ^ f) << 5), wB = wl + ((hB ^ f) << 5);
  const int gj = c.tid < 144 ? c.tid : 143;
  const int gtok = gj < 128 ? i * 128 + gj : (i > 0 ? i * 128 - 16 + (gj - 128) : gj - 128);
  const unsigned gofs = gtok * 16;

  AAcc A;
  const f32x4 z4 = {0.f, 0.f, 0.f, 0.f};
#pragma unroll
  for (int x = 0; x < 2; ++x)
#pragma unroll
    for (int y = 0; y < 2; ++y) { A.q[x][y] = z4; A.k[x][y] = z4; A.v[x][y] = z4; }
  A.h = z4; A.qc = z4;
  u32x4 g = {0, 0, 0, 0};
  const bool hv = rg >= 2;
  const AOfs ao = {xo, xh, wA, wB};
  if (hv) a_loop<true>(c, A, a, ao, g, gofs);
  else a_loop<false>(c, A, a, ao, g, gofs);
  STAMP(2);
  g = settle_pair(g, c.rs, WS_XS + gofs, true, c.Tlo);
  if (c.tid < 144) { u32x2 mr = {g.x, g.z}; *(LAS u32x2*)(c.L + T_XS + c.tid * 8) = mr; }
  if (!hv) {
    if (t == 0) {
      *(LAS f32x4*)(c.L + T_CS + (64 + hA * 16 + 4 * q) * 4) = A.h;
      *(LAS f32x4*)(c.L + T_CS + (hA * 16 + 4 * q) * 4) = A.qc;
    }
  } else if (lane < 16) {
    *(LAS float*)(c.L + T_CS + (128 + hA * 16 + lane) * 4) = A.h[0];
  }
  bar();
  g3_prefetch_w(c, g3);
  if (PF & 2) l2_warm(c, rt, (rt & 7) * 2 + half, half, xcol);

  u32x2 Q[2][2], K[2][2], V[2][2], H;
  {
    f32x4 csq[2], csk[2]; float csv[2];
#pragma unroll
    for (int y = 0; y < 2; ++y) {
      int hh = y ? hB : hA;
      csq[y] = *(LAS f32x4*)(c.L + T_CS + (hh * 16 + 4 * q) * 4);
      csk[y] = *(LAS f32x4*)(c.L + T_CS + (64 + hh * 16 + 4 * q) * 4);
      csv[y] = *(LAS float*)(c.L + T_CS + (128 + hh * 16 + t) * 4);
    }
#pragma unroll
    for (int x = 0; x < 2; ++x) {
      f32x2 mr = *(LAS f32x2*)(c.L + T_XS + (rg * 32 + x * 16 + t) * 8);
      f32x4 m01 = *(LAS f32x4*)(c.L + T_XS + (rg * 32 + x * 16 + 4 * q) * 8);
      f32x4 m23 = *(LAS f32x4*)(c.L + T_XS + (rg * 32 + x * 16 + 4 * q + 2) * 8);
      const float rq = mr.y * 0.25f;
#pragma unroll
      for (int y = 0; y < 2; ++y) {
        f32x4 aq = A.q[x][y], ak = A.k[x][y], av = A.v[x][y];
        Q[x][y].x = pk2(rq * (aq[0] - mr.x * csq[y][0]), rq * (aq[1] - mr.x * csq[y][1]));
        Q[x][y].y = pk2(rq * (aq[2] - mr.x * csq[y][2]), rq * (aq[3] - mr.x * csq[y][3]));
        K[x][y].x = pk2(mr.y * (ak[0] - mr.x * csk[y][0]), mr.y * (ak[1] - mr.x * csk[y][1]));
        K[x][y].y = pk2(mr.y * (ak[2] - mr.x * csk[y][2]), mr.y * (ak[3] - mr.x * csk[y][3]));
        V[x][y].x = pk2(m01[1] * (av[0] - m01[0] * csv[y]), m01[3] * (av[1] - m01[2] * csv[y]));
        V[x][y].y = pk2(m23[1] * (av[2] - m23[0] * csv[y]), m23[3] * (av[3] - m23[2] * csv[y]));
      }
    }
    if (!hv) {
      f32x2 mr = *(LAS f32x2*)(c.L + T_XS + (128 + t) * 8);
      H.x = pk2(mr.y * (A.h[0] - mr.x * csk[0][0]), mr.y * (A.h[1] - mr.x * csk[0][1]));
      H.y = pk2(mr.y * (A.h[2] - mr.x * csk[0][2]), mr.y * (A.h[3] - mr.x * csk[0][3]));
    } else {
      f32x4 m01 = *(LAS f32x4*)(c.L + T_XS + (128 + 4 * q) * 8);
      f32x4 m23 = *(LAS f32x4*)(c.L + T_XS + (128 + 4 * q + 2) * 8);
      H.x = pk2(m01[1] * (A.h[0] - m01[0] * csv[0]), m01[3] * (A.h[1] - m01[2] * csv[0]));
      H.y = pk2(m23[1] * (A.h[2] - m23[0] * csv[0]), m23[3] * (A.h[3] - m23[2] * csv[0]));
    }
  }
  const unsigned KX = G_Z, VX = G_SLOT + G_Z;
  {
    *(LAS u32x2*)(c.L + (hv ? VX : KX) + hA * 512 + lane * 8) = H;
    if (rg < 3) {
      *(LAS u32x2*)(c.L + KX + ((rg + 1) * 4 + hA) * 512 + lane * 8) = K[1][0];
      *(LAS u32x2*)(c.L + KX + ((rg + 1) * 4 + hB) * 512 + lane * 8) = K[1][1];
      *(LAS u32x2*)(c.L + VX + ((rg + 1) * 4 + hA) * 512 + lane * 8) = V[1][0];
      *(LAS u32x2*)(c.L + VX + ((rg + 1) * 4 + hB) * 512 + lane * 8) = V[1][1];
    }
  }
  bar();
  u32x2 Kp[2], Vp[2];
  Kp[0] = lds64(c.L + KX + (rg * 4 + hA) * 512 + lane * 8);
  Kp[1] = lds64(c.L + KX + (rg * 4 + hB) * 512 + lane * 8);
  Vp[0] = lds64(c.L + VX + (rg * 4 + hA) * 512 + lane * 8);
  Vp[1] = lds64(c.L + VX + (rg * 4 + hB) * 512 + lane * 8);

  const float NEG = -1e30f, LOG2E = 1.4426950408889634f;
  float bc[4], bp[4];
#pragma unroll
  for (int j = 0; j < 4; ++j) {
    int d = t - (4 * q + j);
    bc[j] = (d >= 0 && !(d & 1)) ? 0.f : NEG;
    int dp = d + 16;
    bp[j] = (dp <= 14 && !(dp & 1)) ? 0.f : NEG;
  }
  const bool first = (i == 0 && rg == 0);
#pragma unroll
  for (int x = 1; x >= 0; --x) {
    u32x2 O[2];
#pragma unroll
    for (int y = 0; y < 2; ++y) {
      u32x2 kp = x ? K[0][y] : Kp[y], vp = x ? V[0][y] : Vp[y];
      f32x4 sp = mfma16(kp, Q[x][y], z4), sc = mfma16(K[x][y], Q[x][y], z4);
      float e[8];
#pragma unroll
      for (int j = 0; j < 4; ++j) {
        e[j] = __builtin_fmaf(sp[j], LOG2E, (x == 0 && first) ? NEG : bp[j]);
        e[4 + j] = __builtin_fmaf(sc[j], LOG2E, bc[j]);
      }
      float mx = fmaxf(fmaxf(fmaxf(e[0], e[1]), fmaxf(e[2], e[3])), fmaxf(fmaxf(e[4], e[5]), fmaxf(e[6], e[7])));
      mx = maxq(mx);
      float sum = 0.f;
#pragma unroll
      for (int j = 0; j < 8; ++j) { e[j] = __builtin_amdgcn_exp2f(e[j] - mx); sum += e[j]; }
      float inv = __builtin_amdgcn_rcpf(sumq(sum));
      u32x4 P = {pk2(e[0] * inv, e[1] * inv), pk2(e[2] * inv, e[3] * inv), pk2(e[4] * inv, e[5] * inv), pk2(e[6] * inv, e[7] * inv)};
      u32x4 VV = {vp.x, vp.y, V[x][y].x, V[x][y].y};
      f32x4 o = mfma32(VV, P, z4);
      O[y].x = pk2(o[0], o[1]); O[y].y = pk2(o[2], o[3]);
    }
    u32x4 fr = pairswap(O[0], O[1]);
    int qp = (((q & 1) << 1) | (q >> 1)) ^ ((rg & 1) << 1);
    unsigned off = WS_O + ((i * 8 + rg * 2 + x) * 16 + 2 * hc + cg) * 1024 + qp * 256 + t * 16;
    __builtin_amdgcn_raw_buffer_store_b128(fr, c.rs, off, 0, 16);
  }
  wait_vm<0>();
  bar();
  if (c.tid == 0) gstore64(ws + WS_FO + (i * 8 + hc) * 128, tagT(c));
  STAMP(3);
}

struct ZAddr { unsigned vx0, vx1, vw; const char *bx, *bw; };
template <int S, int H, int P> DEV void z_dma(const Ctx& c, const ZAddr& a) {
  if (S >= 16 || S < 0) return;
  const unsigned l = c.lb + (S % 5) * Z_SLOT;
  const char* bw = a.bw + (size_t)(S & 7) * 64 * 2752 + (S < 8 ? 1376 : 0);
  if (S < 8) {
    if (H == 0) dma16(P ? a.vx1 : a.vx0, a.bx + S * 128, l + c.w * 2048 + P * 1024);
    else dma16m(a.vw, bw + P * 4 * 2752, l + Z_W + c.w * 1536 + P * 768, 0xffffffffffffull);
  } else if (P == 0) {
    dma16m(a.vw, bw + H * 4 * 2752, l + Z_W + c.w * 1536 + H * 768, 0xffffffffffffull);
  }
}
template <int S, int H> DEV void z_half(const Ctx& c, const ZAddr& a) { z_dma<S, H, 0>(c, a); z_dma<S, H, 1>(c, a); }
constexpr int z_n(int s) { return s >= 16 ? 0 : (s < 8 ? 4 : 2); }
constexpr int z_wait(int s) { return z_n(s + 1) + z_n(s + 2) + z_n(s + 3); }

struct ZW { u32x4 f[3]; };
template <int S, int KK> DEV void z_read_w(const Ctx& c, ZW& F, const unsigned (&wb)[3]) {
  LAS char* L = c.L + (S % 5) * Z_SLOT;
#pragma unroll
  for (int j = 0; j < 3; ++j) F.f[j] = trfrag(L + Z_W + wb[j] + KK * 6144, 768);
}
template <int S, int KK> DEV void z_read_x(const Ctx& c, u32x4 (&xf)[2][16], unsigned xo) {
  LAS char* L = c.L + (S % 5) * Z_SLOT;
  xf[0][2 * S + KK] = lds128(L + (xo ^ (KK * 64)));
  xf[1][2 * S + KK] = lds128(L + (xo ^ (KK * 64)) + 2048);
}
template <bool SWAP, int J, int RS, int RK, int SN, int H>
DEV void z_step(const Ctx& c, const ZAddr& a, u32x4 (&xf)[2][16], const ZW& F, ZW& N, f32x4 (&acc)[2][3], f32x4& cs, unsigned xo,
                const unsigned (&wb)[3]) {
  const u32x4 ones = {0x3f803f80u, 0x3f803f80u, 0x3f803f80u, 0x3f803f80u};
  constexpr int R = RS < 0 ? 0 : RS, JN = (2 * R + RK) & 15;
  LAS char* L = c.L + (R % 5) * Z_SLOT;
  auto mm = [&](int x, int j) { acc[x][j] = SWAP ? mfmaX(F.f[j], xf[x][J], acc[x][j]) : mfmaX(xf[x][J], F.f[j], acc[x][j]); };
  sb();
  mm(0, 0); sb();
  if (RS >= 0) N.f[0] = trfrag(L + Z_W + wb[0] + RK * 6144, 768);
  sb();
  mm(1, 0); sb();
  if (RS >= 0 && !SWAP) { xf[0][JN] = lds128(L + (xo ^ (RK * 64))); xf[1][JN] = lds128(L + (xo ^ (RK * 64)) + 2048); }
  if (RS >= 0 && SWAP) N.f[1] = trfrag(L + Z_W + wb[1] + RK * 6144, 768);
  sb();
  mm(0, 1); sb();
  if (RS >= 0 && !SWAP) N.f[1] = trfrag(L + Z_W + wb[1] + RK * 6144, 768);
  if (RS >= 0 && SWAP) N.f[2] = trfrag(L + Z_W + wb[2] + RK * 6144, 768);
  sb();
  mm(1, 1); sb();
  if (RS >= 0 && !SWAP) N.f[2] = trfrag(L + Z_W + wb[2] + RK * 6144, 768);
  sb();
  mm(0, 2); sb();
  z_dma<SN, H, 0>(c, a);
  sb();
  mm(1, 2); sb();
  z_dma<SN, H, 1>(c, a);
  sb();
  cs = SWAP ? mfmaX(F.f[0], ones, cs) : mfmaX(ones, F.f[0], cs);
  sb();
}
template <int S> DEV void z_stage_a(const Ctx& c, const ZAddr& a, u32x4 (&xf)[2][16], ZW& F0, ZW& F1, f32x4 (&acc)[2][3], f32x4& cs,
                                    unsigned xo, const unsigned (&wb)[3], u32x4& g, unsigned gofs) {
  z_step<false, 2 * S, S, 1, (S == 0 ? -1 : S + 4), 1>(c, a, xf, F0, F1, acc, cs, xo, wb);
  if (S == 7) asm volatile("s_waitcnt vmcnt(4) ; TIE %0" : "+v"(g) : : "memory");
  else wait_vm<z_wait(S + 1)>();
  bar();
  STAMP1(16 + S);
  if (S == 5) g = aload128c(gofs, c.ws + WS_XS);
  z_step<false, 2 * S + 1, (S < 7 ? S + 1 : -1), 0, S + 5, 0>(c, a, xf, F1, F0, acc, cs, xo, wb);
}
template <int S> DEV void z_stage_b(const Ctx& c, const ZAddr& a, u32x4 (&xf)[2][16], ZW& F0, ZW& F1, f32x4 (&acc)[2][3], f32x4& cs,
                                    const unsigned (&wb)[3], u32x4 (&g)[2], unsigned gofs, const char* gbase) {
  z_step<true, 2 * (S - 8), S, 1, S + 4, 1>(c, a, xf, F0, F1, acc, cs, 0, wb);
  if (S < 15) {
    wait_vm<z_wait(S + 1)>();
    bar();
    if (S == 14) { g[0] = aload128c(gofs, gbase); g[1] = aload128c(gofs, gbase + 2048); }
  }
  z_step<true, 2 * (S - 8) + 1, (S < 15 ? S + 1 : -1), 0, S + 5, 0>(c, a, xf, F1, F0, acc, cs, 0, wb);
  if (S == 15) asm volatile("s_waitcnt vmcnt(0) ; TIE %0 %1" : "+v"(g[0]), "+v"(g[1]) : : "memory");
}

DEV int fb_start(int c) { return c < 3 ? 6 * c : 18 + 5 * (c - 3); }

DEV void job_z(Ctx& c, int i, int zc, int s0row, const G3A& g3) {
  u32x4 xr; u32x2 ev;
  s0_issue(c, xr, ev, s0row);
  char* ws = c.ws;
  const int lane = c.lane, w = c.w, rg = w & 3, cg = w >> 2;
  const int t = lane & 15, q = lane >> 4;
  const int fb0 = fb_start(zc), nb = zc < 3 ? 6 : 5;
  ZAddr a;
  {
    int r0 = 16 * w + (lane >> 3);
    a.vx0 = r0 * 1024 + (((lane & 7) ^ ((r0 >> 1) & 7)) << 4);
    int r1 = r0 + 8;
    a.vx1 = r1 * 1024 + (((lane & 7) ^ ((r1 >> 1) & 7)) << 4);
    a.bx = c.x + (size_t)i * 128 * 1024;
    int l48 = lane < 48 ? lane : 47;
    int kr = l48 / 12, cc = l48 % 12;
    int b = (cc >> 1) ^ (w & 1);
    int fbg = fb0 + b; fbg = fbg > 42 ? 42 : fbg;
    a.vw = (8 * w + kr) * 2752 + fbg * 32 + (cc & 1) * 16;
    a.bw = c.win;
  }
  z_half<0, 0>(c, a); z_half<0, 1>(c, a); z_half<1, 0>(c, a); z_half<1, 1>(c, a); z_half<2, 0>(c, a); z_half<2, 1>(c, a);
  s0_finish<12>(c, xr, ev, s0row);
  STAMP(15);
  z_half<3, 0>(c, a); z_half<3, 1>(c, a); z_half<4, 0>(c, a); z_half<4, 1>(c, a);
  if (PF & 1) tlb_touch(c);

  const unsigned xo = rg * 32 * 128 + t * 128 + ((q ^ (t >> 1)) << 4);
  unsigned wb[3]; int lb3[3];
#pragma unroll
  for (int j = 0; j < 3; ++j) {
    int jr = j + (rg == 3 ? 0 : rg); jr = jr >= 3 ? jr - 3 : jr;
    lb3[j] = 3 * cg + jr;
    wb[j] = (8 * q + (t >> 2)) * 192 + ((lb3[j] ^ (q & 1)) << 5) + (t & 3) * 8;
  }
  const int gj = c.tid < 128 ? c.tid : 127;
  const unsigned gofs = (i * 128 + gj) * 16;
  const f32x4 z4 = {0.f, 0.f, 0.f, 0.f};
  u32x4 xf[2][16];
  f32x4 acc[2][3], cs = z4;
#pragma unroll
  for (int j = 0; j < 3; ++j) { acc[0][j] = z4; acc[1][j] = z4; }
  u32x4 g = {0, 0, 0, 0};
  ZW F0, F1;
  wait_vm<16 + (PF & 1)>();
  bar();
  z_read_x<0, 0>(c, xf, xo); z_read_w<0, 0>(c, F0, wb);
  z_stage_a<0>(c, a, xf, F0, F1, acc, cs, xo, wb, g, gofs); z_stage_a<1>(c, a, xf, F0, F1, acc, cs, xo, wb, g, gofs);
  z_stage_a<2>(c, a, xf, F0, F1, acc, cs, xo, wb, g, gofs); z_stage_a<3>(c, a, xf, F0, F1, acc, cs, xo, wb, g, gofs);
  z_stage_a<4>(c, a, xf, F0, F1, acc, cs, xo, wb, g, gofs); z_stage_a<5>(c, a, xf, F0, F1, acc, cs, xo, wb, g, gofs);
  z_stage_a<6>(c, a, xf, F0, F1, acc, cs, xo, wb, g, gofs); z_stage_a<7>(c, a, xf, F0, F1, acc, cs, xo, wb, g, gofs);
  asm volatile("" : "+v"(acc[0][0]), "+v"(acc[0][1]), "+v"(acc[0][2]), "+v"(acc[1][0]), "+v"(acc[1][1]), "+v"(acc[1][2]), "+v"(cs));
  STAMP(2);
  g = settle_pair(g, c.rs, WS_XS + gofs, true, c.Tlo);
  if (c.tid < 128) { u32x2 mr = {g.x, g.z}; *(LAS u32x2*)(c.L + T_XS + c.tid * 8) = mr; }
  if (rg < 3 && lane < 16) *(LAS float*)(c.L + T_CS + (lb3[0] * 16 + lane) * 4) = cs[0];
  bar();
  u32x2 Z2[2][3];
  {
    float csf[3];
#pragma unroll
    for (int j = 0; j < 3; ++j) csf[j] = *(LAS float*)(c.L + T_CS + (lb3[j] * 16 + t) * 4);
#pragma unroll
    for (int x = 0; x < 2; ++x) {
      f32x4 m01 = *(LAS f32x4*)(c.L + T_XS + (rg * 32 + x * 16 + 4 * q) * 8);
      f32x4 m23 = *(LAS f32x4*)(c.L + T_XS + (rg * 32 + x * 16 + 4 * q + 2) * 8);
      float s1[4] = {0.f, 0.f, 0.f, 0.f}, s2[4] = {0.f, 0.f, 0.f, 0.f};
#pragma unroll
      for (int j = 0; j < 3; ++j) {
        f32x4 v = acc[x][j];
        float y0 = gelu_t(m01[1] * (v[0] - m01[0] * csf[j])), y1 = gelu_t(m01[3] * (v[1] - m01[2] * csf[j]));
        float y2 = gelu_t(m23[1] * (v[2] - m23[0] * csf[j])), y3 = gelu_t(m23[3] * (v[3] - m23[2] * csf[j]));
        unsigned p0 = pk2(y0, y1), p1 = pk2(y2, y3);
        Z2[x][j].x = p0; Z2[x][j].y = p1;
        if (lb3[j] < nb) {
          y0 = blo(p0); y1 = bhi(p0); y2 = blo(p1); y3 = bhi(p1);
          s1[0] += y0; s1[1] += y1; s1[2] += y2; s1[3] += y3;
          s2[0] = __builtin_fmaf(y0, y0, s2[0]); s2[1] = __builtin_fmaf(y1, y1, s2[1]);
          s2[2] = __builtin_fmaf(y2, y2, s2[2]); s2[3] = __builtin_fmaf(y3, y3, s2[3]);
        }
      }
#pragma unroll
      for (int r = 0; r < 4; ++r) { s1[r] = sum16(s1[r]); s2[r] = sum16(s2[r]); }
      if (t == 0) {
        f32x4 o0 = {s1[0], s2[0], s1[1], s2[1]}, o1 = {s1[2], s2[2], s1[3], s2[3]};
        LAS char* p = c.L + T_ZP + (cg * 128 + rg * 32 + x * 16 + 4 * q) * 8;
        *(LAS f32x4*)p = o0; *(LAS f32x4*)(p + 16) = o1;
      }
    }
  }
  bar();
  if (c.tid < 256) {
    int tok = c.tid >> 1, st = c.tid & 1;
    float v = *(LAS float*)(c.L + T_ZP + tok * 8 + st * 4) + *(LAS float*)(c.L + T_ZP + (128 + tok) * 8 + st * 4);
    gstore64(ws + WS_ZS + ((i * 8 + zc) * 128 + tok) * 16 + st * 8, ((u64)c.Tlo << 32) | __float_as_uint(v));
  }
  STAMP(3);
  cs = z4;
#pragma unroll
  for (int j = 0; j < 3; ++j) { acc[0][j] = z4; acc[1][j] = z4; }
  u32x4 gs[2] = {{0, 0, 0, 0}, {0, 0, 0, 0}};
  const int stok = c.tid & 127, pg = c.tid >> 7;
  const unsigned sofs = ((2 * pg) * 128 + stok) * 16;
  const char* sbase = ws + WS_ZS + (size_t)i * 8 * 128 * 16;
  z_read_w<8, 0>(c, F0, wb);
  z_stage_b<8>(c, a, xf, F0, F1, acc, cs, wb, gs, sofs, sbase);  z_stage_b<9>(c, a, xf, F0, F1, acc, cs, wb, gs, sofs, sbase);
  z_stage_b<10>(c, a, xf, F0, F1, acc, cs, wb, gs, sofs, sbase); z_stage_b<11>(c, a, xf, F0, F1, acc, cs, wb, gs, sofs, sbase);
  z_stage_b<12>(c, a, xf, F0, F1, acc, cs, wb, gs, sofs, sbase); z_stage_b<13>(c, a, xf, F0, F1, acc, cs, wb, gs, sofs, sbase);
  z_stage_b<14>(c, a, xf, F0, F1, acc, cs, wb, gs, sofs, sbase); z_stage_b<15>(c, a, xf, F0, F1, acc, cs, wb, gs, sofs, sbase);
  asm volatile("" : "+v"(acc[0][0]), "+v"(acc[0][1]), "+v"(acc[0][2]), "+v"(acc[1][0]), "+v"(acc[1][1]), "+v"(acc[1][2]), "+v"(cs));
  STAMP(4);
  {
    const unsigned so = WS_ZS + i * 8 * 128 * 16 + sofs;
    gs[0] = settle_pair(gs[0], c.rs, so, true, c.Tlo);
    gs[1] = settle_pair(gs[1], c.rs, so + 2048, true, c.Tlo);
    f32x2 p = {__uint_as_float(gs[0].x) + __uint_as_float(gs[1].x), __uint_as_float(gs[0].z) + __uint_as_float(gs[1].z)};
    *(LAS f32x2*)(c.L + T_ZQ + (pg * 128 + stok) * 8) = p;
  }
  if (rg < 3 && t == 0) *(LAS f32x4*)(c.L + T_CS + 512 + (lb3[0] * 16 + 4 * q) * 4) = cs;
  bar();
  g3_prefetch_w(c, g3);
  if (c.tid < 128) {
    f32x2 p0 = *(LAS f32x2*)(c.L + T_ZQ + c.tid * 8), p1 = *(LAS f32x2*)(c.L + T_ZQ + (128 + c.tid) * 8);
    f32x2 p2 = *(LAS f32x2*)(c.L + T_ZQ + (256 + c.tid) * 8), p3 = *(LAS f32x2*)(c.L + T_ZQ + (384 + c.tid) * 8);
    float s = (p0.x + p1.x) + (p2.x + p3.x), ss = (p0.y + p1.y) + (p2.y + p3.y);
    float m = s * (1.0f / 688.0f);
    float var = fmaxf(ss * (1.0f / 688.0f) - m * m, 0.f);
    f32x2 o = {m, 1.0f / sqrtf(var + 1e-5f)};
    *(LAS f32x2*)(c.L + T_ZF + c.tid * 8) = o;
  }
  bar();
  {
    const int qp = ((q & 1) << 1) | (q >> 1);
    f32x4 mr[2][2];
#pragma unroll
    for (int x = 0; x < 2; ++x) {
      mr[x][0] = *(LAS f32x4*)(c.L + T_ZF + (rg * 32 + x * 16 + 4 * q) * 8);
      mr[x][1] = *(LAS f32x4*)(c.L + T_ZF + (rg * 32 + x * 16 + 4 * q + 2) * 8);
    }
#pragma unroll
    for (int j = 0; j < 3; ++j) {
      u32x2 n[2];
#pragma unroll
      for (int x = 0; x < 2; ++x) {
        unsigned p0 = Z2[x][j].x, p1 = Z2[x][j].y;
        n[x].x = pk2((blo(p0) - mr[x][0][0]) * mr[x][0][1], (bhi(p0) - mr[x][0][2]) * mr[x][0][3]);
        n[x].y = pk2((blo(p1) - mr[x][1][0]) * mr[x][1][1], (bhi(p1) - mr[x][1][2]) * mr[x][1][3]);
      }
      u32x4 fr = pairswap(n[0], n[1]);
      if (lb3[j] < nb) {
        unsigned off = WS_Z2N + ((i * 4 + rg) * 44 + fb0 + lb3[j]) * 1024 + qp * 256 + t * 16;
        __builtin_amdgcn_raw_buffer_store_b128(fr, c.rs, off, 0, 16);
      }
    }
  }
  wait_vm<0>();
  bar();
  if (c.tid == 0) gstore64(ws + WS_FZ2 + (zc * 16 + i) * 128, tagT(c));
  STAMP(5);
  {
    f32x2 mr[2];
#pragma unroll
    for (int x = 0; x < 2; ++x) mr[x] = *(LAS f32x2*)(c.L + T_XS + (rg * 32 + x * 16 + t) * 8);
#pragma unroll
    for (int j = 0; j < 3; ++j) {
      f32x4 c4 = *(LAS f32x4*)(c.L + T_CS + 512 + (lb3[j] * 16 + 4 * q) * 4);
      u32x2 n[2];
#pragma unroll
      for (int x = 0; x < 2; ++x) {
        f32x4 v = acc[x][j];
        n[x].x = pk2(gelu_t(mr[x].y * (v[0] - mr[x].x * c4[0])), gelu_t(mr[x].y * (v[1] - mr[x].x * c4[1])));
        n[x].y = pk2(gelu_t(mr[x].y * (v[2] - mr[x].x * c4[2])), gelu_t(mr[x].y * (v[3] - mr[x].x * c4[3])));
      }
      u32x4 fr = pairswap(n[0], n[1]);
      if (lb3[j] < nb) {
        int fb = fb0 + lb3[j];
        unsigned off = WS_Z1 + ((i * 8 + rg * 2 + (q & 1)) * 22 + (fb >> 1)) * 1024 + (2 * (fb & 1) + (q >> 1)) * 256 + t * 16;
        __builtin_amdgcn_raw_buffer_store_b128(fr, c.rs, off, 0, 16);
      }
    }
  }
  wait_vm<0>();
  bar();
  if (c.tid == 0) gstore64(ws + WS_FZ1 + (i * 8 + zc) * 128, tagT(c));
  STAMP(6);
}

struct HAddr { unsigned va, vw; const char *bo, *bu, *bwo, *bwout; };
template <int T> DEV void h_issue_w(const Ctx& c, const HAddr& a) {
  const unsigned l = c.lb + ((32 + T) % 7) * G_SLOT + 8192 + c.w * 1024;
  if (T < 8) dma16(a.vw, a.bwo + (size_t)T * 64 * 1024, l);
  else if (T < 18) dma16(a.vw, a.bwout + (size_t)(T - 8) * 64 * 1024, l);
  else {
    int k = 8 * c.w + (c.lane >> 3);
    unsigned v = k > 47 ? a.vw - (k - 47) * 1024 : a.vw;
    dma16(v, a.bwout + (size_t)640 * 1024, l);
  }
}
template <int T> DEV void h_issue_a(const Ctx& c, const HAddr& a) {
  const unsigned l = c.lb + ((32 + T) % 7) * G_SLOT + c.w * 1024;
  if (T < 8) dma16c(a.va, a.bo + T * 2048, l);
  else dma16c(a.va, a.bu + (T - 8) * 2048, l);
}
struct HF { u32x4 b[2], f0[2], f1[2]; };
template <int T> DEV void h_read(const Ctx& c, HF& F, unsigned ao, unsigned w0, unsigned w1) {
  LAS char* L = c.L + ((32 + T) % 7) * G_SLOT;
#pragma unroll
  for (int kk = 0; kk < 2; ++kk) {
    F.b[kk] = lds128(L + ao + kk * 4096);
    F.f0[kk] = trfrag(L + 8192 + w0 + kk * 4096, 512);
    F.f1[kk] = trfrag(L + 8192 + w1 + kk * 4096, 512);
  }
  sb();
}
template <int T, int IA, int IW> DEV void h_mma(const Ctx& c, const HAddr& h, const HF& F, f32x4 (&acc)[2]) {
  if (IA >= 0) h_issue_a<(IA < 0 ? 0 : IA)>(c, h);
  sb();
  acc[0] = mfma32(F.f0[0], F.b[0], acc[0]);
  acc[1] = mfma32(F.f1[0], F.b[0], acc[1]);
  sb();
  if (IW >= 0) h_issue_w<(IW < 0 ? 0 : IW)>(c, h);
  sb();
  u32x4 b = F.b[1];
  if (T == 18) {
    if (c.lane >= 32) { u32x4 z = {0, 0, 0, 0}; b = z; }
  }
  acc[0] = mfma32(F.f0[1], b, acc[0]);
  acc[1] = mfma32(F.f1[1], b, acc[1]);
  sb();
}
template <int T, int IA, int IW, bool NEXT> DEV void h_it(const Ctx& c, const HAddr& h, HF& Fc, HF& Fn, f32x4 (&acc)[2],
                                                         unsigned ao, unsigned w0, unsigned w1) {
  bar();
  if (NEXT) h_read<T + 1>(c, Fn, ao, w0, w1);
  h_mma<T, IA, IW>(c, h, Fc, acc);
}

template <int V> struct IC { static constexpr int v = V; };
struct GF { u32x4 b0, b1, a0, a1, a2; };

DEV void job_g(Ctx& c, int rt, int gc, const G3A& g3, bool epoch_wg) {
  char* ws = c.ws;
  const int lane = c.lane, w = c.w;
  const int t = lane & 15, q = lane >> 4;
  const int rg = w & 1, cg = (w >> 1) & 1, kh = w >> 2;
  const int fb0 = fb_start(gc), nb = gc < 3 ? 6 : 5;
  const u64 T = tagT(c);
  float bias = blo(*(const GAS u16*)(size_t)(c.bsp + ((rt * 4 + rg * 2 + kh) * 16 + t) * 2));
  if (w == 0) poll_flags(ws + WS_FZ2 + (gc * 16 + (lane & 15)) * 128, lane < 16, T);
  asm volatile("" : "+v"(bias));
  bar();
  STAMP(8);
  const char* zb = ws + WS_Z2N + (size_t)fb0 * 1024;
  const unsigned vz1 = ((w / 6) * 44 + (w % 6)) * 1024 + lane * 16;
  const int u2 = 8 + (w >> 1);
  const unsigned vz2 = (44 + (u2 - 6)) * 1024 + (w & 1) * 512 + (lane & 31) * 16;
  const unsigned lz1 = c.lb + G_Z + w * 1024, lz2 = c.lb + G_Z + u2 * 1024 + (w & 1) * 512;
#pragma unroll
  for (int s = 0; s < 7; ++s) {
    dma16c(vz1, zb + (size_t)s * 88 * 1024, lz1 + s * G_SLOT);
    dma16mc(vz2, zb + (size_t)s * 88 * 1024, lz2 + s * G_SLOT, 0xffffffffull);
  }
  HAddr h;
  {
    h.va = ((w & 3) * 16 + (w >> 2)) * 1024 + lane * 16;
    int k = 8 * w + (lane >> 3);
    int f = ((k >> 1) & 1) | (((k >> 3) & 1) << 1);
    h.vw = k * 1024 + (((lane & 7) ^ (f << 1)) << 4);
    h.bo = ws + WS_O + (size_t)rt * 4 * 16 * 1024;
    h.bu = ws + WS_U + (size_t)rt * 4 * 22 * 1024;
    h.bwo = c.wo + gc * 128;
    h.bwout = c.wout + gc * 128;
  }
  const unsigned vau = ((w & 3) * 22 + (w >> 2)) * 1024 + lane * 16;
  const unsigned bo_ = rg * 32 * 128 + t * 128 + (((4 * kh + q) ^ (t >> 1)) << 4);
  const unsigned ao_ = G_Z + (kh * 6 + 3 * cg) * 1024 + lane * 16;
  const f32x4 z4 = {0.f, 0.f, 0.f, 0.f};
  f32x4 acc[2][3];
#pragma unroll
  for (int j = 0; j < 3; ++j) { acc[0][j] = z4; acc[1][j] = z4; }
  const int tbk = rt * 4 + rg * 2 + kh;
  const unsigned flofs = lane < 8 ? WS_FO + ((rt >> 1) * 8 + lane) * 128 : WS_FZ1 + ((rt >> 1) * 8 + gc) * 128;
  unsigned z1ofs[3];
#pragma unroll
  for (int j = 0; j < 3; ++j) {
    int fb = fb0 + 3 * cg + j; fb = fb > 42 ? 42 : fb;
    z1ofs[j] = (tbk * 22 + (fb >> 1)) * 1024 + (2 * (fb & 1) + (q >> 1)) * 256 + t * 16 + (q & 1) * 8;
  }
  unsigned slot = 0;
  const char* pw = g3.bw + 7 * 128;
  const char* pz = zb + (size_t)7 * 88 * 1024;
  auto rd = [&](GF& F, unsigned sl) {
    LAS char* L = c.L + sl;
    F.a0 = lds128(L + ao_); F.b0 = lds128(L + bo_); F.b1 = lds128(L + bo_ + 2048);
    F.a1 = lds128(L + ao_ + 1024); F.a2 = lds128(L + ao_ + 2048);
    sb();
  };
  auto it = [&](auto kind, auto ht, GF& Fc, GF& Fn, bool next) {
    constexpr int KIND = decltype(kind)::v, HT = decltype(ht)::v;
    bar();
    const unsigned nsl = slot == 6 * G_SLOT ? 0 : slot + G_SLOT;
    if (KIND == 0) dma16(g3.vw, pw, c.lb + slot + w * 1024);
    if (KIND == 1) h_issue_a<HT>(c, h);
    sb();
    if (next) rd(Fn, nsl);
    acc[0][0] = mfma32(Fc.a0, Fc.b0, acc[0][0]); acc[1][0] = mfma32(Fc.a0, Fc.b1, acc[1][0]);
    acc[0][1] = mfma32(Fc.a1, Fc.b0, acc[0][1]);
    sb();
    if (KIND == 0) dma16c(vz1, pz, lz1 + slot);
    if (KIND == 1) h_issue_w<HT>(c, h);
    sb();
    acc[1][1] = mfma32(Fc.a1, Fc.b1, acc[1][1]);
    acc[0][2] = mfma32(Fc.a2, Fc.b0, acc[0][2]); acc[1][2] = mfma32(Fc.a2, Fc.b1, acc[1][2]);
    sb();
    if (KIND == 0) { dma16mc(vz2, pz, lz2 + slot, 0xffffffffull); pw += 128; pz += 88 * 1024; }
    sb();
    slot = nsl;
  };
  const IC<0> K0; const IC<1> K1; const IC<2> K2;
  GF F0, F1;
  wait_vm<12>();
  bar();
  rd(F0, 0);
  wait_vm<10>(); it(K0, K0, F0, F1, true);
  wait_vm<11>(); it(K0, K0, F1, F0, true);
  wait_vm<12>(); it(K0, K0, F0, F1, true);
  wait_vm<13>(); it(K0, K0, F1, F0, true);
  wait_vm<14>(); it(K0, K0, F0, F1, true);
  wait_vm<15>(); it(K0, K0, F1, F0, true);
#pragma unroll 1
  for (int s = 6; s < 10; s += 2) { wait_vm<15>(); it(K0, K0, F0, F1, true); wait_vm<15>(); it(K0, K0, F1, F0, true); }
  u32x2 fl = aload64c(flofs, ws);
#pragma unroll 1
  for (int s = 10; s < 16; s += 2) { wait_vm<15>(); it(K0, K0, F0, F1, true); wait_vm<15>(); it(K0, K0, F1, F0, true); }
  asm volatile("; TIE %0" : "+v"(fl));
  if (!__all(fl.x == c.Tlo && fl.y == c.Thi)) poll_flags(ws + flofs, true, T);
  u32x2 z1v[3];
  z1v[0] = aload64c(z1ofs[0], ws + WS_Z1);
  z1v[1] = aload64c(z1ofs[1], ws + WS_Z1);
  z1v[2] = aload64c(z1ofs[2], ws + WS_Z1);
#pragma unroll 1
  for (int s = 16; s < 24; s += 2) { wait_vm<15>(); it(K0, K0, F0, F1, true); wait_vm<15>(); it(K0, K0, F1, F0, true); }
  wait_vm<15>(); it(K0, K0, F0, F1, true);
  wait_vm<15>(); it(K1, IC<0>(), F1, F0, true);
  wait_vm<14>(); it(K1, IC<1>(), F0, F1, true);
  wait_vm<13>(); it(K1, IC<2>(), F1, F0, true);
  wait_vm<12>(); it(K1, IC<3>(), F0, F1, true);
  wait_vm<11>(); it(K1, IC<4>(), F1, F0, true);
  wait_vm<10>(); it(K1, IC<5>(), F0, F1, true);
  it(K2, K0, F1, F0, false);
  asm volatile("; TIE %0 %1 %2" : "+v"(z1v[0]), "+v"(z1v[1]), "+v"(z1v[2]));
  STAMP(9);
  {
    const unsigned e01 = 3 * G_SLOT + w * 2048 + lane * 16, e2 = T_EX2 + w * 1024 + lane * 16;
    const unsigned p01 = 3 * G_SLOT + (w ^ 4) * 2048 + lane * 16, p2 = T_EX2 + (w ^ 4) * 1024 + lane * 16;
    f32x4 keep[3], send[3];
#pragma unroll
    for (int j = 0; j < 3; ++j) { keep[j] = kh ? acc[1][j] : acc[0][j]; send[j] = kh ? acc[0][j] : acc[1][j]; }
    *(LAS f32x4*)(c.L + e01) = send[0]; *(LAS f32x4*)(c.L + e01 + 1024) = send[1]; *(LAS f32x4*)(c.L + e2) = send[2];
    bar();
    keep[0] += *(LAS f32x4*)(c.L + p01); keep[1] += *(LAS f32x4*)(c.L + p01 + 1024); keep[2] += *(LAS f32x4*)(c.L + p2);
#pragma unroll
    for (int j = 0; j < 3; ++j) {
      unsigned g0 = pk2(keep[j][0] + bias, keep[j][1] + bias), g1 = pk2(keep[j][2] + bias, keep[j][3] + bias);
      unsigned a0 = z1v[j].x, a1 = z1v[j].y;
      u32x2 u;
      u.x = pk2(blo(a0) * blo(g0), bhi(a0) * bhi(g0));
      u.y = pk2(blo(a1) * blo(g1), bhi(a1) * bhi(g1));
      if (3 * cg + j < nb) __builtin_amdgcn_raw_buffer_store_b64(u, c.rs, WS_U + z1ofs[j], 0, 16);
    }
  }
  wait_vm<0>();
  bar();
  if (c.tid == 0) gstore64(ws + WS_FU + (rt * 8 + gc) * 128, T);
  STAMP(10);

  const int tb = w & 3, np = w >> 2;
  const unsigned ha = tb * 1024 + lane * 16;
  const int fw = ((t >> 3) & 1) | ((q & 1) << 1);
  const unsigned wl = (8 * q + (t >> 2)) * 128 + (t & 3) * 8;
  const unsigned hw0 = wl + (((2 * np) ^ fw) << 5), hw1 = wl + (((2 * np + 1) ^ fw) << 5);
  f32x4 sa[2] = {z4, z4};
  HF H0, H1;
  h_issue_a<6>(c, h); h_issue_w<6>(c, h);
  h_read<0>(c, H0, ha, hw0, hw1);
  h_it<0, 7, 7, true>(c, h, H0, H1, sa, ha, hw0, hw1);
  h_it<1, -1, 8, true>(c, h, H1, H0, sa, ha, hw0, hw1);
  h_it<2, -1, 9, true>(c, h, H0, H1, sa, ha, hw0, hw1);
  h_it<3, -1, 10, true>(c, h, H1, H0, sa, ha, hw0, hw1);
  h_it<4, -1, 11, true>(c, h, H0, H1, sa, ha, hw0, hw1);
  wait_vm<6>(); h_it<5, -1, 12, true>(c, h, H1, H0, sa, ha, hw0, hw1);
  wait_vm<5>(); h_it<6, -1, 13, true>(c, h, H0, H1, sa, ha, hw0, hw1);
  h_it<7, -1, 14, false>(c, h, H1, H0, sa, ha, hw0, hw1);
  asm volatile("" : "+v"(sa[0]), "+v"(sa[1]));
  STAMP(11);
  h.va = vau;
  if (w == 0) poll_flags(ws + WS_FU + (rt * 8 + (lane & 7)) * 128, lane < 8, T);
  if (epoch_wg && w == 1) {
#pragma unroll
    for (int j = 0; j < 2; ++j) {
      poll_flags(ws + WS_FZ2 + (j * 64 + lane) * 128, true, T);
      poll_flags(ws + WS_FO + (j * 64 + lane) * 128, true, T);
    }
    if (lane == 0) gstore64(ws + WS_EPOCH, T);
  }
  bar();
  STAMP(12);
  h_issue_a<8>(c, h); h_issue_a<9>(c, h); h_issue_a<10>(c, h); h_issue_a<11>(c, h); h_issue_a<12>(c, h); h_issue_a<13>(c, h);
  h_issue_a<14>(c, h);
  const int orow = rt * 64 + tb * 16 + t;
  const unsigned oofs = orow * 1024 + gc * 128 + np * 64 + (q & 1) * 32 + (q >> 1) * 16;
  f32x4 ga[2] = {z4, z4};
  wait_vm<6>();
  bar();
  h_read<8>(c, H0, ha, hw0, hw1);
  wait_vm<5>(); h_it<8, 15, 15, true>(c, h, H0, H1, ga, ha, hw0, hw1);
  wait_vm<6>(); h_it<9, 16, 16, true>(c, h, H1, H0, ga, ha, hw0, hw1);
  wait_vm<7>(); h_it<10, 17, 17, true>(c, h, H0, H1, ga, ha, hw0, hw1);
  wait_vm<8>(); h_it<11, 18, 18, true>(c, h, H1, H0, ga, ha, hw0, hw1);
  wait_vm<9>(); h_it<12, -1, -1, true>(c, h, H0, H1, ga, ha, hw0, hw1);
  u32x4 xres;
  asm volatile("global_load_dwordx4 %0, %1, %2" : "=v"(xres) : "v"(oofs), "s"(uni(c.x)) : "memory");
  wait_vm<9>(); h_it<13, -1, -1, true>(c, h, H1, H0, ga, ha, hw0, hw1);
  wait_vm<7>(); h_it<14, -1, -1, true>(c, h, H0, H1, ga, ha, hw0, hw1);
  wait_vm<5>(); h_it<15, -1, -1, true>(c, h, H1, H0, ga, ha, hw0, hw1);
  wait_vm<3>(); h_it<16, -1, -1, true>(c, h, H0, H1, ga, ha, hw0, hw1);
  wait_vm<1>(); h_it<17, -1, -1, true>(c, h, H1, H0, ga, ha, hw0, hw1);
  h_mma<18, -1, -1>(c, h, H0, ga);
  asm volatile("s_waitcnt vmcnt(0) ; TIE %0" : "+v"(xres) : : "memory");
  STAMP(13);
  {
    u32x2 S[2], G[2];
#pragma unroll
    for (int b = 0; b < 2; ++b) {
      S[b].x = pk2(sa[b][0], sa[b][1]); S[b].y = pk2(sa[b][2], sa[b][3]);
      G[b].x = pk2(ga[b][0], ga[b][1]); G[b].y = pk2(ga[b][2], ga[b][3]);
    }
    u32x4 s8 = pairswap(S[0], S[1]), g8 = pairswap(G[0], G[1]);
    u32x4 o;
#pragma unroll
    for (int j = 0; j < 4; ++j) {
      unsigned su = s8[j], gu = g8[j], xu = xres[j];
      float g0 = blo(gu), g1 = bhi(gu);
      float e0 = g0 * __builtin_amdgcn_rcpf(1.0f + __builtin_amdgcn_exp2f(g0 * -1.4426950408889634f));
      float e1 = g1 * __builtin_amdgcn_rcpf(1.0f + __builtin_amdgcn_exp2f(g1 * -1.4426950408889634f));
      o[j] = pk2(__builtin_fmaf(blo(su), e0, blo(xu)), __builtin_fmaf(bhi(su), e1, bhi(xu)));
    }
    if (OUTSC == 0) *(GAS u32x4*)(size_t)(c.out + oofs) = o;
    else {
      __amdgpu_buffer_rsrc_t ro = __builtin_amdgcn_make_buffer_rsrc((void*)c.out, 0, 2048 * 1024, 0x00020000);
      __builtin_amdgcn_raw_buffer_store_b128(o, ro, oofs, 0, OUTSC == 1 ? 16 : OUTSC == 2 ? 2 : 17);
    }
  }
  STAMP(14); STAMPC(31);
}

__global__ __launch_bounds__(512, 2) void fused_kernel(const char* x, const char* wqkv, const char* wo, const char* win, const char* wsp,
                                                       const char* bsp, const char* wout, char* out, char* ws) {
  extern __shared__ __attribute__((aligned(16))) char smem[];
  Ctx c;
  c.x = x; c.wqkv = wqkv; c.wo = wo; c.win = win; c.wsp = wsp; c.bsp = bsp; c.wout = wout; c.out = out; c.ws = ws;
  c.L = (LAS char*)smem;
  c.lb = (unsigned)(size_t)c.L;
  c.tid = threadIdx.x; c.lane = c.tid & 63; c.w = __builtin_amdgcn_readfirstlane(c.tid >> 6);
  c.rs = __builtin_amdgcn_make_buffer_rsrc((void*)ws, 0, WS_USED, 0x00020000);
  c.Tlo = 0; c.Thi = 0;
  c.bid = blockIdx.x;
  STAMP(0); STAMPC(30);
  const int bid = blockIdx.x, xcd = bid & 7, sl = bid >> 3;
  const int i = (xcd & 1) * 8 + (sl >> 2), rsel = sl & 3, col = 2 * (xcd >> 1) + (rsel & 1);
  const int r16 = (xcd >> 1) * 4 + rsel;
  const int rt = (xcd >> 1) * 8 + (sl >> 2), gc = (xcd & 1) * 4 + (sl & 3);
  const int s0row = i * 128 + r16 * 8 + c.w;
  const G3A g3 = g3_addr(c, rt);
  if (rsel < 2) job_a(c, i, col, s0row, g3, rt, rsel, xcd & 1);
  else job_z(c, i, col, s0row, g3);
  STAMP(7);
  job_g(c, rt, gc, g3, bid == 0);
}

extern "C" void kernel_launch(void* const* d_in, const int* in_sizes, int n_in, void* d_out, int out_size, void* d_ws, size_t ws_size,
                              hipStream_t stream) {
  (void)in_sizes; (void)n_in; (void)out_size; (void)ws_size;
  (void)hipFuncSetAttribute((const void*)fused_kernel, hipFuncAttributeMaxDynamicSharedMemorySize, LDS_BYTES);
  fused_kernel<<<256, 512, LDS_BYTES, stream>>>((const char*)d_in[0], (const char*)d_in[1], (const char*)d_in[2], (const char*)d_in[4],
                                                (const char*)d_in[5], (const char*)d_in[6], (const char*)d_in[7], (char*)d_out, (char*)d_ws);
}
```

```cpp
#define MEAS 15
#define MEASB 0
#include <hip/hip_runtime.h>
#define EXP 0
#define PF 0
#define OUTSC 0

typedef unsigned short u16;
typedef unsigned long long u64;
typedef __bf16 bf16x8 __attribute__((ext_vector_type(8)));
typedef __bf16 bf16x2 __attribute__((ext_vector_type(2)));
typedef short s16x4 __attribute__((ext_vector_type(4)));
typedef float f32x4 __attribute__((ext_vector_type(4)));
typedef float f32x2 __attribute__((ext_vector_type(2)));
typedef unsigned u32x4 __attribute__((ext_vector_type(4)));
typedef unsigned u32x2 __attribute__((ext_vector_type(2)));
#define LAS __attribute__((address_space(3)))
#define GAS __attribute__((address_space(1)))
#define DEV __device__ __forceinline__

constexpr unsigned WS_EPOCH = 0;
constexpr unsigned WS_FZ2 = 4096;
constexpr unsigned WS_FZ1 = WS_FZ2 + 128 * 128;
constexpr unsigned WS_FO  = WS_FZ1 + 128 * 128;
constexpr unsigned WS_FU  = WS_FO + 128 * 128;
constexpr unsigned WS_XS  = 0x20000;
constexpr unsigned WS_ZS  = 0x30000;
constexpr unsigned WS_Z2N = 0x100000;
constexpr unsigned WS_Z1  = 0x400000;
constexpr unsigned WS_U   = 0x700000;
constexpr unsigned WS_O   = 0xA00000;
constexpr unsigned WS_DBG = 0xD00000;
constexpr unsigned WS_USED = 0xE00000;

constexpr unsigned LDS_BYTES = 160 * 1024;
constexpr unsigned A_SLOT = 43008, A_W = 18432;
constexpr unsigned Z_SLOT = 28672, Z_W = 16384;
constexpr unsigned G_SLOT = 20480, G_Z = 8192;
constexpr unsigned T_BASE = 143360;
constexpr unsigned T_EX2 = T_BASE;
constexpr unsigned T_XS = T_BASE + 8192;
constexpr unsigned T_CS = T_XS + 1280;
constexpr unsigned T_ZP = T_CS + 1024;
constexpr unsigned T_ZQ = T_ZP + 2048;
constexpr unsigned T_ZF = T_ZQ + 4096;
constexpr unsigned T_DUMMY = T_ZF + 1024;
static_assert(T_DUMMY + 256 <= LDS_BYTES, "lds");

constexpr unsigned SPIN_CAP = 200000;

#define STAMP(k) do { if ((k) == MEAS) c.tk = __builtin_amdgcn_s_memrealtime(); if ((k) == 0) c.t0 = __builtin_amdgcn_s_memrealtime(); } while (0)
#define STAMPC(k) do { if (MEAS == 99) { if ((k) == 30) c.c0 = __builtin_amdgcn_s_memtime(); else c.c1 = __builtin_amdgcn_s_memtime(); } } while (0)
#define STAMP1(k) do {} while (0)

DEV const char* uni(const char* p) {
  u64 v = (u64)(size_t)p;
  unsigned lo = __builtin_amdgcn_readfirstlane((unsigned)v), hi = __builtin_amdgcn_readfirstlane((unsigned)(v >> 32));
  return (const char*)(size_t)(((u64)hi << 32) | lo);
}
DEV unsigned uni(unsigned v) { return __builtin_amdgcn_readfirstlane(v); }
struct Ctx0 { unsigned sink; };
DEV void dma16(unsigned voff, const char* sbase, unsigned lds) {
  asm volatile("s_mov_b32 m0, %2\n\ts_nop 0\n\tglobal_load_lds_dwordx4 %0, %1" ::"v"(voff), "s"(uni(sbase)), "s"(uni(lds)) : "memory");
}
DEV void dma16c(unsigned voff, const char* sbase, unsigned lds) {
  asm volatile("s_mov_b32 m0, %2\n\ts_nop 0\n\tglobal_load_lds_dwordx4 %0, %1 sc1" ::"v"(voff), "s"(uni(sbase)), "s"(uni(lds)) : "memory");
}
DEV void dma16m(unsigned voff, const char* sbase, unsigned lds, u64 mask) {
  asm volatile("s_mov_b32 m0, %2\n\ts_mov_b64 exec, %3\n\tglobal_load_lds_dwordx4 %0, %1\n\ts_mov_b64 exec, -1"
               ::"v"(voff), "s"(uni(sbase)), "s"(uni(lds)), "s"(mask) : "memory");
}
DEV void dma16mc(unsigned voff, const char* sbase, unsigned lds, u64 mask) {
  asm volatile("s_mov_b32 m0, %2\n\ts_mov_b64 exec, %3\n\tglobal_load_lds_dwordx4 %0, %1 sc1\n\ts_mov_b64 exec, -1"
               ::"v"(voff), "s"(uni(sbase)), "s"(uni(lds)), "s"(mask) : "memory");
}
DEV void touch(const Ctx0& c, const char* p) {
  asm volatile("s_mov_b32 m0, %1\n\ts_nop 0\n\tglobal_load_lds_dword %0, off" ::"v"(p), "s"(uni(c.sink)) : "memory");
}
template <int N> DEV void wait_vm() { asm volatile("s_waitcnt vmcnt(%0)" ::"n"(N) : "memory"); }
DEV void sb() { __builtin_amdgcn_sched_barrier(0); }
DEV void bar() { asm volatile("s_waitcnt lgkmcnt(0)\n\ts_barrier" ::: "memory"); }

template <int V = 0> DEV f32x4 mfmaX(u32x4 a, u32x4 b, f32x4 c) {
  return __builtin_amdgcn_mfma_f32_16x16x32_bf16(__builtin_bit_cast(bf16x8, a), __builtin_bit_cast(bf16x8, b), c, 0, 0, 0);
}
DEV f32x4 mfma32(u32x4 a, u32x4 b, f32x4 c) {
  return __builtin_amdgcn_mfma_f32_16x16x32_bf16(__builtin_bit_cast(bf16x8, a), __builtin_bit_cast(bf16x8, b), c, 0, 0, 0);
}
DEV f32x4 mfma16(u32x2 a, u32x2 b, f32x4 c) {
  return __builtin_amdgcn_mfma_f32_16x16x16bf16_1k(__builtin_bit_cast(s16x4, a), __builtin_bit_cast(s16x4, b), c, 0, 0, 0);
}
DEV u32x4 lds128(LAS char* p) { return *(LAS u32x4*)p; }
DEV u32x2 lds64(LAS char* p) { return *(LAS u32x2*)p; }
DEV u32x4 trfrag(LAS char* p, int off4) {
  s16x4 a = __builtin_amdgcn_ds_read_tr16_b64_v4i16((LAS s16x4*)p);
  s16x4 b = __builtin_amdgcn_ds_read_tr16_b64_v4i16((LAS s16x4*)(p + off4));
  u32x2 ua = __builtin_bit_cast(u32x2, a), ub = __builtin_bit_cast(u32x2, b);
  u32x4 r = {ua.x, ua.y, ub.x, ub.y};
  return r;
}
DEV unsigned pk2(float a, float b) {
  f32x2 v = {a, b};
  bf16x2 h = __builtin_convertvector(v, bf16x2);
  return __builtin_bit_cast(unsigned, h);
}
DEV float blo(unsigned u) { return __uint_as_float(u << 16); }
DEV float bhi(unsigned u) { return __uint_as_float(u & 0xffff0000u); }
template <int CTRL> DEV float dpp(float v) {
  return __uint_as_float(__builtin_amdgcn_update_dpp(0u, __float_as_uint(v), CTRL, 0xf, 0xf, true));
}
DEV float sum16(float v) {
  v += dpp<0x128>(v); v += dpp<0x124>(v); v += dpp<0x122>(v); v += dpp<0x121>(v);
  return v;
}
DEV float sumq(float v) {
  unsigned u = __float_as_uint(v);
  auto a = __builtin_amdgcn_permlane16_swap(u, u, false, false);
  v = __uint_as_float(a[0]) + __uint_as_float(a[1]);
  u = __float_as_uint(v);
  auto b = __builtin_amdgcn_permlane32_swap(u, u, false, false);
  return __uint_as_float(b[0]) + __uint_as_float(b[1]);
}
DEV float maxq(float v) {
  unsigned u = __float_as_uint(v);
  auto a = __builtin_amdgcn_permlane16_swap(u, u, false, false);
  v = fmaxf(__uint_as_float(a[0]), __uint_as_float(a[1]));
  u = __float_as_uint(v);
  auto b = __builtin_amdgcn_permlane32_swap(u, u, false, false);
  return fmaxf(__uint_as_float(b[0]), __uint_as_float(b[1]));
}
DEV u32x4 pairswap(u32x2 b0, u32x2 b1) {
  auto s0 = __builtin_amdgcn_permlane16_swap(b0.x, b1.x, false, false);
  auto s1 = __builtin_amdgcn_permlane16_swap(b0.y, b1.y, false, false);
  u32x4 r = {s0[0], s1[0], s0[1], s1[1]};
  return r;
}
DEV float gelu_t(float v) {
  float e = __builtin_amdgcn_exp2f(v * __builtin_fmaf(v * v, -0.10294324f, -2.3022082f));
  return v * __builtin_amdgcn_rcpf(1.0f + e);
}
DEV u64 gload64(const char* p) { return __hip_atomic_load((GAS u64*)(size_t)p, __ATOMIC_RELAXED, __HIP_MEMORY_SCOPE_AGENT); }
DEV void gstore64(const char* p, u64 v) { __hip_atomic_store((GAS u64*)(size_t)p, v, __ATOMIC_RELAXED, __HIP_MEMORY_SCOPE_AGENT); }
DEV void poll_flags(const char* p, bool active, u64 T) {
  for (unsigned it = 0; it < SPIN_CAP; ++it) {
    bool ok = !active || gload64(p) == T;
    if (__all(ok)) break;
    __builtin_amdgcn_s_sleep(1);
  }
}
DEV u32x4 settle_pair(u32x4 g, __amdgpu_buffer_rsrc_t rs, unsigned off, bool active, unsigned T32) {
  for (unsigned it = 0; it < SPIN_CAP; ++it) {
    bool ok = !active || (g.y == T32 && g.w == T32);
    if (__all(ok)) break;
    __builtin_amdgcn_s_sleep(1);
    g = __builtin_amdgcn_raw_buffer_load_b128(rs, off, 0, 16);
  }
  return g;
}
DEV u32x4 aload128c(unsigned voff, const char* sbase) {
  u32x4 r;
  asm volatile("global_load_dwordx4 %0, %1, %2 sc1" : "=v"(r) : "v"(voff), "s"(uni(sbase)) : "memory");
  return r;
}
DEV u32x2 aload64c(unsigned voff, const char* sbase) {
  u32x2 r;
  asm volatile("global_load_dwordx2 %0, %1, %2 sc1" : "=v"(r) : "v"(voff), "s"(uni(sbase)) : "memory");
  return r;
}

struct Ctx {
  const char *x, *wqkv, *wo, *win, *wsp, *bsp, *wout;
  char *out, *ws;
  LAS char* L;
  unsigned lb;
  int tid, lane, w, bid;
  unsigned Tlo, Thi;
  __amdgpu_buffer_rsrc_t rs;
  u64 t0, tk, c0, c1;
};
DEV u64 tagT(const Ctx& c) { return ((u64)c.Thi << 32) | c.Tlo; }

DEV void s0_issue(const Ctx& c, u32x4& xr, u32x2& ev, int row) {
  ev = aload64c(0, c.ws + WS_EPOCH);
  asm volatile("global_load_dwordx4 %0, %1, %2" : "=v"(xr) : "v"(c.lane * 16), "s"(uni(c.x + (size_t)row * 1024)) : "memory");
}
template <int P> DEV void s0_finish(Ctx& c, u32x4 xr, u32x2 ev, int row) {
  asm volatile("s_waitcnt vmcnt(%2) ; TIE %0 %1" : "+v"(xr), "+v"(ev) : "n"(P) : "memory");
  u64 e = (((u64)(unsigned)__builtin_amdgcn_readfirstlane(ev.y)) << 32) | (unsigned)__builtin_amdgcn_readfirstlane(ev.x);
  e += 1;
  c.Tlo = (unsigned)e; c.Thi = (unsigned)(e >> 32);
  unsigned x0 = xr.x, x1 = xr.y, x2 = xr.z, x3 = xr.w;
  float v[8] = {blo(x0), bhi(x0), blo(x1), bhi(x1), blo(x2), bhi(x2), blo(x3), bhi(x3)};
  float s = ((v[0] + v[1]) + (v[2] + v[3])) + ((v[4] + v[5]) + (v[6] + v[7]));
  s = sumq(sum16(s));
  float m = s * (1.0f / 512.0f), q = 0.f;
#pragma unroll
  for (int j = 0; j < 8; ++j) { float d = v[j] - m; q = __builtin_fmaf(d, d, q); }
  q = sumq(sum16(q));
  float r = 1.0f / sqrtf(q * (1.0f / 512.0f) + 1e-5f);
  if (c.lane < 2)
    gstore64(c.ws + WS_XS + row * 16 + c.lane * 8, ((u64)c.Tlo << 32) | __float_as_uint(c.lane ? r : m));
}

struct G3A { unsigned vw; const char* bw; };
DEV G3A g3_addr(const Ctx& c, int rt) {
  int r = 8 * c.w + (c.lane >> 3);
  G3A a;
  a.vw = r * 4096 + (((c.lane & 7) ^ ((r >> 1) & 7)) << 4);
  a.bw = c.wsp + (size_t)rt * 64 * 4096;
  return a;
}
DEV void g3_prefetch_w(const Ctx& c, const G3A& a) {
#pragma unroll
  for (int s = 0; s < 7; ++s) dma16(a.vw, a.bw + s * 128, c.lb + s * G_SLOT + c.w * 1024);
}

DEV void tlb_touch(const Ctx& c) {
  const int n = c.tid;
  const char* p = c.ws + (size_t)n * 65536;
  if (n >= 224) p = c.wsp + (size_t)(n - 224) * 65536;
  if (n >= 352) p = c.out + (size_t)(n - 352) * 65536;
  if (n >= 384) p = c.wo + (size_t)(n - 384) * 65536;
  if (n >= 392) p = c.wout + (size_t)(n - 392) * 65536;
  if (n >= 403) p = c.bsp;
  Ctx0 k = {c.lb + T_DUMMY};
  touch(k, p);
}
DEV void l2_warm(const Ctx& c, int rt, int a16, int half, int xcol) {
  Ctx0 k = {c.lb + T_DUMMY};
#pragma unroll
  for (int j = 0; j < 2; ++j) {
    int row = rt * 64 + half * 32 + 4 * c.w + 2 * j + (c.lane >> 5);
    touch(k, c.wsp + (size_t)row * 4096 + (c.lane & 31) * 128);
  }
  int n = c.w * 64 + c.lane; n = n < 300 ? n : 299;
  int row = a16 * 75 + (n >> 2);
  const char* p = row < 512 ? c.wo + (size_t)row * 1024 : c.wout + (size_t)(row - 512) * 1024;
  if (c.w < 5) touch(k, p + xcol * 512 + (n & 3) * 128);
}

struct AAddr { unsigned vx0, vx1, vh, vw; const char *bx, *bh, *bw; u64 hmask; };
template <int H, int P> DEV void a_dma(const Ctx& c, const AAddr& a, int s, unsigned slot) {
  const unsigned l = c.lb + slot;
  if (H == 0) {
    if (P == 0) dma16(a.vx0, a.bx + s * 128, l + c.w * 2048);
    if (P == 1) dma16(a.vx1, a.bx + s * 128, l + c.w * 2048 + 1024);
    if (P == 2) dma16m(a.vh, a.bh + s * 128, l + 128 * 128 + c.w * 256, a.hmask);
  } else {
    dma16(a.vw, a.bw + (size_t)s * 64 * 3072 + P * 1024, l + A_W + P * 8192 + c.w * 1024);
  }
}
template <int H> DEV void a_half(const Ctx& c, const AAddr& a, int s, unsigned slot) {
  a_dma<H, 0>(c, a, s, slot); a_dma<H, 1>(c, a, s, slot); a_dma<H, 2>(c, a, s, slot);
}

struct AAcc { f32x4 q[2][2], k[2][2], v[2][2], h, qc; };
struct AF { u32x4 x0, x1, hx, qa, qb, ka, kb, va, vb; };
struct AOfs { unsigned xo, xh, wA, wB; };

DEV void a_read(const Ctx& c, AF& F, unsigned slot, int kk, const AOfs& o) {
  LAS char* L = c.L + slot;
  LAS char* pa = L + A_W + o.wA + kk * 4096;
  LAS char* pb = L + A_W + o.wB + kk * 4096;
  F.qa = trfrag(pa, 512);
  F.x0 = lds128(L + (o.xo ^ (kk * 64)));
  F.x1 = lds128(L + (o.xo ^ (kk * 64)) + 2048);
  F.qb = trfrag(pb, 512);
  F.ka = trfrag(pa + 8192, 512); F.kb = trfrag(pb + 8192, 512);
  F.va = trfrag(pa + 16384, 512); F.vb = trfrag(pb + 16384, 512);
  F.hx = lds128(L + (o.xh ^ (kk * 64)));
  sb();
}
template <bool HV, int H, bool NEXT> DEV void a_step(const Ctx& c, AAcc& A, const AF& F, AF& N, unsigned rslot, int kk, const AOfs& o,
                                                     const AAddr& a, int sn, unsigned slot) {
  LAS char* L = c.L + rslot;
  LAS char* pa = L + A_W + o.wA + kk * 4096;
  LAS char* pb = L + A_W + o.wB + kk * 4096;
  LAS char* px = L + (o.xo ^ (kk * 64));
  sb();
  A.q[0][0] = mfmaX(F.qa, F.x0, A.q[0][0]); sb();
  if (NEXT) N.qa = trfrag(pa, 512);
  sb();
  A.q[1][0] = mfmaX(F.qa, F.x1, A.q[1][0]); sb();
  if (NEXT) { N.x0 = lds128(px); N.x1 = lds128(px + 2048); }
  sb();
  A.q[0][1] = mfmaX(F.qb, F.x0, A.q[0][1]); sb();
  if (NEXT) N.qb = trfrag(pb, 512);
  sb();
  A.q[1][1] = mfmaX(F.qb, F.x1, A.q[1][1]); sb();
  if (NEXT) N.ka = trfrag(pa + 8192, 512);
  sb();
  A.k[0][0] = mfmaX(F.ka, F.x0, A.k[0][0]); sb();
  if (NEXT) N.kb = trfrag(pb + 8192, 512);
  sb();
  A.k[1][0] = mfmaX(F.ka, F.x1, A.k[1][0]); sb();
  if (NEXT) N.va = trfrag(pa + 16384, 512);
  sb();
  A.k[0][1] = mfmaX(F.kb, F.x0, A.k[0][1]); sb();
  if (NEXT) N.vb = trfrag(pb + 16384, 512);
  sb();
  A.k[1][1] = mfmaX(F.kb, F.x1, A.k[1][1]); sb();
  if (NEXT) N.hx = lds128(L + (o.xh ^ (kk * 64)));
  sb();
  A.v[0][0] = mfmaX(F.x0, F.va, A.v[0][0]); sb();
  if (H >= 0) a_dma<(H < 0 ? 0 : H), 0>(c, a, sn, slot);
  sb();
  A.v[1][0] = mfmaX(F.x1, F.va, A.v[1][0]);
  A.v[0][1] = mfmaX(F.x0, F.vb, A.v[0][1]); sb();
  if (H >= 0) a_dma<(H < 0 ? 0 : H), 1>(c, a, sn, slot);
  sb();
  A.v[1][1] = mfmaX(F.x1, F.vb, A.v[1][1]);
  if (HV) {
    A.h = mfmaX(F.hx, F.va, A.h);
  } else {
    A.h = mfmaX(F.ka, F.hx, A.h);
  }
  sb();
  if (H >= 0) a_dma<(H < 0 ? 0 : H), 2>(c, a, sn, slot);
  sb();
  if (!HV) A.qc = mfmaX(F.qa, F.hx, A.qc);
  sb();
}

template <bool HV> DEV void a_loop(const Ctx& c, AAcc& A, const AAddr& a, const AOfs& o, u32x4& g, unsigned gofs) {
  AF F0, F1;
  wait_vm<9 + (PF & 1)>();
  bar();
  a_read(c, F0, 0, 0, o);
  unsigned s0 = 0, s1 = A_SLOT, s2 = 2 * A_SLOT;
#pragma unroll 1
  for (int s = 0; s < 5; ++s) {
    a_step<HV, 1, true>(c, A, F0, F1, s0, 1, o, a, s + 2, s2);
    wait_vm<6>();
    bar();
    STAMP1(16 + s);
    a_step<HV, 0, true>(c, A, F1, F0, s1, 0, o, a, s + 3, s0);
    unsigned t = s0; s0 = s1; s1 = s2; s2 = t;
  }
  a_step<HV, 1, true>(c, A, F0, F1, 2 * A_SLOT, 1, o, a, 7, A_SLOT);
  wait_vm<6>();
  bar();
  g = aload128c(gofs, c.ws + WS_XS);
  a_step<HV, -1, true>(c, A, F1, F0, 0, 0, o, a, 0, 0);
  a_step<HV, -1, true>(c, A, F0, F1, 0, 1, o, a, 0, 0);
  asm volatile("s_waitcnt vmcnt(0) ; TIE %0" : "+v"(g) : : "memory");
  bar();
  a_step<HV, -1, true>(c, A, F1, F0, A_SLOT, 0, o, a, 0, 0);
  a_step<HV, -1, true>(c, A, F0, F1, A_SLOT, 1, o, a, 0, 0);
  a_step<HV, -1, false>(c, A, F1, F0, 0, 0, o, a, 0, 0);
}

DEV void job_a(Ctx& c, int i, int hc, int s0row, const G3A& g3, int rt, int half, int xcol) {
  u32x4 xr; u32x2 ev;
  s0_issue(c, xr, ev, s0row);
  char* ws = c.ws;
  const int lane = c.lane, w = c.w, rg = w & 3, cg = w >> 2;
  const int t = lane & 15, q = lane >> 4;
  AAddr a;
  {
    int r0 = 16 * w + (lane >> 3);
    a.vx0 = r0 * 1024 + (((lane & 7) ^ ((r0 >> 1) & 7)) << 4);
    int r1 = r0 + 8;
    a.vx1 = r1 * 1024 + (((lane & 7) ^ ((r1 >> 1) & 7)) << 4);
    a.bx = c.x + (size_t)i * 128 * 1024;
    int rh = 2 * w + ((lane >> 3) & 1);
    a.vh = rh * 1024 + (((lane & 7) ^ w) << 4);
    a.bh = c.x + (size_t)(i > 0 ? i * 128 - 16 : 0) * 1024;
    a.hmask = w == 0 ? 0xff00ull : 0xffffull;
    int k = 8 * w + (lane >> 3);
    int f = ((k >> 1) & 1) | (((k >> 3) & 1) << 1);
    a.vw = k * 3072 + (((lane & 7) ^ (f << 1)) << 4);
    a.bw = c.wqkv + hc * 128;
  }
  if (c.tid < 24) {
    u32x4 ones = {0x3f803f80u, 0x3f803f80u, 0x3f803f80u, 0x3f803f80u};
    *(LAS u32x4*)(c.L + (c.tid >> 3) * A_SLOT + 128 * 128 + (c.tid & 7) * 16) = ones;
  }
  a_half<0>(c, a, 0, 0); a_half<1>(c, a, 0, 0);
  a_half<0>(c, a, 1, A_SLOT); a_half<1>(c, a, 1, A_SLOT);
  s0_finish<12>(c, xr, ev, s0row);
  STAMP(15);
  a_half<0>(c, a, 2, 2 * A_SLOT);
  if (PF & 1) tlb_touch(c);

  const unsigned xlane = t * 128 + ((q ^ (t >> 1)) << 4);
  const unsigned xo = rg * 32 * 128 + xlane, xh = 128 * 128 + xlane;
  const int f = ((t >> 3) & 1) | ((q & 1) << 1);
  const int hA = 2 * cg + (rg & 1), hB = hA ^ 1;
  const unsigned wl = (8 * q + (t >> 2)) * 128 + (t & 3) * 8;
  const unsigned wA = wl + ((hA ^ f) << 5), wB = wl + ((hB ^ f) << 5);
  const int gj = c.tid < 144 ? c.tid : 143;
  const int gtok = gj < 128 ? i * 128 + gj : (i > 0 ? i * 128 - 16 + (gj - 128) : gj - 128);
  const unsigned gofs = gtok * 16;

  AAcc A;
  const f32x4 z4 = {0.f, 0.f, 0.f, 0.f};
#pragma unroll
  for (int x = 0; x < 2; ++x)
#pragma unroll
    for (int y = 0; y < 2; ++y) { A.q[x][y] = z4; A.k[x][y] = z4; A.v[x][y] = z4; }
  A.h = z4; A.qc = z4;
  u32x4 g = {0, 0, 0, 0};
  const bool hv = rg >= 2;
  const AOfs ao = {xo, xh, wA, wB};
  if (hv) a_loop<true>(c, A, a, ao, g, gofs);
  else a_loop<false>(c, A, a, ao, g, gofs);
  STAMP(2);
  g = settle_pair(g, c.rs, WS_XS + gofs, true, c.Tlo);
  if (c.tid < 144) { u32x2 mr = {g.x, g.z}; *(LAS u32x2*)(c.L + T_XS + c.tid * 8) = mr; }
  if (!hv) {
    if (t == 0) {
      *(LAS f32x4*)(c.L + T_CS + (64 + hA * 16 + 4 * q) * 4) = A.h;
      *(LAS f32x4*)(c.L + T_CS + (hA * 16 + 4 * q) * 4) = A.qc;
    }
  } else if (lane < 16) {
    *(LAS float*)(c.L + T_CS + (128 + hA * 16 + lane) * 4) = A.h[0];
  }
  bar();
  g3_prefetch_w(c, g3);
  if (PF & 2) l2_warm(c, rt, (rt & 7) * 2 + half, half, xcol);

  u32x2 Q[2][2], K[2][2], V[2][2], H;
  {
    f32x4 csq[2], csk[2]; float csv[2];
#pragma unroll
    for (int y = 0; y < 2; ++y) {
      int hh = y ? hB : hA;
      csq[y] = *(LAS f32x4*)(c.L + T_CS + (hh * 16 + 4 * q) * 4);
      csk[y] = *(LAS f32x4*)(c.L + T_CS + (64 + hh * 16 + 4 * q) * 4);
      csv[y] = *(LAS float*)(c.L + T_CS + (128 + hh * 16 + t) * 4);
    }
#pragma unroll
    for (int x = 0; x < 2; ++x) {
      f32x2 mr = *(LAS f32x2*)(c.L + T_XS + (rg * 32 + x * 16 + t) * 8);
      f32x4 m01 = *(LAS f32x4*)(c.L + T_XS + (rg * 32 + x * 16 + 4 * q) * 8);
      f32x4 m23 = *(LAS f32x4*)(c.L + T_XS + (rg * 32 + x * 16 + 4 * q + 2) * 8);
      const float rq = mr.y * 0.25f;
#pragma unroll
      for (int y = 0; y < 2; ++y) {
        f32x4 aq = A.q[x][y], ak = A.k[x][y], av = A.v[x][y];
        Q[x][y].x = pk2(rq * (aq[0] - mr.x * csq[y][0]), rq * (aq[1] - mr.x * csq[y][1]));
        Q[x][y].y = pk2(rq * (aq[2] - mr.x * csq[y][2]), rq * (aq[3] - mr.x * csq[y][3]));
        K[x][y].x = pk2(mr.y * (ak[0] - mr.x * csk[y][0]), mr.y * (ak[1] - mr.x * csk[y][1]));
        K[x][y].y = pk2(mr.y * (ak[2] - mr.x * csk[y][2]), mr.y * (ak[3] - mr.x * csk[y][3]));
        V[x][y].x = pk2(m01[1] * (av[0] - m01[0] * csv[y]), m01[3] * (av[1] - m01[2] * csv[y]));
        V[x][y].y = pk2(m23[1] * (av[2] - m23[0] * csv[y]), m23[3] * (av[3] - m23[2] * csv[y]));
      }
    }
    if (!hv) {
      f32x2 mr = *(LAS f32x2*)(c.L + T_XS + (128 + t) * 8);
      H.x = pk2(mr.y * (A.h[0] - mr.x * csk[0][0]), mr.y * (A.h[1] - mr.x * csk[0][1]));
      H.y = pk2(mr.y * (A.h[2] - mr.x * csk[0][2]), mr.y * (A.h[3] - mr.x * csk[0][3]));
    } else {
      f32x4 m01 = *(LAS f32x4*)(c.L + T_XS + (128 + 4 * q) * 8);
      f32x4 m23 = *(LAS f32x4*)(c.L + T_XS + (128 + 4 * q + 2) * 8);
      H.x = pk2(m01[1] * (A.h[0] - m01[0] * csv[0]), m01[3] * (A.h[1] - m01[2] * csv[0]));
      H.y = pk2(m23[1] * (A.h[2] - m23[0] * csv[0]), m23[3] * (A.h[3] - m23[2] * csv[0]));
    }
  }
  const unsigned KX = G_Z, VX = G_SLOT + G_Z;
  {
    *(LAS u32x2*)(c.L + (hv ? VX : KX) + hA * 512 + lane * 8) = H;
    if (rg < 3) {
      *(LAS u32x2*)(c.L + KX + ((rg + 1) * 4 + hA) * 512 + lane * 8) = K[1][0];
      *(LAS u32x2*)(c.L + KX + ((rg + 1) * 4 + hB) * 512 + lane * 8) = K[1][1];
      *(LAS u32x2*)(c.L + VX + ((rg + 1) * 4 + hA) * 512 + lane * 8) = V[1][0];
      *(LAS u32x2*)(c.L + VX + ((rg + 1) * 4 + hB) * 512 + lane * 8) = V[1][1];
    }
  }
  bar();
  u32x2 Kp[2], Vp[2];
  Kp[0] = lds64(c.L + KX + (rg * 4 + hA) * 512 + lane * 8);
  Kp[1] = lds64(c.L + KX + (rg * 4 + hB) * 512 + lane * 8);
  Vp[0] = lds64(c.L + VX + (rg * 4 + hA) * 512 + lane * 8);
  Vp[1] = lds64(c.L + VX + (rg * 4 + hB) * 512 + lane * 8);

  const float NEG = -1e30f, LOG2E = 1.4426950408889634f;
  float bc[4], bp[4];
#pragma unroll
  for (int j = 0; j < 4; ++j) {
    int d = t - (4 * q + j);
    bc[j] = (d >= 0 && !(d & 1)) ? 0.f : NEG;
    int dp = d + 16;
    bp[j] = (dp <= 14 && !(dp & 1)) ? 0.f : NEG;
  }
  const bool first = (i == 0 && rg == 0);
#pragma unroll
  for (int x = 1; x >= 0; --x) {
    u32x2 O[2];
#pragma unroll
    for (int y = 0; y < 2; ++y) {
      u32x2 kp = x ? K[0][y] : Kp[y], vp = x ? V[0][y] : Vp[y];
      f32x4 sp = mfma16(kp, Q[x][y], z4), sc = mfma16(K[x][y], Q[x][y], z4);
      float e[8];
#pragma unroll
      for (int j = 0; j < 4; ++j) {
        e[j] = __builtin_fmaf(sp[j], LOG2E, (x == 0 && first) ? NEG : bp[j]);
        e[4 + j] = __builtin_fmaf(sc[j], LOG2E, bc[j]);
      }
      float mx = fmaxf(fmaxf(fmaxf(e[0], e[1]), fmaxf(e[2], e[3])), fmaxf(fmaxf(e[4], e[5]), fmaxf(e[6], e[7])));
      mx = maxq(mx);
      float sum = 0.f;
#pragma unroll
      for (int j = 0; j < 8; ++j) { e[j] = __builtin_amdgcn_exp2f(e[j] - mx); sum += e[j]; }
      float inv = __builtin_amdgcn_rcpf(sumq(sum));
      u32x4 P = {pk2(e[0] * inv, e[1] * inv), pk2(e[2] * inv, e[3] * inv), pk2(e[4] * inv, e[5] * inv), pk2(e[6] * inv, e[7] * inv)};
      u32x4 VV = {vp.x, vp.y, V[x][y].x, V[x][y].y};
      f32x4 o = mfma32(VV, P, z4);
      O[y].x = pk2(o[0], o[1]); O[y].y = pk2(o[2], o[3]);
    }
    u32x4 fr = pairswap(O[0], O[1]);
    int qp = (((q & 1) << 1) | (q >> 1)) ^ ((rg & 1) << 1);
    unsigned off = WS_O + ((i * 8 + rg * 2 + x) * 16 + 2 * hc + cg) * 1024 + qp * 256 + t * 16;
    __builtin_amdgcn_raw_buffer_store_b128(fr, c.rs, off, 0, 16);
  }
  wait_vm<0>();
  bar();
  if (c.tid == 0) gstore64(ws + WS_FO + (i * 8 + hc) * 128, tagT(c));
  STAMP(3);
}

struct ZAddr { unsigned vx0, vx1, vw; const char *bx, *bw; };
template <int S, int H, int P> DEV void z_dma(const Ctx& c, const ZAddr& a) {
  if (S >= 16 || S < 0) return;
  const unsigned l = c.lb + (S % 5) * Z_SLOT;
  const char* bw = a.bw + (size_t)(S & 7) * 64 * 2752 + (S < 8 ? 1376 : 0);
  if (S < 8) {
    if (H == 0) dma16(P ? a.vx1 : a.vx0, a.bx + S * 128, l + c.w * 2048 + P * 1024);
    else dma16m(a.vw, bw + P * 4 * 2752, l + Z_W + c.w * 1536 + P * 768, 0xffffffffffffull);
  } else if (P == 0) {
    dma16m(a.vw, bw + H * 4 * 2752, l + Z_W + c.w * 1536 + H * 768, 0xffffffffffffull);
  }
}
template <int S, int H> DEV void z_half(const Ctx& c, const ZAddr& a) { z_dma<S, H, 0>(c, a); z_dma<S, H, 1>(c, a); }
constexpr int z_n(int s) { return s >= 16 ? 0 : (s < 8 ? 4 : 2); }
constexpr int z_wait(int s) { return z_n(s + 1) + z_n(s + 2) + z_n(s + 3); }

struct ZW { u32x4 f[3]; };
template <int S, int KK> DEV void z_read_w(const Ctx& c, ZW& F, const unsigned (&wb)[3]) {
  LAS char* L = c.L + (S % 5) * Z_SLOT;
#pragma unroll
  for (int j = 0; j < 3; ++j) F.f[j] = trfrag(L + Z_W + wb[j] + KK * 6144, 768);
}
template <int S, int KK> DEV void z_read_x(const Ctx& c, u32x4 (&xf)[2][16], unsigned xo) {
  LAS char* L = c.L + (S % 5) * Z_SLOT;
  xf[0][2 * S + KK] = lds128(L + (xo ^ (KK * 64)));
  xf[1][2 * S + KK] = lds128(L + (xo ^ (KK * 64)) + 2048);
}
template <bool SWAP, int J, int RS, int RK, int SN, int H>
DEV void z_step(const Ctx& c, const ZAddr& a, u32x4 (&xf)[2][16], const ZW& F, ZW& N, f32x4 (&acc)[2][3], f32x4& cs, unsigned xo,
                const unsigned (&wb)[3]) {
  const u32x4 ones = {0x3f803f80u, 0x3f803f80u, 0x3f803f80u, 0x3f803f80u};
  constexpr int R = RS < 0 ? 0 : RS, JN = (2 * R + RK) & 15;
  LAS char* L = c.L + (R % 5) * Z_SLOT;
  auto mm = [&](int x, int j) { acc[x][j] = SWAP ? mfmaX(F.f[j], xf[x][J], acc[x][j]) : mfmaX(xf[x][J], F.f[j], acc[x][j]); };
  sb();
  mm(0, 0); sb();
  if (RS >= 0) N.f[0] = trfrag(L + Z_W + wb[0] + RK * 6144, 768);
  sb();
  mm(1, 0); sb();
  if (RS >= 0 && !SWAP) { xf[0][JN] = lds128(L + (xo ^ (RK * 64))); xf[1][JN] = lds128(L + (xo ^ (RK * 64)) + 2048); }
  if (RS >= 0 && SWAP) N.f[1] = trfrag(L + Z_W + wb[1] + RK * 6144, 768);
  sb();
  mm(0, 1); sb();
  if (RS >= 0 && !SWAP) N.f[1] = trfrag(L + Z_W + wb[1] + RK * 6144, 768);
  if (RS >= 0 && SWAP) N.f[2] = trfrag(L + Z_W + wb[2] + RK * 6144, 768);
  sb();
  mm(1, 1); sb();
  if (RS >= 0 && !SWAP) N.f[2] = trfrag(L + Z_W + wb[2] + RK * 6144, 768);
  sb();
  mm(0, 2); sb();
  z_dma<SN, H, 0>(c, a);
  sb();
  mm(1, 2); sb();
  z_dma<SN, H, 1>(c, a);
  sb();
  cs = SWAP ? mfmaX(F.f[0], ones, cs) : mfmaX(ones, F.f[0], cs);
  sb();
}
template <int S> DEV void z_stage_a(const Ctx& c, const ZAddr& a, u32x4 (&xf)[2][16], ZW& F0, ZW& F1, f32x4 (&acc)[2][3], f32x4& cs,
                                    unsigned xo, const unsigned (&wb)[3], u32x4& g, unsigned gofs) {
  z_step<false, 2 * S, S, 1, (S == 0 ? -1 : S + 4), 1>(c, a, xf, F0, F1, acc, cs, xo, wb);
  if (S == 7) asm volatile("s_waitcnt vmcnt(4) ; TIE %0" : "+v"(g) : : "memory");
  else wait_vm<z_wait(S + 1)>();
  bar();
  STAMP1(16 + S);
  if (S == 5) g = aload128c(gofs, c.ws + WS_XS);
  z_step<false, 2 * S + 1, (S < 7 ? S + 1 : -1), 0, S + 5, 0>(c, a, xf, F1, F0, acc, cs, xo, wb);
}
template <int S> DEV void z_stage_b(const Ctx& c, const ZAddr& a, u32x4 (&xf)[2][16], ZW& F0, ZW& F1, f32x4 (&acc)[2][3], f32x4& cs,
                                    const unsigned (&wb)[3], u32x4 (&g)[2], unsigned gofs, const char* gbase) {
  z_step<true, 2 * (S - 8), S, 1, S + 4, 1>(c, a, xf, F0, F1, acc, cs, 0, wb);
  if (S < 15) {
    wait_vm<z_wait(S + 1)>();
    bar();
    if (S == 14) { g[0] = aload128c(gofs, gbase); g[1] = aload128c(gofs, gbase + 2048); }
  }
  z_step<true, 2 * (S - 8) + 1, (S < 15 ? S + 1 : -1), 0, S + 5, 0>(c, a, xf, F1, F0, acc, cs, 0, wb);
  if (S == 15) asm volatile("s_waitcnt vmcnt(0) ; TIE %0 %1" : "+v"(g[0]), "+v"(g[1]) : : "memory");
}

DEV int fb_start(int c) { return c < 3 ? 6 * c : 18 + 5 * (c - 3); }

DEV void job_z(Ctx& c, int i, int zc, int s0row, const G3A& g3) {
  u32x4 xr; u32x2 ev;
  s0_issue(c, xr, ev, s0row);
  char* ws = c.ws;
  const int lane = c.lane, w = c.w, rg = w & 3, cg = w >> 2;
  const int t = lane & 15, q = lane >> 4;
  const int fb0 = fb_start(zc), nb = zc < 3 ? 6 : 5;
  ZAddr a;
  {
    int r0 = 16 * w + (lane >> 3);
    a.vx0 = r0 * 1024 + (((lane & 7) ^ ((r0 >> 1) & 7)) << 4);
    int r1 = r0 + 8;
    a.vx1 = r1 * 1024 + (((lane & 7) ^ ((r1 >> 1) & 7)) << 4);
    a.bx = c.x + (size_t)i * 128 * 1024;
    int l48 = lane < 48 ? lane : 47;
    int kr = l48 / 12, cc = l48 % 12;
    int b = (cc >> 1) ^ (w & 1);
    int fbg = fb0 + b; fbg = fbg > 42 ? 42 : fbg;
    a.vw = (8 * w + kr) * 2752 + fbg * 32 + (cc & 1) * 16;
    a.bw = c.win;
  }
  z_half<0, 0>(c, a); z_half<0, 1>(c, a); z_half<1, 0>(c, a); z_half<1, 1>(c, a); z_half<2, 0>(c, a); z_half<2, 1>(c, a);
  s0_finish<12>(c, xr, ev, s0row);
  STAMP(15);
  z_half<3, 0>(c, a); z_half<3, 1>(c, a); z_half<4, 0>(c, a); z_half<4, 1>(c, a);
  if (PF & 1) tlb_touch(c);

  const unsigned xo = rg * 32 * 128 + t * 128 + ((q ^ (t >> 1)) << 4);
  unsigned wb[3]; int lb3[3];
#pragma unroll
  for (int j = 0; j < 3; ++j) {
    int jr = j + (rg == 3 ? 0 : rg); jr = jr >= 3 ? jr - 3 : jr;
    lb3[j] = 3 * cg + jr;
    wb[j] = (8 * q + (t >> 2)) * 192 + ((lb3[j] ^ (q & 1)) << 5) + (t & 3) * 8;
  }
  const int gj = c.tid < 128 ? c.tid : 127;
  const unsigned gofs = (i * 128 + gj) * 16;
  const f32x4 z4 = {0.f, 0.f, 0.f, 0.f};
  u32x4 xf[2][16];
  f32x4 acc[2][3], cs = z4;
#pragma unroll
  for (int j = 0; j < 3; ++j) { acc[0][j] = z4; acc[1][j] = z4; }
  u32x4 g = {0, 0, 0, 0};
  ZW F0, F1;
  wait_vm<16 + (PF & 1)>();
  bar();
  z_read_x<0, 0>(c, xf, xo); z_read_w<0, 0>(c, F0, wb);
  z_stage_a<0>(c, a, xf, F0, F1, acc, cs, xo, wb, g, gofs); z_stage_a<1>(c, a, xf, F0, F1, acc, cs, xo, wb, g, gofs);
  z_stage_a<2>(c, a, xf, F0, F1, acc, cs, xo, wb, g, gofs); z_stage_a<3>(c, a, xf, F0, F1, acc, cs, xo, wb, g, gofs);
  z_stage_a<4>(c, a, xf, F0, F1, acc, cs, xo, wb, g, gofs); z_stage_a<5>(c, a, xf, F0, F1, acc, cs, xo, wb, g, gofs);
  z_stage_a<6>(c, a, xf, F0, F1, acc, cs, xo, wb, g, gofs); z_stage_a<7>(c, a, xf, F0, F1, acc, cs, xo, wb, g, gofs);
  asm volatile("" : "+v"(acc[0][0]), "+v"(acc[0][1]), "+v"(acc[0][2]), "+v"(acc[1][0]), "+v"(acc[1][1]), "+v"(acc[1][2]), "+v"(cs));
  STAMP(2);
  g = settle_pair(g, c.rs, WS_XS + gofs, true, c.Tlo);
  if (c.tid < 128) { u32x2 mr = {g.x, g.z}; *(LAS u32x2*)(c.L + T_XS + c.tid * 8) = mr; }
  if (rg < 3 && lane < 16) *(LAS float*)(c.L + T_CS + (lb3[0] * 16 + lane) * 4) = cs[0];
  bar();
  u32x2 Z2[2][3];
  {
    float csf[3];
#pragma unroll
    for (int j = 0; j < 3; ++j) csf[j] = *(LAS float*)(c.L + T_CS + (lb3[j] * 16 + t) * 4);
#pragma unroll
    for (int x = 0; x < 2; ++x) {
      f32x4 m01 = *(LAS f32x4*)(c.L + T_XS + (rg * 32 + x * 16 + 4 * q) * 8);
      f32x4 m23 = *(LAS f32x4*)(c.L + T_XS + (rg * 32 + x * 16 + 4 * q + 2) * 8);
      float s1[4] = {0.f, 0.f, 0.f, 0.f}, s2[4] = {0.f, 0.f, 0.f, 0.f};
#pragma unroll
      for (int j = 0; j < 3; ++j) {
        f32x4 v = acc[x][j];
        float y0 = gelu_t(m01[1] * (v[0] - m01[0] * csf[j])), y1 = gelu_t(m01[3] * (v[1] - m01[2] * csf[j]));
        float y2 = gelu_t(m23[1] * (v[2] - m23[0] * csf[j])), y3 = gelu_t(m23[3] * (v[3] - m23[2] * csf[j]));
        unsigned p0 = pk2(y0, y1), p1 = pk2(y2, y3);
        Z2[x][j].x = p0; Z2[x][j].y = p1;
        if (lb3[j] < nb) {
          y0 = blo(p0); y1 = bhi(p0); y2 = blo(p1); y3 = bhi(p1);
          s1[0] += y0; s1[1] += y1; s1[2] += y2; s1[3] += y3;
          s2[0] = __builtin_fmaf(y0, y0, s2[0]); s2[1] = __builtin_fmaf(y1, y1, s2[1]);
          s2[2] = __builtin_fmaf(y2, y2, s2[2]); s2[3] = __builtin_fmaf(y3, y3, s2[3]);
        }
      }
#pragma unroll
      for (int r = 0; r < 4; ++r) { s1[r] = sum16(s1[r]); s2[r] = sum16(s2[r]); }
      if (t == 0) {
        f32x4 o0 = {s1[0], s2[0], s1[1], s2[1]}, o1 = {s1[2], s2[2], s1[3], s2[3]};
        LAS char* p = c.L + T_ZP + (cg * 128 + rg * 32 + x * 16 + 4 * q) * 8;
        *(LAS f32x4*)p = o0; *(LAS f32x4*)(p + 16) = o1;
      }
    }
  }
  bar();
  if (c.tid < 256) {
    int tok = c.tid >> 1, st = c.tid & 1;
    float v = *(LAS float*)(c.L + T_ZP + tok * 8 + st * 4) + *(LAS float*)(c.L + T_ZP + (128 + tok) * 8 + st * 4);
    gstore64(ws + WS_ZS + ((i * 8 + zc) * 128 + tok) * 16 + st * 8, ((u64)c.Tlo << 32) | __float_as_uint(v));
  }
  STAMP(3);
  cs = z4;
#pragma unroll
  for (int j = 0; j < 3; ++j) { acc[0][j] = z4; acc[1][j] = z4; }
  u32x4 gs[2] = {{0, 0, 0, 0}, {0, 0, 0, 0}};
  const int stok = c.tid & 127, pg = c.tid >> 7;
  const unsigned sofs = ((2 * pg) * 128 + stok) * 16;
  const char* sbase = ws + WS_ZS + (size_t)i * 8 * 128 * 16;
  z_read_w<8, 0>(c, F0, wb);
  z_stage_b<8>(c, a, xf, F0, F1, acc, cs, wb, gs, sofs, sbase);  z_stage_b<9>(c, a, xf, F0, F1, acc, cs, wb, gs, sofs, sbase);
  z_stage_b<10>(c, a, xf, F0, F1, acc, cs, wb, gs, sofs, sbase); z_stage_b<11>(c, a, xf, F0, F1, acc, cs, wb, gs, sofs, sbase);
  z_stage_b<12>(c, a, xf, F0, F1, acc, cs, wb, gs, sofs, sbase); z_stage_b<13>(c, a, xf, F0, F1, acc, cs, wb, gs, sofs, sbase);
  z_stage_b<14>(c, a, xf, F0, F1, acc, cs, wb, gs, sofs, sbase); z_stage_b<15>(c, a, xf, F0, F1, acc, cs, wb, gs, sofs, sbase);
  asm volatile("" : "+v"(acc[0][0]), "+v"(acc[0][1]), "+v"(acc[0][2]), "+v"(acc[1][0]), "+v"(acc[1][1]), "+v"(acc[1][2]), "+v"(cs));
  STAMP(4);
  {
    const unsigned so = WS_ZS + i * 8 * 128 * 16 + sofs;
    gs[0] = settle_pair(gs[0], c.rs, so, true, c.Tlo);
    gs[1] = settle_pair(gs[1], c.rs, so + 2048, true, c.Tlo);
    f32x2 p = {__uint_as_float(gs[0].x) + __uint_as_float(gs[1].x), __uint_as_float(gs[0].z) + __uint_as_float(gs[1].z)};
    *(LAS f32x2*)(c.L + T_ZQ + (pg * 128 + stok) * 8) = p;
  }
  if (rg < 3 && t == 0) *(LAS f32x4*)(c.L + T_CS + 512 + (lb3[0] * 16 + 4 * q) * 4) = cs;
  bar();
  g3_prefetch_w(c, g3);
  if (c.tid < 128) {
    f32x2 p0 = *(LAS f32x2*)(c.L + T_ZQ + c.tid * 8), p1 = *(LAS f32x2*)(c.L + T_ZQ + (128 + c.tid) * 8);
    f32x2 p2 = *(LAS f32x2*)(c.L + T_ZQ + (256 + c.tid) * 8), p3 = *(LAS f32x2*)(c.L + T_ZQ + (384 + c.tid) * 8);
    float s = (p0.x + p1.x) + (p2.x + p3.x), ss = (p0.y + p1.y) + (p2.y + p3.y);
    float m = s * (1.0f / 688.0f);
    float var = fmaxf(ss * (1.0f / 688.0f) - m * m, 0.f);
    f32x2 o = {m, 1.0f / sqrtf(var + 1e-5f)};
    *(LAS f32x2*)(c.L + T_ZF + c.tid * 8) = o;
  }
  bar();
  {
    const int qp = ((q & 1) << 1) | (q >> 1);
    f32x4 mr[2][2];
#pragma unroll
    for (int x = 0; x < 2; ++x) {
      mr[x][0] = *(LAS f32x4*)(c.L + T_ZF + (rg * 32 + x * 16 + 4 * q) * 8);
      mr[x][1] = *(LAS f32x4*)(c.L + T_ZF + (rg * 32 + x * 16 + 4 * q + 2) * 8);
    }
#pragma unroll
    for (int j = 0; j < 3; ++j) {
      u32x2 n[2];
#pragma unroll
      for (int x = 0; x < 2; ++x) {
        unsigned p0 = Z2[x][j].x, p1 = Z2[x][j].y;
        n[x].x = pk2((blo(p0) - mr[x][0][0]) * mr[x][0][1], (bhi(p0) - mr[x][0][2]) * mr[x][0][3]);
        n[x].y = pk2((blo(p1) - mr[x][1][0]) * mr[x][1][1], (bhi(p1) - mr[x][1][2]) * mr[x][1][3]);
      }
      u32x4 fr = pairswap(n[0], n[1]);
      if (lb3[j] < nb) {
        unsigned off = WS_Z2N + ((i * 4 + rg) * 44 + fb0 + lb3[j]) * 1024 + qp * 256 + t * 16;
        __builtin_amdgcn_raw_buffer_store_b128(fr, c.rs, off, 0, 16);
      }
    }
  }
  wait_vm<0>();
  bar();
  if (c.tid == 0) gstore64(ws + WS_FZ2 + (zc * 16 + i) * 128, tagT(c));
  STAMP(5);
  {
    f32x2 mr[2];
#pragma unroll
    for (int x = 0; x < 2; ++x) mr[x] = *(LAS f32x2*)(c.L + T_XS + (rg * 32 + x * 16 + t) * 8);
#pragma unroll
    for (int j = 0; j < 3; ++j) {
      f32x4 c4 = *(LAS f32x4*)(c.L + T_CS + 512 + (lb3[j] * 16 + 4 * q) * 4);
      u32x2 n[2];
#pragma unroll
      for (int x = 0; x < 2; ++x) {
        f32x4 v = acc[x][j];
        n[x].x = pk2(gelu_t(mr[x].y * (v[0] - mr[x].x * c4[0])), gelu_t(mr[x].y * (v[1] - mr[x].x * c4[1])));
        n[x].y = pk2(gelu_t(mr[x].y * (v[2] - mr[x].x * c4[2])), gelu_t(mr[x].y * (v[3] - mr[x].x * c4[3])));
      }
      u32x4 fr = pairswap(n[0], n[1]);
      if (lb3[j] < nb) {
        int fb = fb0 + lb3[j];
        unsigned off = WS_Z1 + ((i * 8 + rg * 2 + (q & 1)) * 22 + (fb >> 1)) * 1024 + (2 * (fb & 1) + (q >> 1)) * 256 + t * 16;
        __builtin_amdgcn_raw_buffer_store_b128(fr, c.rs, off, 0, 16);
      }
    }
  }
  wait_vm<0>();
  bar();
  if (c.tid == 0) gstore64(ws + WS_FZ1 + (i * 8 + zc) * 128, tagT(c));
  STAMP(6);
}

struct HAddr { unsigned va, vw; const char *bo, *bu, *bwo, *bwout; };
template <int T> DEV void h_issue_w(const Ctx& c, const HAddr& a) {
  const unsigned l = c.lb + ((32 + T) % 7) * G_SLOT + 8192 + c.w * 1024;
  if (T < 8) dma16(a.vw, a.bwo + (size_t)T * 64 * 1024, l);
  else if (T < 18) dma16(a.vw, a.bwout + (size_t)(T - 8) * 64 * 1024, l);
  else {
    int k = 8 * c.w + (c.lane >> 3);
    unsigned v = k > 47 ? a.vw - (k - 47) * 1024 : a.vw;
    dma16(v, a.bwout + (size_t)640 * 1024, l);
  }
}
template <int T> DEV void h_issue_a(const Ctx& c, const HAddr& a) {
  const unsigned l = c.lb + ((32 + T) % 7) * G_SLOT + c.w * 1024;
  if (T < 8) dma16c(a.va, a.bo + T * 2048, l);
  else dma16c(a.va, a.bu + (T - 8) * 2048, l);
}
struct HF { u32x4 b[2], f0[2], f1[2]; };
template <int T> DEV void h_read(const Ctx& c, HF& F, unsigned ao, unsigned w0, unsigned w1) {
  LAS char* L = c.L + ((32 + T) % 7) * G_SLOT;
#pragma unroll
  for (int kk = 0; kk < 2; ++kk) {
    F.b[kk] = lds128(L + ao + kk * 4096);
    F.f0[kk] = trfrag(L + 8192 + w0 + kk * 4096, 512);
    F.f1[kk] = trfrag(L + 8192 + w1 + kk * 4096, 512);
  }
  sb();
}
template <int T, int IA, int IW> DEV void h_mma(const Ctx& c, const HAddr& h, const HF& F, f32x4 (&acc)[2]) {
  if (IA >= 0) h_issue_a<(IA < 0 ? 0 : IA)>(c, h);
  sb();
  acc[0] = mfma32(F.f0[0], F.b[0], acc[0]);
  acc[1] = mfma32(F.f1[0], F.b[0], acc[1]);
  sb();
  if (IW >= 0) h_issue_w<(IW < 0 ? 0 : IW)>(c, h);
  sb();
  u32x4 b = F.b[1];
  if (T == 18) {
    if (c.lane >= 32) { u32x4 z = {0, 0, 0, 0}; b = z; }
  }
  acc[0] = mfma32(F.f0[1], b, acc[0]);
  acc[1] = mfma32(F.f1[1], b, acc[1]);
  sb();
}
template <int T, int IA, int IW, bool NEXT> DEV void h_it(const Ctx& c, const HAddr& h, HF& Fc, HF& Fn, f32x4 (&acc)[2],
                                                         unsigned ao, unsigned w0, unsigned w1) {
  bar();
  if (NEXT) h_read<T + 1>(c, Fn, ao, w0, w1);
  h_mma<T, IA, IW>(c, h, Fc, acc);
}

template <int V> struct IC { static constexpr int v = V; };
struct GF { u32x4 b0, b1, a0, a1, a2; };

DEV void job_g(Ctx& c, int rt, int gc, const G3A& g3, bool epoch_wg) {
  char* ws = c.ws;
  const int lane = c.lane, w = c.w;
  const int t = lane & 15, q = lane >> 4;
  const int rg = w & 1, cg = (w >> 1) & 1, kh = w >> 2;
  const int fb0 = fb_start(gc), nb = gc < 3 ? 6 : 5;
  const u64 T = tagT(c);
  float bias = blo(*(const GAS u16*)(size_t)(c.bsp + ((rt * 4 + rg * 2 + kh) * 16 + t) * 2));
  if (w == 0) poll_flags(ws + WS_FZ2 + (gc * 16 + (lane & 15)) * 128, lane < 16, T);
  asm volatile("" : "+v"(bias));
  bar();
  STAMP(8);
  const char* zb = ws + WS_Z2N + (size_t)fb0 * 1024;
  const unsigned vz1 = ((w / 6) * 44 + (w % 6)) * 1024 + lane * 16;
  const int u2 = 8 + (w >> 1);
  const unsigned vz2 = (44 + (u2 - 6)) * 1024 + (w & 1) * 512 + (lane & 31) * 16;
  const unsigned lz1 = c.lb + G_Z + w * 1024, lz2 = c.lb + G_Z + u2 * 1024 + (w & 1) * 512;
#pragma unroll
  for (int s = 0; s < 7; ++s) {
    dma16c(vz1, zb + (size_t)s * 88 * 1024, lz1 + s * G_SLOT);
    dma16mc(vz2, zb + (size_t)s * 88 * 1024, lz2 + s * G_SLOT, 0xffffffffull);
  }
  HAddr h;
  {
    h.va = ((w & 3) * 16 + (w >> 2)) * 1024 + lane * 16;
    int k = 8 * w + (lane >> 3);
    int f = ((k >> 1) & 1) | (((k >> 3) & 1) << 1);
    h.vw = k * 1024 + (((lane & 7) ^ (f << 1)) << 4);
    h.bo = ws + WS_O + (size_t)rt * 4 * 16 * 1024;
    h.bu = ws + WS_U + (size_t)rt * 4 * 22 * 1024;
    h.bwo = c.wo + gc * 128;
    h.bwout = c.wout + gc * 128;
  }
  const unsigned vau = ((w & 3) * 22 + (w >> 2)) * 1024 + lane * 16;
  const unsigned bo_ = rg * 32 * 128 + t * 128 + (((4 * kh + q) ^ (t >> 1)) << 4);
  const unsigned ao_ = G_Z + (kh * 6 + 3 * cg) * 1024 + lane * 16;
  const f32x4 z4 = {0.f, 0.f, 0.f, 0.f};
  f32x4 acc[2][3];
#pragma unroll
  for (int j = 0; j < 3; ++j) { acc[0][j] = z4; acc[1][j] = z4; }
  const int tbk = rt * 4 + rg * 2 + kh;
  const unsigned flofs = lane < 8 ? WS_FO + ((rt >> 1) * 8 + lane) * 128 : WS_FZ1 + ((rt >> 1) * 8 + gc) * 128;
  unsigned z1ofs[3];
#pragma unroll
  for (int j = 0; j < 3; ++j) {
    int fb = fb0 + 3 * cg + j; fb = fb > 42 ? 42 : fb;
    z1ofs[j] = (tbk * 22 + (fb >> 1)) * 1024 + (2 * (fb & 1) + (q >> 1)) * 256 + t * 16 + (q & 1) * 8;
  }
  unsigned slot = 0;
  const char* pw = g3.bw + 7 * 128;
  const char* pz = zb + (size_t)7 * 88 * 1024;
  auto rd = [&](GF& F, unsigned sl) {
    LAS char* L = c.L + sl;
    F.a0 = lds128(L + ao_); F.b0 = lds128(L + bo_); F.b1 = lds128(L + bo_ + 2048);
    F.a1 = lds128(L + ao_ + 1024); F.a2 = lds128(L + ao_ + 2048);
    sb();
  };
  auto it = [&](auto kind, auto ht, GF& Fc, GF& Fn, bool next) {
    constexpr int KIND = decltype(kind)::v, HT = decltype(ht)::v;
    bar();
    const unsigned nsl = slot == 6 * G_SLOT ? 0 : slot + G_SLOT;
    if (KIND == 0) dma16(g3.vw, pw, c.lb + slot + w * 1024);
    if (KIND == 1) h_issue_a<HT>(c, h);
    sb();
    if (next) rd(Fn, nsl);
    acc[0][0] = mfma32(Fc.a0, Fc.b0, acc[0][0]); acc[1][0] = mfma32(Fc.a0, Fc.b1, acc[1][0]);
    acc[0][1] = mfma32(Fc.a1, Fc.b0, acc[0][1]);
    sb();
    if (KIND == 0) dma16c(vz1, pz, lz1 + slot);
    if (KIND == 1) h_issue_w<HT>(c, h);
    sb();
    acc[1][1] = mfma32(Fc.a1, Fc.b1, acc[1][1]);
    acc[0][2] = mfma32(Fc.a2, Fc.b0, acc[0][2]); acc[1][2] = mfma32(Fc.a2, Fc.b1, acc[1][2]);
    sb();
    if (KIND == 0) { dma16mc(vz2, pz, lz2 + slot, 0xffffffffull); pw += 128; pz += 88 * 1024; }
    sb();
    slot = nsl;
  };
  const IC<0> K0; const IC<1> K1; const IC<2> K2;
  GF F0, F1;
  wait_vm<12>();
  bar();
  rd(F0, 0);
  wait_vm<10>(); it(K0, K0, F0, F1, true);
  wait_vm<11>(); it(K0, K0, F1, F0, true);
  wait_vm<12>(); it(K0, K0, F0, F1, true);
  wait_vm<13>(); it(K0, K0, F1, F0, true);
  wait_vm<14>(); it(K0, K0, F0, F1, true);
  wait_vm<15>(); it(K0, K0, F1, F0, true);
#pragma unroll 1
  for (int s = 6; s < 10; s += 2) { wait_vm<15>(); it(K0, K0, F0, F1, true); wait_vm<15>(); it(K0, K0, F1, F0, true); }
  u32x2 fl = aload64c(flofs, ws);
#pragma unroll 1
  for (int s = 10; s < 16; s += 2) { wait_vm<15>(); it(K0, K0, F0, F1, true); wait_vm<15>(); it(K0, K0, F1, F0, true); }
  asm volatile("; TIE %0" : "+v"(fl));
  if (!__all(fl.x == c.Tlo && fl.y == c.Thi)) poll_flags(ws + flofs, true, T);
  u32x2 z1v[3];
  z1v[0] = aload64c(z1ofs[0], ws + WS_Z1);
  z1v[1] = aload64c(z1ofs[1], ws + WS_Z1);
  z1v[2] = aload64c(z1ofs[2], ws + WS_Z1);
#pragma unroll 1
  for (int s = 16; s < 24; s += 2) { wait_vm<15>(); it(K0, K0, F0, F1, true); wait_vm<15>(); it(K0, K0, F1, F0, true); }
  wait_vm<15>(); it(K0, K0, F0, F1, true);
  wait_vm<15>(); it(K1, IC<0>(), F1, F0, true);
  wait_vm<14>(); it(K1, IC<1>(), F0, F1, true);
  wait_vm<13>(); it(K1, IC<2>(), F1, F0, true);
  wait_vm<12>(); it(K1, IC<3>(), F0, F1, true);
  wait_vm<11>(); it(K1, IC<4>(), F1, F0, true);
  wait_vm<10>(); it(K1, IC<5>(), F0, F1, true);
  it(K2, K0, F1, F0, false);
  asm volatile("; TIE %0 %1 %2" : "+v"(z1v[0]), "+v"(z1v[1]), "+v"(z1v[2]));
  STAMP(9);
  {
    const unsigned e01 = 3 * G_SLOT + w * 2048 + lane * 16, e2 = T_EX2 + w * 1024 + lane * 16;
    const unsigned p01 = 3 * G_SLOT + (w ^ 4) * 2048 + lane * 16, p2 = T_EX2 + (w ^ 4) * 1024 + lane * 16;
    f32x4 keep[3], send[3];
#pragma unroll
    for (int j = 0; j < 3; ++j) { keep[j] = kh ? acc[1][j] : acc[0][j]; send[j] = kh ? acc[0][j] : acc[1][j]; }
    *(LAS f32x4*)(c.L + e01) = send[0]; *(LAS f32x4*)(c.L + e01 + 1024) = send[1]; *(LAS f32x4*)(c.L + e2) = send[2];
    bar();
    keep[0] += *(LAS f32x4*)(c.L + p01); keep[1] += *(LAS f32x4*)(c.L + p01 + 1024); keep[2] += *(LAS f32x4*)(c.L + p2);
#pragma unroll
    for (int j = 0; j < 3; ++j) {
      unsigned g0 = pk2(keep[j][0] + bias, keep[j][1] + bias), g1 = pk2(keep[j][2] + bias, keep[j][3] + bias);
      unsigned a0 = z1v[j].x, a1 = z1v[j].y;
      u32x2 u;
      u.x = pk2(blo(a0) * blo(g0), bhi(a0) * bhi(g0));
      u.y = pk2(blo(a1) * blo(g1), bhi(a1) * bhi(g1));
      if (3 * cg + j < nb) __builtin_amdgcn_raw_buffer_store_b64(u, c.rs, WS_U + z1ofs[j], 0, 16);
    }
  }
  wait_vm<0>();
  bar();
  if (c.tid == 0) gstore64(ws + WS_FU + (rt * 8 + gc) * 128, T);
  STAMP(10);

  const int tb = w & 3, np = w >> 2;
  const unsigned ha = tb * 1024 + lane * 16;
  const int fw = ((t >> 3) & 1) | ((q & 1) << 1);
  const unsigned wl = (8 * q + (t >> 2)) * 128 + (t & 3) * 8;
  const unsigned hw0 = wl + (((2 * np) ^ fw) << 5), hw1 = wl + (((2 * np + 1) ^ fw) << 5);
  f32x4 sa[2] = {z4, z4};
  HF H0, H1;
  h_issue_a<6>(c, h); h_issue_w<6>(c, h);
  h_read<0>(c, H0, ha, hw0, hw1);
  h_it<0, 7, 7, true>(c, h, H0, H1, sa, ha, hw0, hw1);
  h_it<1, -1, 8, true>(c, h, H1, H0, sa, ha, hw0, hw1);
  h_it<2, -1, 9, true>(c, h, H0, H1, sa, ha, hw0, hw1);
  h_it<3, -1, 10, true>(c, h, H1, H0, sa, ha, hw0, hw1);
  h_it<4, -1, 11, true>(c, h, H0, H1, sa, ha, hw0, hw1);
  wait_vm<6>(); h_it<5, -1, 12, true>(c, h, H1, H0, sa, ha, hw0, hw1);
  wait_vm<5>(); h_it<6, -1, 13, true>(c, h, H0, H1, sa, ha, hw0, hw1);
  h_it<7, -1, 14, false>(c, h, H1, H0, sa, ha, hw0, hw1);
  asm volatile("" : "+v"(sa[0]), "+v"(sa[1]));
  STAMP(11);
  h.va = vau;
  if (w == 0) poll_flags(ws + WS_FU + (rt * 8 + (lane & 7)) * 128, lane < 8, T);
  if (epoch_wg && w == 1) {
#pragma unroll
    for (int j = 0; j < 2; ++j) {
      poll_flags(ws + WS_FZ2 + (j * 64 + lane) * 128, true, T);
      poll_flags(ws + WS_FO + (j * 64 + lane) * 128, true, T);
    }
    if (lane == 0) gstore64(ws + WS_EPOCH, T);
  }
  bar();
  STAMP(12);
  h_issue_a<8>(c, h); h_issue_a<9>(c, h); h_issue_a<10>(c, h); h_issue_a<11>(c, h); h_issue_a<12>(c, h); h_issue_a<13>(c, h);
  h_issue_a<14>(c, h);
  const int orow = rt * 64 + tb * 16 + t;
  const unsigned oofs = orow * 1024 + gc * 128 + np * 64 + (q & 1) * 32 + (q >> 1) * 16;
  f32x4 ga[2] = {z4, z4};
  wait_vm<6>();
  bar();
  h_read<8>(c, H0, ha, hw0, hw1);
  wait_vm<5>(); h_it<8, 15, 15, true>(c, h, H0, H1, ga, ha, hw0, hw1);
  wait_vm<6>(); h_it<9, 16, 16, true>(c, h, H1, H0, ga, ha, hw0, hw1);
  wait_vm<7>(); h_it<10, 17, 17, true>(c, h, H0, H1, ga, ha, hw0, hw1);
  wait_vm<8>(); h_it<11, 18, 18, true>(c, h, H1, H0, ga, ha, hw0, hw1);
  wait_vm<9>(); h_it<12, -1, -1, true>(c, h, H0, H1, ga, ha, hw0, hw1);
  u32x4 xres;
  asm volatile("global_load_dwordx4 %0, %1, %2" : "=v"(xres) : "v"(oofs), "s"(uni(c.x)) : "memory");
  wait_vm<9>(); h_it<13, -1, -1, true>(c, h, H1, H0, ga, ha, hw0, hw1);
  wait_vm<7>(); h_it<14, -1, -1, true>(c, h, H0, H1, ga, ha, hw0, hw1);
  wait_vm<5>(); h_it<15, -1, -1, true>(c, h, H1, H0, ga, ha, hw0, hw1);
  wait_vm<3>(); h_it<16, -1, -1, true>(c, h, H0, H1, ga, ha, hw0, hw1);
  wait_vm<1>(); h_it<17, -1, -1, true>(c, h, H1, H0, ga, ha, hw0, hw1);
  h_mma<18, -1, -1>(c, h, H0, ga);
  asm volatile("s_waitcnt vmcnt(0) ; TIE %0" : "+v"(xres) : : "memory");
  STAMP(13);
  {
    u32x2 S[2], G[2];
#pragma unroll
    for (int b = 0; b < 2; ++b) {
      S[b].x = pk2(sa[b][0], sa[b][1]); S[b].y = pk2(sa[b][2], sa[b][3]);
      G[b].x = pk2(ga[b][0], ga[b][1]); G[b].y = pk2(ga[b][2], ga[b][3]);
    }
    u32x4 s8 = pairswap(S[0], S[1]), g8 = pairswap(G[0], G[1]);
    u32x4 o;
#pragma unroll
    for (int j = 0; j < 4; ++j) {
      unsigned su = s8[j], gu = g8[j], xu = xres[j];
      float g0 = blo(gu), g1 = bhi(gu);
      float e0 = g0 * __builtin_amdgcn_rcpf(1.0f + __builtin_amdgcn_exp2f(g0 * -1.4426950408889634f));
      float e1 = g1 * __builtin_amdgcn_rcpf(1.0f + __builtin_amdgcn_exp2f(g1 * -1.4426950408889634f));
      o[j] = pk2(__builtin_fmaf(blo(su), e0, blo(xu)), __builtin_fmaf(bhi(su), e1, bhi(xu)));
    }
    if (OUTSC == 0) *(GAS u32x4*)(size_t)(c.out + oofs) = o;
    else {
      __amdgpu_buffer_rsrc_t ro = __builtin_amdgcn_make_buffer_rsrc((void*)c.out, 0, 2048 * 1024, 0x00020000);
      __builtin_amdgcn_raw_buffer_store_b128(o, ro, oofs, 0, OUTSC == 1 ? 16 : OUTSC == 2 ? 2 : 17);
    }
  }
  STAMP(14); STAMPC(31);
}

__global__ __launch_bounds__(512, 2) void fused_kernel(const char* x, const char* wqkv, const char* wo, const char* win, const char* wsp,
                                                       const char* bsp, const char* wout, char* out, char* ws) {
  extern __shared__ __attribute__((aligned(16))) char smem[];
  Ctx c;
  c.x = x; c.wqkv = wqkv; c.wo = wo; c.win = win; c.wsp = wsp; c.bsp = bsp; c.wout = wout; c.out = out; c.ws = ws;
  c.L = (LAS char*)smem;
  c.lb = (unsigned)(size_t)c.L;
  c.tid = threadIdx.x; c.lane = c.tid & 63; c.w = __builtin_amdgcn_readfirstlane(c.tid >> 6);
  c.rs = __builtin_amdgcn_make_buffer_rsrc((void*)ws, 0, WS_USED, 0x00020000);
  c.Tlo = 0; c.Thi = 0;
  c.bid = blockIdx.x;
  STAMP(0); STAMPC(30);
  const int bid = blockIdx.x, xcd = bid & 7, sl = bid >> 3;
  const int i = (xcd & 1) * 8 + (sl >> 2), rsel = sl & 3, col = 2 * (xcd >> 1) + (rsel & 1);
  const int r16 = (xcd >> 1) * 4 + rsel;
  const int rt = (xcd >> 1) * 8 + (sl >> 2), gc = (xcd & 1) * 4 + (sl & 3);
  const int s0row = i * 128 + r16 * 8 + c.w;
  const G3A g3 = g3_addr(c, rt);
  if (rsel < 2) job_a(c, i, col, s0row, g3, rt, rsel, xcd & 1);
  else job_z(c, i, col, s0row, g3);
  STAMP(7);
  job_g(c, rt, gc, g3, bid == 0);
  if (bid == MEASB && c.w == 0) {
    u64 d = MEAS == 99 ? (c.c1 - c.c0) / 10 : 8 * (c.tk - c.t0);
    u64 target = __builtin_amdgcn_s_memrealtime() + (d < 100000 ? d : 100000);
    for (int n = 0; n < 1000000; ++n) { if (__builtin_amdgcn_s_memrealtime() >= target) break; __builtin_amdgcn_s_sleep(4); }
  }
}

extern "C" void kernel_launch(void* const* d_in, const int* in_sizes, int n_in, void* d_out, int out_size, void* d_ws, size_t ws_size,
                              hipStream_t stream) {
  (void)in_sizes; (void)n_in; (void)out_size; (void)ws_size;
  (void)hipFuncSetAttribute((const void*)fused_kernel, hipFuncAttributeMaxDynamicSharedMemorySize, LDS_BYTES);
  fused_kernel<<<256, 512, LDS_BYTES, stream>>>((const char*)d_in[0], (const char*)d_in[1], (const char*)d_in[2], (const char*)d_in[4],
                                                (const char*)d_in[5], (const char*)d_in[6], (const char*)d_in[7], (char*)d_out, (char*)d_ws);
}
```

```cpp
#define MEAS2 0
#include <hip/hip_runtime.h>
#define EXP 0
#define PF 0
#define OUTSC 0

typedef unsigned short u16;
typedef unsigned long long u64;
typedef __bf16 bf16x8 __attribute__((ext_vector_type(8)));
typedef __bf16 bf16x2 __attribute__((ext_vector_type(2)));
typedef short s16x4 __attribute__((ext_vector_type(4)));
typedef float f32x4 __attribute__((ext_vector_type(4)));
typedef float f32x2 __attribute__((ext_vector_type(2)));
typedef unsigned u32x4 __attribute__((ext_vector_type(4)));
typedef unsigned u32x2 __attribute__((ext_vector_type(2)));
#define LAS __attribute__((address_space(3)))
#define GAS __attribute__((address_space(1)))
#define DEV __device__ __forceinline__

constexpr unsigned WS_EPOCH = 0;
constexpr unsigned WS_FZ2 = 4096;
constexpr unsigned WS_FZ1 = WS_FZ2 + 128 * 128;
constexpr unsigned WS_FO  = WS_FZ1 + 128 * 128;
constexpr unsigned WS_FU  = WS_FO + 128 * 128;
constexpr unsigned WS_XS  = 0x20000;
constexpr unsigned WS_ZS  = 0x30000;
constexpr unsigned WS_Z2N = 0x100000;
constexpr unsigned WS_Z1  = 0x400000;
constexpr unsigned WS_U   = 0x700000;
constexpr unsigned WS_O   = 0xA00000;
constexpr unsigned WS_DBG = 0xD00000;
constexpr unsigned WS_USED = 0xE00000;

constexpr unsigned LDS_BYTES = 160 * 1024;
constexpr unsigned A_SLOT = 43008, A_W = 18432;
constexpr unsigned Z_SLOT = 28672, Z_W = 16384;
constexpr unsigned G_SLOT = 20480, G_Z = 8192;
constexpr unsigned T_BASE = 143360;
constexpr unsigned T_EX2 = T_BASE;
constexpr unsigned T_XS = T_BASE + 8192;
constexpr unsigned T_CS = T_XS + 1280;
constexpr unsigned T_ZP = T_CS + 1024;
constexpr unsigned T_ZQ = T_ZP + 2048;
constexpr unsigned T_ZF = T_ZQ + 4096;
constexpr unsigned T_DUMMY = T_ZF + 1024;
static_assert(T_DUMMY + 256 <= LDS_BYTES, "lds");

constexpr unsigned SPIN_CAP = 200000;

#define STAMP(k) do { if ((k) == 14) c.tk = __builtin_amdgcn_s_memrealtime(); if ((k) == 0) c.t0 = __builtin_amdgcn_s_memrealtime(); } while (0)
#define STAMPC(k) do {} while (0)
#define STAMP1(k) do {} while (0)

DEV const char* uni(const char* p) {
  u64 v = (u64)(size_t)p;
  unsigned lo = __builtin_amdgcn_readfirstlane((unsigned)v), hi = __builtin_amdgcn_readfirstlane((unsigned)(v >> 32));
  return (const char*)(size_t)(((u64)hi << 32) | lo);
}
DEV unsigned uni(unsigned v) { return __builtin_amdgcn_readfirstlane(v); }
struct Ctx0 { unsigned sink; };
DEV void dma16(unsigned voff, const char* sbase, unsigned lds) {
  asm volatile("s_mov_b32 m0, %2\n\ts_nop 0\n\tglobal_load_lds_dwordx4 %0, %1" ::"v"(voff), "s"(uni(sbase)), "s"(uni(lds)) : "memory");
}
DEV void dma16c(unsigned voff, const char* sbase, unsigned lds) {
  asm volatile("s_mov_b32 m0, %2\n\ts_nop 0\n\tglobal_load_lds_dwordx4 %0, %1 sc1" ::"v"(voff), "s"(uni(sbase)), "s"(uni(lds)) : "memory");
}
DEV void dma16m(unsigned voff, const char* sbase, unsigned lds, u64 mask) {
  asm volatile("s_mov_b32 m0, %2\n\ts_mov_b64 exec, %3\n\tglobal_load_lds_dwordx4 %0, %1\n\ts_mov_b64 exec, -1"
               ::"v"(voff), "s"(uni(sbase)), "s"(uni(lds)), "s"(mask) : "memory");
}
DEV void dma16mc(unsigned voff, const char* sbase, unsigned lds, u64 mask) {
  asm volatile("s_mov_b32 m0, %2\n\ts_mov_b64 exec, %3\n\tglobal_load_lds_dwordx4 %0, %1 sc1\n\ts_mov_b64 exec, -1"
               ::"v"(voff), "s"(uni(sbase)), "s"(uni(lds)), "s"(mask) : "memory");
}
DEV void touch(const Ctx0& c, const char* p) {
  asm volatile("s_mov_b32 m0, %1\n\ts_nop 0\n\tglobal_load_lds_dword %0, off" ::"v"(p), "s"(uni(c.sink)) : "memory");
}
template <int N> DEV void wait_vm() { asm volatile("s_waitcnt vmcnt(%0)" ::"n"(N) : "memory"); }
DEV void sb() { __builtin_amdgcn_sched_barrier(0); }
DEV void bar() { asm volatile("s_waitcnt lgkmcnt(0)\n\ts_barrier" ::: "memory"); }

template <int V = 0> DEV f32x4 mfmaX(u32x4 a, u32x4 b, f32x4 c) {
  return __builtin_amdgcn_mfma_f32_16x16x32_bf16(__builtin_bit_cast(bf16x8, a), __builtin_bit_cast(bf16x8, b), c, 0, 0, 0);
}
DEV f32x4 mfma32(u32x4 a, u32x4 b, f32x4 c) {
  return __builtin_amdgcn_mfma_f32_16x16x32_bf16(__builtin_bit_cast(bf16x8, a), __builtin_bit_cast(bf16x8, b), c, 0, 0, 0);
}
DEV f32x4 mfma16(u32x2 a, u32x2 b, f32x4 c) {
  return __builtin_amdgcn_mfma_f32_16x16x16bf16_1k(__builtin_bit_cast(s16x4, a), __builtin_bit_cast(s16x4, b), c, 0, 0, 0);
}
DEV u32x4 lds128(LAS char* p) { return *(LAS u32x4*)p; }
DEV u32x2 lds64(LAS char* p) { return *(LAS u32x2*)p; }
DEV u32x4 trfrag(LAS char* p, int off4) {
  s16x4 a = __builtin_amdgcn_ds_read_tr16_b64_v4i16((LAS s16x4*)p);
  s16x4 b = __builtin_amdgcn_ds_read_tr16_b64_v4i16((LAS s16x4*)(p + off4));
  u32x2 ua = __builtin_bit_cast(u32x2, a), ub = __builtin_bit_cast(u32x2, b);
  u32x4 r = {ua.x, ua.y, ub.x, ub.y};
  return r;
}
DEV unsigned pk2(float a, float b) {
  f32x2 v = {a, b};
  bf16x2 h = __builtin_convertvector(v, bf16x2);
  return __builtin_bit_cast(unsigned, h);
}
DEV float blo(unsigned u) { return __uint_as_float(u << 16); }
DEV float bhi(unsigned u) { return __uint_as_float(u & 0xffff0000u); }
template <int CTRL> DEV float dpp(float v) {
  return __uint_as_float(__builtin_amdgcn_update_dpp(0u, __float_as_uint(v), CTRL, 0xf, 0xf, true));
}
DEV float sum16(float v) {
  v += dpp<0x128>(v); v += dpp<0x124>(v); v += dpp<0x122>(v); v += dpp<0x121>(v);
  return v;
}
DEV float sumq(float v) {
  unsigned u = __float_as_uint(v);
  auto a = __builtin_amdgcn_permlane16_swap(u, u, false, false);
  v = __uint_as_float(a[0]) + __uint_as_float(a[1]);
  u = __float_as_uint(v);
  auto b = __builtin_amdgcn_permlane32_swap(u, u, false, false);
  return __uint_as_float(b[0]) + __uint_as_float(b[1]);
}
DEV float maxq(float v) {
  unsigned u = __float_as_uint(v);
  auto a = __builtin_amdgcn_permlane16_swap(u, u, false, false);
  v = fmaxf(__uint_as_float(a[0]), __uint_as_float(a[1]));
  u = __float_as_uint(v);
  auto b = __builtin_amdgcn_permlane32_swap(u, u, false, false);
  return fmaxf(__uint_as_float(b[0]), __uint_as_float(b[1]));
}
DEV u32x4 pairswap(u32x2 b0, u32x2 b1) {
  auto s0 = __builtin_amdgcn_permlane16_swap(b0.x, b1.x, false, false);
  auto s1 = __builtin_amdgcn_permlane16_swap(b0.y, b1.y, false, false);
  u32x4 r = {s0[0], s1[0], s0[1], s1[1]};
  return r;
}
DEV float gelu_t(float v) {
  float e = __builtin_amdgcn_exp2f(v * __builtin_fmaf(v * v, -0.10294324f, -2.3022082f));
  return v * __builtin_amdgcn_rcpf(1.0f + e);
}
DEV u64 gload64(const char* p) { return __hip_atomic_load((GAS u64*)(size_t)p, __ATOMIC_RELAXED, __HIP_MEMORY_SCOPE_AGENT); }
DEV void gstore64(const char* p, u64 v) { __hip_atomic_store((GAS u64*)(size_t)p, v, __ATOMIC_RELAXED, __HIP_MEMORY_SCOPE_AGENT); }
DEV void poll_flags(const char* p, bool active, u64 T) {
  for (unsigned it = 0; it < SPIN_CAP; ++it) {
    bool ok = !active || gload64(p) == T;
    if (__all(ok)) break;
    __builtin_amdgcn_s_sleep(1);
  }
}
DEV u32x4 settle_pair(u32x4 g, __amdgpu_buffer_rsrc_t rs, unsigned off, bool active, unsigned T32) {
  for (unsigned it = 0; it < SPIN_CAP; ++it) {
    bool ok = !active || (g.y == T32 && g.w == T32);
    if (__all(ok)) break;
    __builtin_amdgcn_s_sleep(1);
    g = __builtin_amdgcn_raw_buffer_load_b128(rs, off, 0, 16);
  }
  return g;
}
DEV u32x4 aload128c(unsigned voff, const char* sbase) {
  u32x4 r;
  asm volatile("global_load_dwordx4 %0, %1, %2 sc1" : "=v"(r) : "v"(voff), "s"(uni(sbase)) : "memory");
  return r;
}
DEV u32x2 aload64c(unsigned voff, const char* sbase) {
  u32x2 r;
  asm volatile("global_load_dwordx2 %0, %1, %2 sc1" : "=v"(r) : "v"(voff), "s"(uni(sbase)) : "memory");
  return r;
}

struct Ctx {
  const char *x, *wqkv, *wo, *win, *wsp, *bsp, *wout;
  char *out, *ws;
  LAS char* L;
  unsigned lb;
  int tid, lane, w, bid;
  unsigned Tlo, Thi;
  __amdgpu_buffer_rsrc_t rs;
  u64 t0, tk, c0, c1;
};
DEV u64 tagT(const Ctx& c) { return ((u64)c.Thi << 32) | c.Tlo; }

DEV void s0_issue(const Ctx& c, u32x4& xr, u32x2& ev, int row) {
  ev = aload64c(0, c.ws + WS_EPOCH);
  asm volatile("global_load_dwordx4 %0, %1, %2" : "=v"(xr) : "v"(c.lane * 16), "s"(uni(c.x + (size_t)row * 1024)) : "memory");
}
template <int P> DEV void s0_finish(Ctx& c, u32x4 xr, u32x2 ev, int row) {
  asm volatile("s_waitcnt vmcnt(%2) ; TIE %0 %1" : "+v"(xr), "+v"(ev) : "n"(P) : "memory");
  u64 e = (((u64)(unsigned)__builtin_amdgcn_readfirstlane(ev.y)) << 32) | (unsigned)__builtin_amdgcn_readfirstlane(ev.x);
  e += 1;
  c.Tlo = (unsigned)e; c.Thi = (unsigned)(e >> 32);
  unsigned x0 = xr.x, x1 = xr.y, x2 = xr.z, x3 = xr.w;
  float v[8] = {blo(x0), bhi(x0), blo(x1), bhi(x1), blo(x2), bhi(x2), blo(x3), bhi(x3)};
  float s = ((v[0] + v[1]) + (v[2] + v[3])) + ((v[4] + v[5]) + (v[6] + v[7]));
  s = sumq(sum16(s));
  float m = s * (1.0f / 512.0f), q = 0.f;
#pragma unroll
  for (int j = 0; j < 8; ++j) { float d = v[j] - m; q = __builtin_fmaf(d, d, q); }
  q = sumq(sum16(q));
  float r = 1.0f / sqrtf(q * (1.0f / 512.0f) + 1e-5f);
  if (c.lane < 2)
    gstore64(c.ws + WS_XS + row * 16 + c.lane * 8, ((u64)c.Tlo << 32) | __float_as_uint(c.lane ? r : m));
}

struct G3A { unsigned vw; const char* bw; };
DEV G3A g3_addr(const Ctx& c, int rt) {
  int r = 8 * c.w + (c.lane >> 3);
  G3A a;
  a.vw = r * 4096 + (((c.lane & 7) ^ ((r >> 1) & 7)) << 4);
  a.bw = c.wsp + (size_t)rt * 64 * 4096;
  return a;
}
DEV void g3_prefetch_w(const Ctx& c, const G3A& a) {
#pragma unroll
  for (int s = 0; s < 7; ++s) dma16(a.vw, a.bw + s * 128, c.lb + s * G_SLOT + c.w * 1024);
}

DEV void tlb_touch(const Ctx& c) {
  const int n = c.tid;
  const char* p = c.ws + (size_t)n * 65536;
  if (n >= 224) p = c.wsp + (size_t)(n - 224) * 65536;
  if (n >= 352) p = c.out + (size_t)(n - 352) * 65536;
  if (n >= 384) p = c.wo + (size_t)(n - 384) * 65536;
  if (n >= 392) p = c.wout + (size_t)(n - 392) * 65536;
  if (n >= 403) p = c.bsp;
  Ctx0 k = {c.lb + T_DUMMY};
  touch(k, p);
}
DEV void l2_warm(const Ctx& c, int rt, int a16, int half, int xcol) {
  Ctx0 k = {c.lb + T_DUMMY};
#pragma unroll
  for (int j = 0; j < 2; ++j) {
    int row = rt * 64 + half * 32 + 4 * c.w + 2 * j + (c.lane >> 5);
    touch(k, c.wsp + (size_t)row * 4096 + (c.lane & 31) * 128);
  }
  int n = c.w * 64 + c.lane; n = n < 300 ? n : 299;
  int row = a16 * 75 + (n >> 2);
  const char* p = row < 512 ? c.wo + (size_t)row * 1024 : c.wout + (size_t)(row - 512) * 1024;
  if (c.w < 5) touch(k, p + xcol * 512 + (n & 3) * 128);
}

struct AAddr { unsigned vx0, vx1, vh, vw; const char *bx, *bh, *bw; u64 hmask; };
template <int H, int P> DEV void a_dma(const Ctx& c, const AAddr& a, int s, unsigned slot) {
  const unsigned l = c.lb + slot;
  if (H == 0) {
    if (P == 0) dma16(a.vx0, a.bx + s * 128, l + c.w * 2048);
    if (P == 1) dma16(a.vx1, a.bx + s * 128, l + c.w * 2048 + 1024);
    if (P == 2) dma16m(a.vh, a.bh + s * 128, l + 128 * 128 + c.w * 256, a.hmask);
  } else {
    dma16(a.vw, a.bw + (size_t)s * 64 * 3072 + P * 1024, l + A_W + P * 8192 + c.w * 1024);
  }
}
template <int H> DEV void a_half(const Ctx& c, const AAddr& a, int s, unsigned slot) {
  a_dma<H, 0>(c, a, s, slot); a_dma<H, 1>(c, a, s, slot); a_dma<H, 2>(c, a, s, slot);
}

struct AAcc { f32x4 q[2][2], k[2][2], v[2][2], h, qc; };
struct AF { u32x4 x0, x1, hx, qa, qb, ka, kb, va, vb; };
struct AOfs { unsigned xo, xh, wA, wB; };

DEV void a_read(const Ctx& c, AF& F, unsigned slot, int kk, const AOfs& o) {
  LAS char* L = c.L + slot;
  LAS char* pa = L + A_W + o.wA + kk * 4096;
  LAS char* pb = L + A_W + o.wB + kk * 4096;
  F.qa = trfrag(pa, 512);
  F.x0 = lds128(L + (o.xo ^ (kk * 64)));
  F.x1 = lds128(L + (o.xo ^ (kk * 64)) + 2048);
  F.qb = trfrag(pb, 512);
  F.ka = trfrag(pa + 8192, 512); F.kb = trfrag(pb + 8192, 512);
  F.va = trfrag(pa + 16384, 512); F.vb = trfrag(pb + 16384, 512);
  F.hx = lds128(L + (o.xh ^ (kk * 64)));
  sb();
}
template <bool HV, int H, bool NEXT> DEV void a_step(const Ctx& c, AAcc& A, const AF& F, AF& N, unsigned rslot, int kk, const AOfs& o,
                                                     const AAddr& a, int sn, unsigned slot) {
  LAS char* L = c.L + rslot;
  LAS char* pa = L + A_W + o.wA + kk * 4096;
  LAS char* pb = L + A_W + o.wB + kk * 4096;
  LAS char* px = L + (o.xo ^ (kk * 64));
  sb();
  A.q[0][0] = mfmaX(F.qa, F.x0, A.q[0][0]); sb();
  if (NEXT) N.qa = trfrag(pa, 512);
  sb();
  A.q[1][0] = mfmaX(F.qa, F.x1, A.q[1][0]); sb();
  if (NEXT) { N.x0 = lds128(px); N.x1 = lds128(px + 2048); }
  sb();
  A.q[0][1] = mfmaX(F.qb, F.x0, A.q[0][1]); sb();
  if (NEXT) N.qb = trfrag(pb, 512);
  sb();
  A.q[1][1] = mfmaX(F.qb, F.x1, A.q[1][1]); sb();
  if (NEXT) N.ka = trfrag(pa + 8192, 512);
  sb();
  A.k[0][0] = mfmaX(F.ka, F.x0, A.k[0][0]); sb();
  if (NEXT) N.kb = trfrag(pb + 8192, 512);
  sb();
  A.k[1][0] = mfmaX(F.ka, F.x1, A.k[1][0]); sb();
  if (NEXT) N.va = trfrag(pa + 16384, 512);
  sb();
  A.k[0][1] = mfmaX(F.kb, F.x0, A.k[0][1]); sb();
  if (NEXT) N.vb = trfrag(pb + 16384, 512);
  sb();
  A.k[1][1] = mfmaX(F.kb, F.x1, A.k[1][1]); sb();
  if (NEXT) N.hx = lds128(L + (o.xh ^ (kk * 64)));
  sb();
  A.v[0][0] = mfmaX(F.x0, F.va, A.v[0][0]); sb();
  if (H >= 0) a_dma<(H < 0 ? 0 : H), 0>(c, a, sn, slot);
  sb();
  A.v[1][0] = mfmaX(F.x1, F.va, A.v[1][0]);
  A.v[0][1] = mfmaX(F.x0, F.vb, A.v[0][1]); sb();
  if (H >= 0) a_dma<(H < 0 ? 0 : H), 1>(c, a, sn, slot);
  sb();
  A.v[1][1] = mfmaX(F.x1, F.vb, A.v[1][1]);
  if (HV) {
    A.h = mfmaX(F.hx, F.va, A.h);
  } else {
    A.h = mfmaX(F.ka, F.hx, A.h);
  }
  sb();
  if (H >= 0) a_dma<(H < 0 ? 0 : H), 2>(c, a, sn, slot);
  sb();
  if (!HV) A.qc = mfmaX(F.qa, F.hx, A.qc);
  sb();
}

template <bool HV> DEV void a_loop(const Ctx& c, AAcc& A, const AAddr& a, const AOfs& o, u32x4& g, unsigned gofs) {
  AF F0, F1;
  wait_vm<9 + (PF & 1)>();
  bar();
  a_read(c, F0, 0, 0, o);
  unsigned s0 = 0, s1 = A_SLOT, s2 = 2 * A_SLOT;
#pragma unroll 1
  for (int s = 0; s < 5; ++s) {
    a_step<HV, 1, true>(c, A, F0, F1, s0, 1, o, a, s + 2, s2);
    wait_vm<6>();
    bar();
    STAMP1(16 + s);
    a_step<HV, 0, true>(c, A, F1, F0, s1, 0, o, a, s + 3, s0);
    unsigned t = s0; s0 = s1; s1 = s2; s2 = t;
  }
  a_step<HV, 1, true>(c, A, F0, F1, 2 * A_SLOT, 1, o, a, 7, A_SLOT);
  wait_vm<6>();
  bar();
  g = aload128c(gofs, c.ws + WS_XS);
  a_step<HV, -1, true>(c, A, F1, F0, 0, 0, o, a, 0, 0);
  a_step<HV, -1, true>(c, A, F0, F1, 0, 1, o, a, 0, 0);
  asm volatile("s_waitcnt vmcnt(0) ; TIE %0" : "+v"(g) : : "memory");
  bar();
  a_step<HV, -1, true>(c, A, F1, F0, A_SLOT, 0, o, a, 0, 0);
  a_step<HV, -1, true>(c, A, F0, F1, A_SLOT, 1, o, a, 0, 0);
  a_step<HV, -1, false>(c, A, F1, F0, 0, 0, o, a, 0, 0);
}

DEV void job_a(Ctx& c, int i, int hc, int s0row, const G3A& g3, int rt, int half, int xcol) {
  u32x4 xr; u32x2 ev;
  s0_issue(c, xr, ev, s0row);
  char* ws = c.ws;
  const int lane = c.lane, w = c.w, rg = w & 3, cg = w >> 2;
  const int t = lane & 15, q = lane >> 4;
  AAddr a;
  {
    int r0 = 16 * w + (lane >> 3);
    a.vx0 = r0 * 1024 + (((lane & 7) ^ ((r0 >> 1) & 7)) << 4);
    int r1 = r0 + 8;
    a.vx1 = r1 * 1024 + (((lane & 7) ^ ((r1 >> 1) & 7)) << 4);
    a.bx = c.x + (size_t)i * 128 * 1024;
    int rh = 2 * w + ((lane >> 3) & 1);
    a.vh = rh * 1024 + (((lane & 7) ^ w) << 4);
    a.bh = c.x + (size_t)(i > 0 ? i * 128 - 16 : 0) * 1024;
    a.hmask = w == 0 ? 0xff00ull : 0xffffull;
    int k = 8 * w + (lane >> 3);
    int f = ((k >> 1) & 1) | (((k >> 3) & 1) << 1);
    a.vw = k * 3072 + (((lane & 7) ^ (f << 1)) << 4);
    a.bw = c.wqkv + hc * 128;
  }
  if (c.tid < 24) {
    u32x4 ones = {0x3f803f80u, 0x3f803f80u, 0x3f803f80u, 0x3f803f80u};
    *(LAS u32x4*)(c.L + (c.tid >> 3) * A_SLOT + 128 * 128 + (c.tid & 7) * 16) = ones;
  }
  a_half<0>(c, a, 0, 0); a_half<1>(c, a, 0, 0);
  a_half<0>(c, a, 1, A_SLOT); a_half<1>(c, a, 1, A_SLOT);
  s0_finish<12>(c, xr, ev, s0row);
  STAMP(15);
  a_half<0>(c, a, 2, 2 * A_SLOT);
  if (PF & 1) tlb_touch(c);

  const unsigned xlane = t * 128 + ((q ^ (t >> 1)) << 4);
  const unsigned xo = rg * 32 * 128 + xlane, xh = 128 * 128 + xlane;
  const int f = ((t >> 3) & 1) | ((q & 1) << 1);
  const int hA = 2 * cg + (rg & 1), hB = hA ^ 1;
  const unsigned wl = (8 * q + (t >> 2)) * 128 + (t & 3) * 8;
  const unsigned wA = wl + ((hA ^ f) << 5), wB = wl + ((hB ^ f) << 5);
  const int gj = c.tid < 144 ? c.tid : 143;
  const int gtok = gj < 128 ? i * 128 + gj : (i > 0 ? i * 128 - 16 + (gj - 128) : gj - 128);
  const unsigned gofs = gtok * 16;

  AAcc A;
  const f32x4 z4 = {0.f, 0.f, 0.f, 0.f};
#pragma unroll
  for (int x = 0; x < 2; ++x)
#pragma unroll
    for (int y = 0; y < 2; ++y) { A.q[x][y] = z4; A.k[x][y] = z4; A.v[x][y] = z4; }
  A.h = z4; A.qc = z4;
  u32x4 g = {0, 0, 0, 0};
  const bool hv = rg >= 2;
  const AOfs ao = {xo, xh, wA, wB};
  if (hv) a_loop<true>(c, A, a, ao, g, gofs);
  else a_loop<false>(c, A, a, ao, g, gofs);
  STAMP(2);
  g = settle_pair(g, c.rs, WS_XS + gofs, true, c.Tlo);
  if (c.tid < 144) { u32x2 mr = {g.x, g.z}; *(LAS u32x2*)(c.L + T_XS + c.tid * 8) = mr; }
  if (!hv) {
    if (t == 0) {
      *(LAS f32x4*)(c.L + T_CS + (64 + hA * 16 + 4 * q) * 4) = A.h;
      *(LAS f32x4*)(c.L + T_CS + (hA * 16 + 4 * q) * 4) = A.qc;
    }
  } else if (lane < 16) {
    *(LAS float*)(c.L + T_CS + (128 + hA * 16 + lane) * 4) = A.h[0];
  }
  bar();
  g3_prefetch_w(c, g3);
  if (PF & 2) l2_warm(c, rt, (rt & 7) * 2 + half, half, xcol);

  u32x2 Q[2][2], K[2][2], V[2][2], H;
  {
    f32x4 csq[2], csk[2]; float csv[2];
#pragma unroll
    for (int y = 0; y < 2; ++y) {
      int hh = y ? hB : hA;
      csq[y] = *(LAS f32x4*)(c.L + T_CS + (hh * 16 + 4 * q) * 4);
      csk[y] = *(LAS f32x4*)(c.L + T_CS + (64 + hh * 16 + 4 * q) * 4);
      csv[y] = *(LAS float*)(c.L + T_CS + (128 + hh * 16 + t) * 4);
    }
#pragma unroll
    for (int x = 0; x < 2; ++x) {
      f32x2 mr = *(LAS f32x2*)(c.L + T_XS + (rg * 32 + x * 16 + t) * 8);
      f32x4 m01 = *(LAS f32x4*)(c.L + T_XS + (rg * 32 + x * 16 + 4 * q) * 8);
      f32x4 m23 = *(LAS f32x4*)(c.L + T_XS + (rg * 32 + x * 16 + 4 * q + 2) * 8);
      const float rq = mr.y * 0.25f;
#pragma unroll
      for (int y = 0; y < 2; ++y) {
        f32x4 aq = A.q[x][y], ak = A.k[x][y], av = A.v[x][y];
        Q[x][y].x = pk2(rq * (aq[0] - mr.x * csq[y][0]), rq * (aq[1] - mr.x * csq[y][1]));
        Q[x][y].y = pk2(rq * (aq[2] - mr.x * csq[y][2]), rq * (aq[3] - mr.x * csq[y][3]));
        K[x][y].x = pk2(mr.y * (ak[0] - mr.x * csk[y][0]), mr.y * (ak[1] - mr.x * csk[y][1]));
        K[x][y].y = pk2(mr.y * (ak[2] - mr.x * csk[y][2]), mr.y * (ak[3] - mr.x * csk[y][3]));
        V[x][y].x = pk2(m01[1] * (av[0] - m01[0] * csv[y]), m01[3] * (av[1] - m01[2] * csv[y]));
        V[x][y].y = pk2(m23[1] * (av[2] - m23[0] * csv[y]), m23[3] * (av[3] - m23[2] * csv[y]));
      }
    }
    if (!hv) {
      f32x2 mr = *(LAS f32x2*)(c.L + T_XS + (128 + t) * 8);
      H.x = pk2(mr.y * (A.h[0] - mr.x * csk[0][0]), mr.y * (A.h[1] - mr.x * csk[0][1]));
      H.y = pk2(mr.y * (A.h[2] - mr.x * csk[0][2]), mr.y * (A.h[3] - mr.x * csk[0][3]));
    } else {
      f32x4 m01 = *(LAS f32x4*)(c.L + T_XS + (128 + 4 * q) * 8);
      f32x4 m23 = *(LAS f32x4*)(c.L + T_XS + (128 + 4 * q + 2) * 8);
      H.x = pk2(m01[1] * (A.h[0] - m01[0] * csv[0]), m01[3] * (A.h[1] - m01[2] * csv[0]));
      H.y = pk2(m23[1] * (A.h[2] - m23[0] * csv[0]), m23[3] * (A.h[3] - m23[2] * csv[0]));
    }
  }
  const unsigned KX = G_Z, VX = G_SLOT + G_Z;
  {
    *(LAS u32x2*)(c.L + (hv ? VX : KX) + hA * 512 + lane * 8) = H;
    if (rg < 3) {
      *(LAS u32x2*)(c.L + KX + ((rg + 1) * 4 + hA) * 512 + lane * 8) = K[1][0];
      *(LAS u32x2*)(c.L + KX + ((rg + 1) * 4 + hB) * 512 + lane * 8) = K[1][1];
      *(LAS u32x2*)(c.L + VX + ((rg + 1) * 4 + hA) * 512 + lane * 8) = V[1][0];
      *(LAS u32x2*)(c.L + VX + ((rg + 1) * 4 + hB) * 512 + lane * 8) = V[1][1];
    }
  }
  bar();
  u32x2 Kp[2], Vp[2];
  Kp[0] = lds64(c.L + KX + (rg * 4 + hA) * 512 + lane * 8);
  Kp[1] = lds64(c.L + KX + (rg * 4 + hB) * 512 + lane * 8);
  Vp[0] = lds64(c.L + VX + (rg * 4 + hA) * 512 + lane * 8);
  Vp[1] = lds64(c.L + VX + (rg * 4 + hB) * 512 + lane * 8);

  const float NEG = -1e30f, LOG2E = 1.4426950408889634f;
  float bc[4], bp[4];
#pragma unroll
  for (int j = 0; j < 4; ++j) {
    int d = t - (4 * q + j);
    bc[j] = (d >= 0 && !(d & 1)) ? 0.f : NEG;
    int dp = d + 16;
    bp[j] = (dp <= 14 && !(dp & 1)) ? 0.f : NEG;
  }
  const bool first = (i == 0 && rg == 0);
#pragma unroll
  for (int x = 1; x >= 0; --x) {
    u32x2 O[2];
#pragma unroll
    for (int y = 0; y < 2; ++y) {
      u32x2 kp = x ? K[0][y] : Kp[y], vp = x ? V[0][y] : Vp[y];
      f32x4 sp = mfma16(kp, Q[x][y], z4), sc = mfma16(K[x][y], Q[x][y], z4);
      float e[8];
#pragma unroll
      for (int j = 0; j < 4; ++j) {
        e[j] = __builtin_fmaf(sp[j], LOG2E, (x == 0 && first) ? NEG : bp[j]);
        e[4 + j] = __builtin_fmaf(sc[j], LOG2E, bc[j]);
      }
      float mx = fmaxf(fmaxf(fmaxf(e[0], e[1]), fmaxf(e[2], e[3])), fmaxf(fmaxf(e[4], e[5]), fmaxf(e[6], e[7])));
      mx = maxq(mx);
      float sum = 0.f;
#pragma unroll
      for (int j = 0; j < 8; ++j) { e[j] = __builtin_amdgcn_exp2f(e[j] - mx); sum += e[j]; }
      float inv = __builtin_amdgcn_rcpf(sumq(sum));
      u32x4 P = {pk2(e[0] * inv, e[1] * inv), pk2(e[2] * inv, e[3] * inv), pk2(e[4] * inv, e[5] * inv), pk2(e[6] * inv, e[7] * inv)};
      u32x4 VV = {vp.x, vp.y, V[x][y].x, V[x][y].y};
      f32x4 o = mfma32(VV, P, z4);
      O[y].x = pk2(o[0], o[1]); O[y].y = pk2(o[2], o[3]);
    }
    u32x4 fr = pairswap(O[0], O[1]);
    int qp = (((q & 1) << 1) | (q >> 1)) ^ ((rg & 1) << 1);
    unsigned off = WS_O + ((i * 8 + rg * 2 + x) * 16 + 2 * hc + cg) * 1024 + qp * 256 + t * 16;
    __builtin_amdgcn_raw_buffer_store_b128(fr, c.rs, off, 0, 16);
  }
  wait_vm<0>();
  bar();
  if (c.tid == 0) gstore64(ws + WS_FO + (i * 8 + hc) * 128, tagT(c));
  STAMP(3);
}

struct ZAddr { unsigned vx0, vx1, vw; const char *bx, *bw; };
template <int S, int H, int P> DEV void z_dma(const Ctx& c, const ZAddr& a) {
  if (S >= 16 || S < 0) return;
  const unsigned l = c.lb + (S % 5) * Z_SLOT;
  const char* bw = a.bw + (size_t)(S & 7) * 64 * 2752 + (S < 8 ? 1376 : 0);
  if (S < 8) {
    if (H == 0) dma16(P ? a.vx1 : a.vx0, a.bx + S * 128, l + c.w * 2048 + P * 1024);
    else dma16m(a.vw, bw + P * 4 * 2752, l + Z_W + c.w * 1536 + P * 768, 0xffffffffffffull);
  } else if (P == 0) {
    dma16m(a.vw, bw + H * 4 * 2752, l + Z_W + c.w * 1536 + H * 768, 0xffffffffffffull);
  }
}
template <int S, int H> DEV void z_half(const Ctx& c, const ZAddr& a) { z_dma<S, H, 0>(c, a); z_dma<S, H, 1>(c, a); }
constexpr int z_n(int s) { return s >= 16 ? 0 : (s < 8 ? 4 : 2); }
constexpr int z_wait(int s) { return z_n(s + 1) + z_n(s + 2) + z_n(s + 3); }

struct ZW { u32x4 f[3]; };
template <int S, int KK> DEV void z_read_w(const Ctx& c, ZW& F, const unsigned (&wb)[3]) {
  LAS char* L = c.L + (S % 5) * Z_SLOT;
#pragma unroll
  for (int j = 0; j < 3; ++j) F.f[j] = trfrag(L + Z_W + wb[j] + KK * 6144, 768);
}
template <int S, int KK> DEV void z_read_x(const Ctx& c, u32x4 (&xf)[2][16], unsigned xo) {
  LAS char* L = c.L + (S % 5) * Z_SLOT;
  xf[0][2 * S + KK] = lds128(L + (xo ^ (KK * 64)));
  xf[1][2 * S + KK] = lds128(L + (xo ^ (KK * 64)) + 2048);
}
template <bool SWAP, int J, int RS, int RK, int SN, int H>
DEV void z_step(const Ctx& c, const ZAddr& a, u32x4 (&xf)[2][16], const ZW& F, ZW& N, f32x4 (&acc)[2][3], f32x4& cs, unsigned xo,
                const unsigned (&wb)[3]) {
  const u32x4 ones = {0x3f803f80u, 0x3f803f80u, 0x3f803f80u, 0x3f803f80u};
  constexpr int R = RS < 0 ? 0 : RS, JN = (2 * R + RK) & 15;
  LAS char* L = c.L + (R % 5) * Z_SLOT;
  auto mm = [&](int x, int j) { acc[x][j] = SWAP ? mfmaX(F.f[j], xf[x][J], acc[x][j]) : mfmaX(xf[x][J], F.f[j], acc[x][j]); };
  sb();
  mm(0, 0); sb();
  if (RS >= 0) N.f[0] = trfrag(L + Z_W + wb[0] + RK * 6144, 768);
  sb();
  mm(1, 0); sb();
  if (RS >= 0 && !SWAP) { xf[0][JN] = lds128(L + (xo ^ (RK * 64))); xf[1][JN] = lds128(L + (xo ^ (RK * 64)) + 2048); }
  if (RS >= 0 && SWAP) N.f[1] = trfrag(L + Z_W + wb[1] + RK * 6144, 768);
  sb();
  mm(0, 1); sb();
  if (RS >= 0 && !SWAP) N.f[1] = trfrag(L + Z_W + wb[1] + RK * 6144, 768);
  if (RS >= 0 && SWAP) N.f[2] = trfrag(L + Z_W + wb[2] + RK * 6144, 768);
  sb();
  mm(1, 1); sb();
  if (RS >= 0 && !SWAP) N.f[2] = trfrag(L + Z_W + wb[2] + RK * 6144, 768);
  sb();
  mm(0, 2); sb();
  z_dma<SN, H, 0>(c, a);
  sb();
  mm(1, 2); sb();
  z_dma<SN, H, 1>(c, a);
  sb();
  cs = SWAP ? mfmaX(F.f[0], ones, cs) : mfmaX(ones, F.f[0], cs);
  sb();
}
template <int S> DEV void z_stage_a(const Ctx& c, const ZAddr& a, u32x4 (&xf)[2][16], ZW& F0, ZW& F1, f32x4 (&acc)[2][3], f32x4& cs,
                                    unsigned xo, const unsigned (&wb)[3], u32x4& g, unsigned gofs) {
  z_step<false, 2 * S, S, 1, (S == 0 ? -1 : S + 4), 1>(c, a, xf, F0, F1, acc, cs, xo, wb);
  if (S == 7) asm volatile("s_waitcnt vmcnt(4) ; TIE %0" : "+v"(g) : : "memory");
  else wait_vm<z_wait(S + 1)>();
  bar();
  STAMP1(16 + S);
  if (S == 5) g = aload128c(gofs, c.ws + WS_XS);
  z_step<false, 2 * S + 1, (S < 7 ? S + 1 : -1), 0, S + 5, 0>(c, a, xf, F1, F0, acc, cs, xo, wb);
}
template <int S> DEV void z_stage_b(const Ctx& c, const ZAddr& a, u32x4 (&xf)[2][16], ZW& F0, ZW& F1, f32x4 (&acc)[2][3], f32x4& cs,
                                    const unsigned (&wb)[3], u32x4 (&g)[2], unsigned gofs, const char* gbase) {
  z_step<true, 2 * (S - 8), S, 1, S + 4, 1>(c, a, xf, F0, F1, acc, cs, 0, wb);
  if (S < 15) {
    wait_vm<z_wait(S + 1)>();
    bar();
    if (S == 14) { g[0] = aload128c(gofs, gbase); g[1] = aload128c(gofs, gbase + 2048); }
  }
  z_step<true, 2 * (S - 8) + 1, (S < 15 ? S + 1 : -1), 0, S + 5, 0>(c, a, xf, F1, F0, acc, cs, 0, wb);
  if (S == 15) asm volatile("s_waitcnt vmcnt(0) ; TIE %0 %1" : "+v"(g[0]), "+v"(g[1]) : : "memory");
}

DEV int fb_start(int c) { return c < 3 ? 6 * c : 18 + 5 * (c - 3); }

DEV void job_z(Ctx& c, int i, int zc, int s0row, const G3A& g3) {
  u32x4 xr; u32x2 ev;
  s0_issue(c, xr, ev, s0row);
  char* ws = c.ws;
  const int lane = c.lane, w = c.w, rg = w & 3, cg = w >> 2;
  const int t = lane & 15, q = lane >> 4;
  const int fb0 = fb_start(zc), nb = zc < 3 ? 6 : 5;
  ZAddr a;
  {
    int r0 = 16 * w + (lane >> 3);
    a.vx0 = r0 * 1024 + (((lane & 7) ^ ((r0 >> 1) & 7)) << 4);
    int r1 = r0 + 8;
    a.vx1 = r1 * 1024 + (((lane & 7) ^ ((r1 >> 1) & 7)) << 4);
    a.bx = c.x + (size_t)i * 128 * 1024;
    int l48 = lane < 48 ? lane : 47;
    int kr = l48 / 12, cc = l48 % 12;
    int b = (cc >> 1) ^ (w & 1);
    int fbg = fb0 + b; fbg = fbg > 42 ? 42 : fbg;
    a.vw = (8 * w + kr) * 2752 + fbg * 32 + (cc & 1) * 16;
    a.bw = c.win;
  }
  z_half<0, 0>(c, a); z_half<0, 1>(c, a); z_half<1, 0>(c, a); z_half<1, 1>(c, a); z_half<2, 0>(c, a); z_half<2, 1>(c, a);
  s0_finish<12>(c, xr, ev, s0row);
  STAMP(15);
  z_half<3, 0>(c, a); z_half<3, 1>(c, a); z_half<4, 0>(c, a); z_half<4, 1>(c, a);
  if (PF & 1) tlb_touch(c);

  const unsigned xo = rg * 32 * 128 + t * 128 + ((q ^ (t >> 1)) << 4);
  unsigned wb[3]; int lb3[3];
#pragma unroll
  for (int j = 0; j < 3; ++j) {
    int jr = j + (rg == 3 ? 0 : rg); jr = jr >= 3 ? jr - 3 : jr;
    lb3[j] = 3 * cg + jr;
    wb[j] = (8 * q + (t >> 2)) * 192 + ((lb3[j] ^ (q & 1)) << 5) + (t & 3) * 8;
  }
  const int gj = c.tid < 128 ? c.tid : 127;
  const unsigned gofs = (i * 128 + gj) * 16;
  const f32x4 z4 = {0.f, 0.f, 0.f, 0.f};
  u32x4 xf[2][16];
  f32x4 acc[2][3], cs = z4;
#pragma unroll
  for (int j = 0; j < 3; ++j) { acc[0][j] = z4; acc[1][j] = z4; }
  u32x4 g = {0, 0, 0, 0};
  ZW F0, F1;
  wait_vm<16 + (PF & 1)>();
  bar();
  z_read_x<0, 0>(c, xf, xo); z_read_w<0, 0>(c, F0, wb);
  z_stage_a<0>(c, a, xf, F0, F1, acc, cs, xo, wb, g, gofs); z_stage_a<1>(c, a, xf, F0, F1, acc, cs, xo, wb, g, gofs);
  z_stage_a<2>(c, a, xf, F0, F1, acc, cs, xo, wb, g, gofs); z_stage_a<3>(c, a, xf, F0, F1, acc, cs, xo, wb, g, gofs);
  z_stage_a<4>(c, a, xf, F0, F1, acc, cs, xo, wb, g, gofs); z_stage_a<5>(c, a, xf, F0, F1, acc, cs, xo, wb, g, gofs);
  z_stage_a<6>(c, a, xf, F0, F1, acc, cs, xo, wb, g, gofs); z_stage_a<7>(c, a, xf, F0, F1, acc, cs, xo, wb, g, gofs);
  asm volatile("" : "+v"(acc[0][0]), "+v"(acc[0][1]), "+v"(acc[0][2]), "+v"(acc[1][0]), "+v"(acc[1][1]), "+v"(acc[1][2]), "+v"(cs));
  STAMP(2);
  g = settle_pair(g, c.rs, WS_XS + gofs, true, c.Tlo);
  if (c.tid < 128) { u32x2 mr = {g.x, g.z}; *(LAS u32x2*)(c.L + T_XS + c.tid * 8) = mr; }
  if (rg < 3 && lane < 16) *(LAS float*)(c.L + T_CS + (lb3[0] * 16 + lane) * 4) = cs[0];
  bar();
  u32x2 Z2[2][3];
  {
    float csf[3];
#pragma unroll
    for (int j = 0; j < 3; ++j) csf[j] = *(LAS float*)(c.L + T_CS + (lb3[j] * 16 + t) * 4);
#pragma unroll
    for (int x = 0; x < 2; ++x) {
      f32x4 m01 = *(LAS f32x4*)(c.L + T_XS + (rg * 32 + x * 16 + 4 * q) * 8);
      f32x4 m23 = *(LAS f32x4*)(c.L + T_XS + (rg * 32 + x * 16 + 4 * q + 2) * 8);
      float s1[4] = {0.f, 0.f, 0.f, 0.f}, s2[4] = {0.f, 0.f, 0.f, 0.f};
#pragma unroll
      for (int j = 0; j < 3; ++j) {
        f32x4 v = acc[x][j];
        float y0 = gelu_t(m01[1] * (v[0] - m01[0] * csf[j])), y1 = gelu_t(m01[3] * (v[1] - m01[2] * csf[j]));
        float y2 = gelu_t(m23[1] * (v[2] - m23[0] * csf[j])), y3 = gelu_t(m23[3] * (v[3] - m23[2] * csf[j]));
        unsigned p0 = pk2(y0, y1), p1 = pk2(y2, y3);
        Z2[x][j].x = p0; Z2[x][j].y = p1;
        if (lb3[j] < nb) {
          y0 = blo(p0); y1 = bhi(p0); y2 = blo(p1); y3 = bhi(p1);
          s1[0] += y0; s1[1] += y1; s1[2] += y2; s1[3] += y3;
          s2[0] = __builtin_fmaf(y0, y0, s2[0]); s2[1] = __builtin_fmaf(y1, y1, s2[1]);
          s2[2] = __builtin_fmaf(y2, y2, s2[2]); s2[3] = __builtin_fmaf(y3, y3, s2[3]);
        }
      }
#pragma unroll
      for (int r = 0; r < 4; ++r) { s1[r] = sum16(s1[r]); s2[r] = sum16(s2[r]); }
      if (t == 0) {
        f32x4 o0 = {s1[0], s2[0], s1[1], s2[1]}, o1 = {s1[2], s2[2], s1[3], s2[3]};
        LAS char* p = c.L + T_ZP + (cg * 128 + rg * 32 + x * 16 + 4 * q) * 8;
        *(LAS f32x4*)p = o0; *(LAS f32x4*)(p + 16) = o1;
      }
    }
  }
  bar();
  if (c.tid < 256) {
    int tok = c.tid >> 1, st = c.tid & 1;
    float v = *(LAS float*)(c.L + T_ZP + tok * 8 + st * 4) + *(LAS float*)(c.L + T_ZP + (128 + tok) * 8 + st * 4);
    gstore64(ws + WS_ZS + ((i * 8 + zc) * 128 + tok) * 16 + st * 8, ((u64)c.Tlo << 32) | __float_as_uint(v));
  }
  STAMP(3);
  cs = z4;
#pragma unroll
  for (int j = 0; j < 3; ++j) { acc[0][j] = z4; acc[1][j] = z4; }
  u32x4 gs[2] = {{0, 0, 0, 0}, {0, 0, 0, 0}};
  const int stok = c.tid & 127, pg = c.tid >> 7;
  const unsigned sofs = ((2 * pg) * 128 + stok) * 16;
  const char* sbase = ws + WS_ZS + (size_t)i * 8 * 128 * 16;
  z_read_w<8, 0>(c, F0, wb);
  z_stage_b<8>(c, a, xf, F0, F1, acc, cs, wb, gs, sofs, sbase);  z_stage_b<9>(c, a, xf, F0, F1, acc, cs, wb, gs, sofs, sbase);
  z_stage_b<10>(c, a, xf, F0, F1, acc, cs, wb, gs, sofs, sbase); z_stage_b<11>(c, a, xf, F0, F1, acc, cs, wb, gs, sofs, sbase);
  z_stage_b<12>(c, a, xf, F0, F1, acc, cs, wb, gs, sofs, sbase); z_stage_b<13>(c, a, xf, F0, F1, acc, cs, wb, gs, sofs, sbase);
  z_stage_b<14>(c, a, xf, F0, F1, acc, cs, wb, gs, sofs, sbase); z_stage_b<15>(c, a, xf, F0, F1, acc, cs, wb, gs, sofs, sbase);
  asm volatile("" : "+v"(acc[0][0]), "+v"(acc[0][1]), "+v"(acc[0][2]), "+v"(acc[1][0]), "+v"(acc[1][1]), "+v"(acc[1][2]), "+v"(cs));
  STAMP(4);
  {
    const unsigned so = WS_ZS + i * 8 * 128 * 16 + sofs;
    gs[0] = settle_pair(gs[0], c.rs, so, true, c.Tlo);
    gs[1] = settle_pair(gs[1], c.rs, so + 2048, true, c.Tlo);
    f32x2 p = {__uint_as_float(gs[0].x) + __uint_as_float(gs[1].x), __uint_as_float(gs[0].z) + __uint_as_float(gs[1].z)};
    *(LAS f32x2*)(c.L + T_ZQ + (pg * 128 + stok) * 8) = p;
  }
  if (rg < 3 && t == 0) *(LAS f32x4*)(c.L + T_CS + 512 + (lb3[0] * 16 + 4 * q) * 4) = cs;
  bar();
  g3_prefetch_w(c, g3);
  if (c.tid < 128) {
    f32x2 p0 = *(LAS f32x2*)(c.L + T_ZQ + c.tid * 8), p1 = *(LAS f32x2*)(c.L + T_ZQ + (128 + c.tid) * 8);
    f32x2 p2 = *(LAS f32x2*)(c.L + T_ZQ + (256 + c.tid) * 8), p3 = *(LAS f32x2*)(c.L + T_ZQ + (384 + c.tid) * 8);
    float s = (p0.x + p1.x) + (p2.x + p3.x), ss = (p0.y + p1.y) + (p2.y + p3.y);
    float m = s * (1.0f / 688.0f);
    float var = fmaxf(ss * (1.0f / 688.0f) - m * m, 0.f);
    f32x2 o = {m, 1.0f / sqrtf(var + 1e-5f)};
    *(LAS f32x2*)(c.L + T_ZF + c.tid * 8) = o;
  }
  bar();
  {
    const int qp = ((q & 1) << 1) | (q >> 1);
    f32x4 mr[2][2];
#pragma unroll
    for (int x = 0; x < 2; ++x) {
      mr[x][0] = *(LAS f32x4*)(c.L + T_ZF + (rg * 32 + x * 16 + 4 * q) * 8);
      mr[x][1] = *(LAS f32x4*)(c.L + T_ZF + (rg * 32 + x * 16 + 4 * q + 2) * 8);
    }
#pragma unroll
    for (int j = 0; j < 3; ++j) {
      u32x2 n[2];
#pragma unroll
      for (int x = 0; x < 2; ++x) {
        unsigned p0 = Z2[x][j].x, p1 = Z2[x][j].y;
        n[x].x = pk2((blo(p0) - mr[x][0][0]) * mr[x][0][1], (bhi(p0) - mr[x][0][2]) * mr[x][0][3]);
        n[x].y = pk2((blo(p1) - mr[x][1][0]) * mr[x][1][1], (bhi(p1) - mr[x][1][2]) * mr[x][1][3]);
      }
      u32x4 fr = pairswap(n[0], n[1]);
      if (lb3[j] < nb) {
        unsigned off = WS_Z2N + ((i * 4 + rg) * 44 + fb0 + lb3[j]) * 1024 + qp * 256 + t * 16;
        __builtin_amdgcn_raw_buffer_store_b128(fr, c.rs, off, 0, 16);
      }
    }
  }
  wait_vm<0>();
  bar();
  if (c.tid == 0) gstore64(ws + WS_FZ2 + (zc * 16 + i) * 128, tagT(c));
  STAMP(5);
  {
    f32x2 mr[2];
#pragma unroll
    for (int x = 0; x < 2; ++x) mr[x] = *(LAS f32x2*)(c.L + T_XS + (rg * 32 + x * 16 + t) * 8);
#pragma unroll
    for (int j = 0; j < 3; ++j) {
      f32x4 c4 = *(LAS f32x4*)(c.L + T_CS + 512 + (lb3[j] * 16 + 4 * q) * 4);
      u32x2 n[2];
#pragma unroll
      for (int x = 0; x < 2; ++x) {
        f32x4 v = acc[x][j];
        n[x].x = pk2(gelu_t(mr[x].y * (v[0] - mr[x].x * c4[0])), gelu_t(mr[x].y * (v[1] - mr[x].x * c4[1])));
        n[x].y = pk2(gelu_t(mr[x].y * (v[2] - mr[x].x * c4[2])), gelu_t(mr[x].y * (v[3] - mr[x].x * c4[3])));
      }
      u32x4 fr = pairswap(n[0], n[1]);
      if (lb3[j] < nb) {
        int fb = fb0 + lb3[j];
        unsigned off = WS_Z1 + ((i * 8 + rg * 2 + (q & 1)) * 22 + (fb >> 1)) * 1024 + (2 * (fb & 1) + (q >> 1)) * 256 + t * 16;
        __builtin_amdgcn_raw_buffer_store_b128(fr, c.rs, off, 0, 16);
      }
    }
  }
  wait_vm<0>();
  bar();
  if (c.tid == 0) gstore64(ws + WS_FZ1 + (i * 8 + zc) * 128, tagT(c));
  STAMP(6);
}

struct HAddr { unsigned va, vw; const char *bo, *bu, *bwo, *bwout; };
template <int T> DEV void h_issue_w(const Ctx& c, const HAddr& a) {
  const unsigned l = c.lb + ((32 + T) % 7) * G_SLOT + 8192 + c.w * 1024;
  if (T < 8) dma16(a.vw, a.bwo + (size_t)T * 64 * 1024, l);
  else if (T < 18) dma16(a.vw, a.bwout + (size_t)(T - 8) * 64 * 1024, l);
  else {
    int k = 8 * c.w + (c.lane >> 3);
    unsigned v = k > 47 ? a.vw - (k - 47) * 1024 : a.vw;
    dma16(v, a.bwout + (size_t)640 * 1024, l);
  }
}
template <int T> DEV void h_issue_a(const Ctx& c, const HAddr& a) {
  const unsigned l = c.lb + ((32 + T) % 7) * G_SLOT + c.w * 1024;
  if (T < 8) dma16c(a.va, a.bo + T * 2048, l);
  else dma16c(a.va, a.bu + (T - 8) * 2048, l);
}
struct HF { u32x4 b[2], f0[2], f1[2]; };
template <int T> DEV void h_read(const Ctx& c, HF& F, unsigned ao, unsigned w0, unsigned w1) {
  LAS char* L = c.L + ((32 + T) % 7) * G_SLOT;
#pragma unroll
  for (int kk = 0; kk < 2; ++kk) {
    F.b[kk] = lds128(L + ao + kk * 4096);
    F.f0[kk] = trfrag(L + 8192 + w0 + kk * 4096, 512);
    F.f1[kk] = trfrag(L + 8192 + w1 + kk * 4096, 512);
  }
  sb();
}
template <int T, int IA, int IW> DEV void h_mma(const Ctx& c, const HAddr& h, const HF& F, f32x4 (&acc)[2]) {
  if (IA >= 0) h_issue_a<(IA < 0 ? 0 : IA)>(c, h);
  sb();
  acc[0] = mfma32(F.f0[0], F.b[0], acc[0]);
  acc[1] = mfma32(F.f1[0], F.b[0], acc[1]);
  sb();
  if (IW >= 0) h_issue_w<(IW < 0 ? 0 : IW)>(c, h);
  sb();
  u32x4 b = F.b[1];
  if (T == 18) {
    if (c.lane >= 32) { u32x4 z = {0, 0, 0, 0}; b = z; }
  }
  acc[0] = mfma32(F.f0[1], b, acc[0]);
  acc[1] = mfma32(F.f1[1], b, acc[1]);
  sb();
}
template <int T, int IA, int IW, bool NEXT> DEV void h_it(const Ctx& c, const HAddr& h, HF& Fc, HF& Fn, f32x4 (&acc)[2],
                                                         unsigned ao, unsigned w0, unsigned w1) {
  bar();
  if (NEXT) h_read<T + 1>(c, Fn, ao, w0, w1);
  h_mma<T, IA, IW>(c, h, Fc, acc);
}

template <int V> struct IC { static constexpr int v = V; };
struct GF { u32x4 b0, b1, a0, a1, a2; };

DEV void job_g(Ctx& c, int rt, int gc, const G3A& g3, bool epoch_wg) {
  char* ws = c.ws;
  const int lane = c.lane, w = c.w;
  const int t = lane & 15, q = lane >> 4;
  const int rg = w & 1, cg = (w >> 1) & 1, kh = w >> 2;
  const int fb0 = fb_start(gc), nb = gc < 3 ? 6 : 5;
  const u64 T = tagT(c);
  float bias = blo(*(const GAS u16*)(size_t)(c.bsp + ((rt * 4 + rg * 2 + kh) * 16 + t) * 2));
  if (w == 0) poll_flags(ws + WS_FZ2 + (gc * 16 + (lane & 15)) * 128, lane < 16, T);
  asm volatile("" : "+v"(bias));
  bar();
  STAMP(8);
  const char* zb = ws + WS_Z2N + (size_t)fb0 * 1024;
  const unsigned vz1 = ((w / 6) * 44 + (w % 6)) * 1024 + lane * 16;
  const int u2 = 8 + (w >> 1);
  const unsigned vz2 = (44 + (u2 - 6)) * 1024 + (w & 1) * 512 + (lane & 31) * 16;
  const unsigned lz1 = c.lb + G_Z + w * 1024, lz2 = c.lb + G_Z + u2 * 1024 + (w & 1) * 512;
#pragma unroll
  for (int s = 0; s < 7; ++s) {
    dma16c(vz1, zb + (size_t)s * 88 * 1024, lz1 + s * G_SLOT);
    dma16mc(vz2, zb + (size_t)s * 88 * 1024, lz2 + s * G_SLOT, 0xffffffffull);
  }
  HAddr h;
  {
    h.va = ((w & 3) * 16 + (w >> 2)) * 1024 + lane * 16;
    int k = 8 * w + (lane >> 3);
    int f = ((k >> 1) & 1) | (((k >> 3) & 1) << 1);
    h.vw = k * 1024 + (((lane & 7) ^ (f << 1)) << 4);
    h.bo = ws + WS_O + (size_t)rt * 4 * 16 * 1024;
    h.bu = ws + WS_U + (size_t)rt * 4 * 22 * 1024;
    h.bwo = c.wo + gc * 128;
    h.bwout = c.wout + gc * 128;
  }
  const unsigned vau = ((w & 3) * 22 + (w >> 2)) * 1024 + lane * 16;
  const unsigned bo_ = rg * 32 * 128 + t * 128 + (((4 * kh + q) ^ (t >> 1)) << 4);
  const unsigned ao_ = G_Z + (kh * 6 + 3 * cg) * 1024 + lane * 16;
  const f32x4 z4 = {0.f, 0.f, 0.f, 0.f};
  f32x4 acc[2][3];
#pragma unroll
  for (int j = 0; j < 3; ++j) { acc[0][j] = z4; acc[1][j] = z4; }
  const int tbk = rt * 4 + rg * 2 + kh;
  const unsigned flofs = lane < 8 ? WS_FO + ((rt >> 1) * 8 + lane) * 128 : WS_FZ1 + ((rt >> 1) * 8 + gc) * 128;
  unsigned z1ofs[3];
#pragma unroll
  for (int j = 0; j < 3; ++j) {
    int fb = fb0 + 3 * cg + j; fb = fb > 42 ? 42 : fb;
    z1ofs[j] = (tbk * 22 + (fb >> 1)) * 1024 + (2 * (fb & 1) + (q >> 1)) * 256 + t * 16 + (q & 1) * 8;
  }
  unsigned slot = 0;
  const char* pw = g3.bw + 7 * 128;
  const char* pz = zb + (size_t)7 * 88 * 1024;
  auto rd = [&](GF& F, unsigned sl) {
    LAS char* L = c.L + sl;
    F.a0 = lds128(L + ao_); F.b0 = lds128(L + bo_); F.b1 = lds128(L + bo_ + 2048);
    F.a1 = lds128(L + ao_ + 1024); F.a2 = lds128(L + ao_ + 2048);
    sb();
  };
  auto it = [&](auto kind, auto ht, GF& Fc, GF& Fn, bool next) {
    constexpr int KIND = decltype(kind)::v, HT = decltype(ht)::v;
    bar();
    const unsigned nsl = slot == 6 * G_SLOT ? 0 : slot + G_SLOT;
    if (KIND == 0) dma16(g3.vw, pw, c.lb + slot + w * 1024);
    if (KIND == 1) h_issue_a<HT>(c, h);
    sb();
    if (next) rd(Fn, nsl);
    acc[0][0] = mfma32(Fc.a0, Fc.b0, acc[0][0]); acc[1][0] = mfma32(Fc.a0, Fc.b1, acc[1][0]);
    acc[0][1] = mfma32(Fc.a1, Fc.b0, acc[0][1]);
    sb();
    if (KIND == 0) dma16c(vz1, pz, lz1 + slot);
    if (KIND == 1) h_issue_w<HT>(c, h);
    sb();
    acc[1][1] = mfma32(Fc.a1, Fc.b1, acc[1][1]);
    acc[0][2] = mfma32(Fc.a2, Fc.b0, acc[0][2]); acc[1][2] = mfma32(Fc.a2, Fc.b1, acc[1][2]);
    sb();
    if (KIND == 0) { dma16mc(vz2, pz, lz2 + slot, 0xffffffffull); pw += 128; pz += 88 * 1024; }
    sb();
    slot = nsl;
  };
  const IC<0> K0; const IC<1> K1; const IC<2> K2;
  GF F0, F1;
  wait_vm<12>();
  bar();
  rd(F0, 0);
  wait_vm<10>(); it(K0, K0, F0, F1, true);
  wait_vm<11>(); it(K0, K0, F1, F0, true);
  wait_vm<12>(); it(K0, K0, F0, F1, true);
  wait_vm<13>(); it(K0, K0, F1, F0, true);
  wait_vm<14>(); it(K0, K0, F0, F1, true);
  wait_vm<15>(); it(K0, K0, F1, F0, true);
#pragma unroll 1
  for (int s = 6; s < 10; s += 2) { wait_vm<15>(); it(K0, K0, F0, F1, true); wait_vm<15>(); it(K0, K0, F1, F0, true); }
  u32x2 fl = aload64c(flofs, ws);
#pragma unroll 1
  for (int s = 10; s < 16; s += 2) { wait_vm<15>(); it(K0, K0, F0, F1, true); wait_vm<15>(); it(K0, K0, F1, F0, true); }
  asm volatile("; TIE %0" : "+v"(fl));
  if (!__all(fl.x == c.Tlo && fl.y == c.Thi)) poll_flags(ws + flofs, true, T);
  u32x2 z1v[3];
  z1v[0] = aload64c(z1ofs[0], ws + WS_Z1);
  z1v[1] = aload64c(z1ofs[1], ws + WS_Z1);
  z1v[2] = aload64c(z1ofs[2], ws + WS_Z1);
#pragma unroll 1
  for (int s = 16; s < 24; s += 2) { wait_vm<15>(); it(K0, K0, F0, F1, true); wait_vm<15>(); it(K0, K0, F1, F0, true); }
  wait_vm<15>(); it(K0, K0, F0, F1, true);
  wait_vm<15>(); it(K1, IC<0>(), F1, F0, true);
  wait_vm<14>(); it(K1, IC<1>(), F0, F1, true);
  wait_vm<13>(); it(K1, IC<2>(), F1, F0, true);
  wait_vm<12>(); it(K1, IC<3>(), F0, F1, true);
  wait_vm<11>(); it(K1, IC<4>(), F1, F0, true);
  wait_vm<10>(); it(K1, IC<5>(), F0, F1, true);
  it(K2, K0, F1, F0, false);
  asm volatile("; TIE %0 %1 %2" : "+v"(z1v[0]), "+v"(z1v[1]), "+v"(z1v[2]));
  STAMP(9);
  {
    const unsigned e01 = 3 * G_SLOT + w * 2048 + lane * 16, e2 = T_EX2 + w * 1024 + lane * 16;
    const unsigned p01 = 3 * G_SLOT + (w ^ 4) * 2048 + lane * 16, p2 = T_EX2 + (w ^ 4) * 1024 + lane * 16;
    f32x4 keep[3], send[3];
#pragma unroll
    for (int j = 0; j < 3; ++j) { keep[j] = kh ? acc[1][j] : acc[0][j]; send[j] = kh ? acc[0][j] : acc[1][j]; }
    *(LAS f32x4*)(c.L + e01) = send[0]; *(LAS f32x4*)(c.L + e01 + 1024) = send[1]; *(LAS f32x4*)(c.L + e2) = send[2];
    bar();
    keep[0] += *(LAS f32x4*)(c.L + p01); keep[1] += *(LAS f32x4*)(c.L + p01 + 1024); keep[2] += *(LAS f32x4*)(c.L + p2);
#pragma unroll
    for (int j = 0; j < 3; ++j) {
      unsigned g0 = pk2(keep[j][0] + bias, keep[j][1] + bias), g1 = pk2(keep[j][2] + bias, keep[j][3] + bias);
      unsigned a0 = z1v[j].x, a1 = z1v[j].y;
      u32x2 u;
      u.x = pk2(blo(a0) * blo(g0), bhi(a0) * bhi(g0));
      u.y = pk2(blo(a1) * blo(g1), bhi(a1) * bhi(g1));
      if (3 * cg + j < nb) __builtin_amdgcn_raw_buffer_store_b64(u, c.rs, WS_U + z1ofs[j], 0, 16);
    }
  }
  wait_vm<0>();
  bar();
  if (c.tid == 0) gstore64(ws + WS_FU + (rt * 8 + gc) * 128, T);
  STAMP(10);

  const int tb = w & 3, np = w >> 2;
  const unsigned ha = tb * 1024 + lane * 16;
  const int fw = ((t >> 3) & 1) | ((q & 1) << 1);
  const unsigned wl = (8 * q + (t >> 2)) * 128 + (t & 3) * 8;
  const unsigned hw0 = wl + (((2 * np) ^ fw) << 5), hw1 = wl + (((2 * np + 1) ^ fw) << 5);
  f32x4 sa[2] = {z4, z4};
  HF H0, H1;
  h_issue_a<6>(c, h); h_issue_w<6>(c, h);
  h_read<0>(c, H0, ha, hw0, hw1);
  h_it<0, 7, 7, true>(c, h, H0, H1, sa, ha, hw0, hw1);
  h_it<1, -1, 8, true>(c, h, H1, H0, sa, ha, hw0, hw1);
  h_it<2, -1, 9, true>(c, h, H0, H1, sa, ha, hw0, hw1);
  h_it<3, -1, 10, true>(c, h, H1, H0, sa, ha, hw0, hw1);
  h_it<4, -1, 11, true>(c, h, H0, H1, sa, ha, hw0, hw1);
  wait_vm<6>(); h_it<5, -1, 12, true>(c, h, H1, H0, sa, ha, hw0, hw1);
  wait_vm<5>(); h_it<6, -1, 13, true>(c, h, H0, H1, sa, ha, hw0, hw1);
  h_it<7, -1, 14, false>(c, h, H1, H0, sa, ha, hw0, hw1);
  asm volatile("" : "+v"(sa[0]), "+v"(sa[1]));
  STAMP(11);
  h.va = vau;
  if (w == 0) poll_flags(ws + WS_FU + (rt * 8 + (lane & 7)) * 128, lane < 8, T);
  if (epoch_wg && w == 1) {
#pragma unroll
    for (int j = 0; j < 2; ++j) {
      poll_flags(ws + WS_FZ2 + (j * 64 + lane) * 128, true, T);
      poll_flags(ws + WS_FO + (j * 64 + lane) * 128, true, T);
    }
    if (lane == 0) gstore64(ws + WS_EPOCH, T);
  }
  bar();
  STAMP(12);
  h_issue_a<8>(c, h); h_issue_a<9>(c, h); h_issue_a<10>(c, h); h_issue_a<11>(c, h); h_issue_a<12>(c, h); h_issue_a<13>(c, h);
  h_issue_a<14>(c, h);
  const int orow = rt * 64 + tb * 16 + t;
  const unsigned oofs = orow * 1024 + gc * 128 + np * 64 + (q & 1) * 32 + (q >> 1) * 16;
  f32x4 ga[2] = {z4, z4};
  wait_vm<6>();
  bar();
  h_read<8>(c, H0, ha, hw0, hw1);
  wait_vm<5>(); h_it<8, 15, 15, true>(c, h, H0, H1, ga, ha, hw0, hw1);
  wait_vm<6>(); h_it<9, 16, 16, true>(c, h, H1, H0, ga, ha, hw0, hw1);
  wait_vm<7>(); h_it<10, 17, 17, true>(c, h, H0, H1, ga, ha, hw0, hw1);
  wait_vm<8>(); h_it<11, 18, 18, true>(c, h, H1, H0, ga, ha, hw0, hw1);
  wait_vm<9>(); h_it<12, -1, -1, true>(c, h, H0, H1, ga, ha, hw0, hw1);
  u32x4 xres;
  asm volatile("global_load_dwordx4 %0, %1, %2" : "=v"(xres) : "v"(oofs), "s"(uni(c.x)) : "memory");
  wait_vm<9>(); h_it<13, -1, -1, true>(c, h, H1, H0, ga, ha, hw0, hw1);
  wait_vm<7>(); h_it<14, -1, -1, true>(c, h, H0, H1, ga, ha, hw0, hw1);
  wait_vm<5>(); h_it<15, -1, -1, true>(c, h, H1, H0, ga, ha, hw0, hw1);
  wait_vm<3>(); h_it<16, -1, -1, true>(c, h, H0, H1, ga, ha, hw0, hw1);
  wait_vm<1>(); h_it<17, -1, -1, true>(c, h, H1, H0, ga, ha, hw0, hw1);
  h_mma<18, -1, -1>(c, h, H0, ga);
  asm volatile("s_waitcnt vmcnt(0) ; TIE %0" : "+v"(xres) : : "memory");
  STAMP(13);
  {
    u32x2 S[2], G[2];
#pragma unroll
    for (int b = 0; b < 2; ++b) {
      S[b].x = pk2(sa[b][0], sa[b][1]); S[b].y = pk2(sa[b][2], sa[b][3]);
      G[b].x = pk2(ga[b][0], ga[b][1]); G[b].y = pk2(ga[b][2], ga[b][3]);
    }
    u32x4 s8 = pairswap(S[0], S[1]), g8 = pairswap(G[0], G[1]);
    u32x4 o;
#pragma unroll
    for (int j = 0; j < 4; ++j) {
      unsigned su = s8[j], gu = g8[j], xu = xres[j];
      float g0 = blo(gu), g1 = bhi(gu);
      float e0 = g0 * __builtin_amdgcn_rcpf(1.0f + __builtin_amdgcn_exp2f(g0 * -1.4426950408889634f));
      float e1 = g1 * __builtin_amdgcn_rcpf(1.0f + __builtin_amdgcn_exp2f(g1 * -1.4426950408889634f));
      o[j] = pk2(__builtin_fmaf(blo(su), e0, blo(xu)), __builtin_fmaf(bhi(su), e1, bhi(xu)));
    }
    if (OUTSC == 0) *(GAS u32x4*)(size_t)(c.out + oofs) = o;
    else {
      __amdgpu_buffer_rsrc_t ro = __builtin_amdgcn_make_buffer_rsrc((void*)c.out, 0, 2048 * 1024, 0x00020000);
      __builtin_amdgcn_raw_buffer_store_b128(o, ro, oofs, 0, OUTSC == 1 ? 16 : OUTSC == 2 ? 2 : 17);
    }
  }
  STAMP(14); STAMPC(31);
}

__global__ __launch_bounds__(512, 2) void fused_kernel(const char* x, const char* wqkv, const char* wo, const char* win, const char* wsp,
                                                       const char* bsp, const char* wout, char* out, char* ws) {
  extern __shared__ __attribute__((aligned(16))) char smem[];
  Ctx c;
  c.x = x; c.wqkv = wqkv; c.wo = wo; c.win = win; c.wsp = wsp; c.bsp = bsp; c.wout = wout; c.out = out; c.ws = ws;
  c.L = (LAS char*)smem;
  c.lb = (unsigned)(size_t)c.L;
  c.tid = threadIdx.x; c.lane = c.tid & 63; c.w = __builtin_amdgcn_readfirstlane(c.tid >> 6);
  c.rs = __builtin_amdgcn_make_buffer_rsrc((void*)ws, 0, WS_USED, 0x00020000);
  c.Tlo = 0; c.Thi = 0;
  c.bid = blockIdx.x;
  STAMP(0); STAMPC(30);
  const int bid = blockIdx.x, xcd = bid & 7, sl = bid >> 3;
  const int i = (xcd & 1) * 8 + (sl >> 2), rsel = sl & 3, col = 2 * (xcd >> 1) + (rsel & 1);
  const int r16 = (xcd >> 1) * 4 + rsel;
  const int rt = (xcd >> 1) * 8 + (sl >> 2), gc = (xcd & 1) * 4 + (sl & 3);
  const int s0row = i * 128 + r16 * 8 + c.w;
  const G3A g3 = g3_addr(c, rt);
  if (rsel < 2) job_a(c, i, col, s0row, g3, rt, rsel, xcd & 1);
  else job_z(c, i, col, s0row, g3);
  STAMP(7);
  job_g(c, rt, gc, g3, bid == 0);
  if (c.tid == 0) {
    atomicMin((u64*)(ws + WS_DBG + 24), c.t0);
    atomicMax((u64*)(ws + WS_DBG + 32), c.tk);
  }
}

__global__ void k_pre(char* ws) {
  if (threadIdx.x == 0) {
    *(u64*)(ws + WS_DBG) = __builtin_amdgcn_s_memrealtime();
    *(u64*)(ws + WS_DBG + 24) = ~0ull;
    *(u64*)(ws + WS_DBG + 32) = 0;
  }
}
__global__ void k_post(char* ws) {
  u64 now = __builtin_amdgcn_s_memrealtime();
  u64 tk0 = *(volatile u64*)(ws + WS_DBG), tmin = *(volatile u64*)(ws + WS_DBG + 24), tmax = *(volatile u64*)(ws + WS_DBG + 32);
  u64 d = MEAS2 == 1 ? tmin - tk0 : MEAS2 == 2 ? now - tmax : MEAS2 == 3 ? now - tk0 : MEAS2 == 4 ? tmax - tmin : 0;
  d *= 8;
  u64 target = __builtin_amdgcn_s_memrealtime() + (d < 100000 ? d : 100000);
  for (int n = 0; n < 1000000; ++n) { if (__builtin_amdgcn_s_memrealtime() >= target) break; __builtin_amdgcn_s_sleep(4); }
}
extern "C" void kernel_launch(void* const* d_in, const int* in_sizes, int n_in, void* d_out, int out_size, void* d_ws, size_t ws_size,
                              hipStream_t stream) {
  (void)in_sizes; (void)n_in; (void)out_size; (void)ws_size;
  (void)hipFuncSetAttribute((const void*)fused_kernel, hipFuncAttributeMaxDynamicSharedMemorySize, LDS_BYTES);
  k_pre<<<1, 64, 0, stream>>>((char*)d_ws);
  fused_kernel<<<256, 512, LDS_BYTES, stream>>>((const char*)d_in[0], (const char*)d_in[1], (const char*)d_in[2], (const char*)d_in[4],
                                                (const char*)d_in[5], (const char*)d_in[6], (const char*)d_in[7], (char*)d_out, (char*)d_ws);
  k_post<<<1, 64, 0, stream>>>((char*)d_ws);
}
```

```cpp
#define MEAS 8
#define MEASB 0
#define PF 2
#include <hip/hip_runtime.h>
#define EXP 0
#define OUTSC 0

typedef unsigned short u16;
typedef unsigned long long u64;
typedef __bf16 bf16x8 __attribute__((ext_vector_type(8)));
typedef __bf16 bf16x2 __attribute__((ext_vector_type(2)));
typedef short s16x4 __attribute__((ext_vector_type(4)));
typedef float f32x4 __attribute__((ext_vector_type(4)));
typedef float f32x2 __attribute__((ext_vector_type(2)));
typedef unsigned u32x4 __attribute__((ext_vector_type(4)));
typedef unsigned u32x2 __attribute__((ext_vector_type(2)));
#define LAS __attribute__((address_space(3)))
#define GAS __attribute__((address_space(1)))
#define DEV __device__ __forceinline__

constexpr unsigned WS_EPOCH = 0;
constexpr unsigned WS_FZ2 = 4096;
constexpr unsigned WS_FZ1 = WS_FZ2 + 128 * 128;
constexpr unsigned WS_FO  = WS_FZ1 + 128 * 128;
constexpr unsigned WS_FU  = WS_FO + 128 * 128;
constexpr unsigned WS_XS  = 0x20000;
constexpr unsigned WS_ZS  = 0x30000;
constexpr unsigned WS_Z2N = 0x100000;
constexpr unsigned WS_Z1  = 0x400000;
constexpr unsigned WS_U   = 0x700000;
constexpr unsigned WS_O   = 0xA00000;
constexpr unsigned WS_DBG = 0xD00000;
constexpr unsigned WS_USED = 0xE00000;

constexpr unsigned LDS_BYTES = 160 * 1024;
constexpr unsigned A_SLOT = 43008, A_W = 18432;
constexpr unsigned Z_SLOT = 28672, Z_W = 16384;
constexpr unsigned G_SLOT = 20480, G_Z = 8192;
constexpr unsigned T_BASE = 143360;
constexpr unsigned T_EX2 = T_BASE;
constexpr unsigned T_XS = T_BASE + 8192;
constexpr unsigned T_CS = T_XS + 1280;
constexpr unsigned T_ZP = T_CS + 1024;
constexpr unsigned T_ZQ = T_ZP + 2048;
constexpr unsigned T_ZF = T_ZQ + 4096;
constexpr unsigned T_DUMMY = T_ZF + 1024;
static_assert(T_DUMMY + 256 <= LDS_BYTES, "lds");

constexpr unsigned SPIN_CAP = 200000;

#define STAMP(k) do { if ((k) == MEAS) c.tk = __builtin_amdgcn_s_memrealtime(); if ((k) == 0) c.t0 = __builtin_amdgcn_s_memrealtime(); } while (0)
#define STAMPC(k) do { if (MEAS == 99) { if ((k) == 30) c.c0 = __builtin_amdgcn_s_memtime(); else c.c1 = __builtin_amdgcn_s_memtime(); } } while (0)
#define STAMP1(k) do {} while (0)

DEV const char* uni(const char* p) {
  u64 v = (u64)(size_t)p;
  unsigned lo = __builtin_amdgcn_readfirstlane((unsigned)v), hi = __builtin_amdgcn_readfirstlane((unsigned)(v >> 32));
  return (const char*)(size_t)(((u64)hi << 32) | lo);
}
DEV unsigned uni(unsigned v) { return __builtin_amdgcn_readfirstlane(v); }
struct Ctx0 { unsigned sink; };
DEV void dma16(unsigned voff, const char* sbase, unsigned lds) {
  asm volatile("s_mov_b32 m0, %2\n\ts_nop 0\n\tglobal_load_lds_dwordx4 %0, %1" ::"v"(voff), "s"(uni(sbase)), "s"(uni(lds)) : "memory");
}
DEV void dma16c(unsigned voff, const char* sbase, unsigned lds) {
  asm volatile("s_mov_b32 m0, %2\n\ts_nop 0\n\tglobal_load_lds_dwordx4 %0, %1 sc1" ::"v"(voff), "s"(uni(sbase)), "s"(uni(lds)) : "memory");
}
DEV void dma16m(unsigned voff, const char* sbase, unsigned lds, u64 mask) {
  asm volatile("s_mov_b32 m0, %2\n\ts_mov_b64 exec, %3\n\tglobal_load_lds_dwordx4 %0, %1\n\ts_mov_b64 exec, -1"
               ::"v"(voff), "s"(uni(sbase)), "s"(uni(lds)), "s"(mask) : "memory");
}
DEV void dma16mc(unsigned voff, const char* sbase, unsigned lds, u64 mask) {
  asm volatile("s_mov_b32 m0, %2\n\ts_mov_b64 exec, %3\n\tglobal_load_lds_dwordx4 %0, %1 sc1\n\ts_mov_b64 exec, -1"
               ::"v"(voff), "s"(uni(sbase)), "s"(uni(lds)), "s"(mask) : "memory");
}
DEV void touch(const Ctx0& c, const char* p) {
  asm volatile("s_mov_b32 m0, %1\n\ts_nop 0\n\tglobal_load_lds_dword %0, off" ::"v"(p), "s"(uni(c.sink)) : "memory");
}
template <int N> DEV void wait_vm() { asm volatile("s_waitcnt vmcnt(%0)" ::"n"(N) : "memory"); }
DEV void sb() { __builtin_amdgcn_sched_barrier(0); }
DEV void bar() { asm volatile("s_waitcnt lgkmcnt(0)\n\ts_barrier" ::: "memory"); }

template <int V = 0> DEV f32x4 mfmaX(u32x4 a, u32x4 b, f32x4 c) {
  return __builtin_amdgcn_mfma_f32_16x16x32_bf16(__builtin_bit_cast(bf16x8, a), __builtin_bit_cast(bf16x8, b), c, 0, 0, 0);
}
DEV f32x4 mfma32(u32x4 a, u32x4 b, f32x4 c) {
  return __builtin_amdgcn_mfma_f32_16x16x32_bf16(__builtin_bit_cast(bf16x8, a), __builtin_bit_cast(bf16x8, b), c, 0, 0, 0);
}
DEV f32x4 mfma16(u32x2 a, u32x2 b, f32x4 c) {
  return __builtin_amdgcn_mfma_f32_16x16x16bf16_1k(__builtin_bit_cast(s16x4, a), __builtin_bit_cast(s16x4, b), c, 0, 0, 0);
}
DEV u32x4 lds128(LAS char* p) { return *(LAS u32x4*)p; }
DEV u32x2 lds64(LAS char* p) { return *(LAS u32x2*)p; }
DEV u32x4 trfrag(LAS char* p, int off4) {
  s16x4 a = __builtin_amdgcn_ds_read_tr16_b64_v4i16((LAS s16x4*)p);
  s16x4 b = __builtin_amdgcn_ds_read_tr16_b64_v4i16((LAS s16x4*)(p + off4));
  u32x2 ua = __builtin_bit_cast(u32x2, a), ub = __builtin_bit_cast(u32x2, b);
  u32x4 r = {ua.x, ua.y, ub.x, ub.y};
  return r;
}
DEV unsigned pk2(float a, float b) {
  f32x2 v = {a, b};
  bf16x2 h = __builtin_convertvector(v, bf16x2);
  return __builtin_bit_cast(unsigned, h);
}
DEV float blo(unsigned u) { return __uint_as_float(u << 16); }
DEV float bhi(unsigned u) { return __uint_as_float(u & 0xffff0000u); }
template <int CTRL> DEV float dpp(float v) {
  return __uint_as_float(__builtin_amdgcn_update_dpp(0u, __float_as_uint(v), CTRL, 0xf, 0xf, true));
}
DEV float sum16(float v) {
  v += dpp<0x128>(v); v += dpp<0x124>(v); v += dpp<0x122>(v); v += dpp<0x121>(v);
  return v;
}
DEV float sumq(float v) {
  unsigned u = __float_as_uint(v);
  auto a = __builtin_amdgcn_permlane16_swap(u, u, false, false);
  v = __uint_as_float(a[0]) + __uint_as_float(a[1]);
  u = __float_as_uint(v);
  auto b = __builtin_amdgcn_permlane32_swap(u, u, false, false);
  return __uint_as_float(b[0]) + __uint_as_float(b[1]);
}
DEV float maxq(float v) {
  unsigned u = __float_as_uint(v);
  auto a = __builtin_amdgcn_permlane16_swap(u, u, false, false);
  v = fmaxf(__uint_as_float(a[0]), __uint_as_float(a[1]));
  u = __float_as_uint(v);
  auto b = __builtin_amdgcn_permlane32_swap(u, u, false, false);
  return fmaxf(__uint_as_float(b[0]), __uint_as_float(b[1]));
}
DEV u32x4 pairswap(u32x2 b0, u32x2 b1) {
  auto s0 = __builtin_amdgcn_permlane16_swap(b0.x, b1.x, false, false);
  auto s1 = __builtin_amdgcn_permlane16_swap(b0.y, b1.y, false, false);
  u32x4 r = {s0[0], s1[0], s0[1], s1[1]};
  return r;
}
DEV float gelu_t(float v) {
  float e = __builtin_amdgcn_exp2f(v * __builtin_fmaf(v * v, -0.10294324f, -2.3022082f));
  return v * __builtin_amdgcn_rcpf(1.0f + e);
}
DEV u64 gload64(const char* p) { return __hip_atomic_load((GAS u64*)(size_t)p, __ATOMIC_RELAXED, __HIP_MEMORY_SCOPE_AGENT); }
DEV void gstore64(const char* p, u64 v) { __hip_atomic_store((GAS u64*)(size_t)p, v, __ATOMIC_RELAXED, __HIP_MEMORY_SCOPE_AGENT); }
DEV void poll_flags(const char* p, bool active, u64 T) {
  for (unsigned it = 0; it < SPIN_CAP; ++it) {
    bool ok = !active || gload64(p) == T;
    if (__all(ok)) break;
    __builtin_amdgcn_s_sleep(1);
  }
}
DEV u32x4 settle_pair(u32x4 g, __amdgpu_buffer_rsrc_t rs, unsigned off, bool active, unsigned T32) {
  for (unsigned it = 0; it < SPIN_CAP; ++it) {
    bool ok = !active || (g.y == T32 && g.w == T32);
    if (__all(ok)) break;
    __builtin_amdgcn_s_sleep(1);
    g = __builtin_amdgcn_raw_buffer_load_b128(rs, off, 0, 16);
  }
  return g;
}
DEV u32x4 aload128c(unsigned voff, const char* sbase) {
  u32x4 r;
  asm volatile("global_load_dwordx4 %0, %1, %2 sc1" : "=v"(r) : "v"(voff), "s"(uni(sbase)) : "memory");
  return r;
}
DEV u32x2 aload64c(unsigned voff, const char* sbase) {
  u32x2 r;
  asm volatile("global_load_dwordx2 %0, %1, %2 sc1" : "=v"(r) : "v"(voff), "s"(uni(sbase)) : "memory");
  return r;
}

struct Ctx {
  const char *x, *wqkv, *wo, *win, *wsp, *bsp, *wout;
  char *out, *ws;
  LAS char* L;
  unsigned lb;
  int tid, lane, w, bid;
  unsigned Tlo, Thi;
  __amdgpu_buffer_rsrc_t rs;
  u64 t0, tk, c0, c1;
};
DEV u64 tagT(const Ctx& c) { return ((u64)c.Thi << 32) | c.Tlo; }

DEV void s0_issue(const Ctx& c, u32x4& xr, u32x2& ev, int row) {
  ev = aload64c(0, c.ws + WS_EPOCH);
  asm volatile("global_load_dwordx4 %0, %1, %2" : "=v"(xr) : "v"(c.lane * 16), "s"(uni(c.x + (size_t)row * 1024)) : "memory");
}
template <int P> DEV void s0_finish(Ctx& c, u32x4 xr, u32x2 ev, int row) {
  asm volatile("s_waitcnt vmcnt(%2) ; TIE %0 %1" : "+v"(xr), "+v"(ev) : "n"(P) : "memory");
  u64 e = (((u64)(unsigned)__builtin_amdgcn_readfirstlane(ev.y)) << 32) | (unsigned)__builtin_amdgcn_readfirstlane(ev.x);
  e += 1;
  c.Tlo = (unsigned)e; c.Thi = (unsigned)(e >> 32);
  unsigned x0 = xr.x, x1 = xr.y, x2 = xr.z, x3 = xr.w;
  float v[8] = {blo(x0), bhi(x0), blo(x1), bhi(x1), blo(x2), bhi(x2), blo(x3), bhi(x3)};
  float s = ((v[0] + v[1]) + (v[2] + v[3])) + ((v[4] + v[5]) + (v[6] + v[7]));
  s = sumq(sum16(s));
  float m = s * (1.0f / 512.0f), q = 0.f;
#pragma unroll
  for (int j = 0; j < 8; ++j) { float d = v[j] - m; q = __builtin_fmaf(d, d, q); }
  q = sumq(sum16(q));
  float r = 1.0f / sqrtf(q * (1.0f / 512.0f) + 1e-5f);
  if (c.lane < 2)
    gstore64(c.ws + WS_XS + row * 16 + c.lane * 8, ((u64)c.Tlo << 32) | __float_as_uint(c.lane ? r : m));
}

struct G3A { unsigned vw; const char* bw; };
DEV G3A g3_addr(const Ctx& c, int rt) {
  int r = 8 * c.w + (c.lane >> 3);
  G3A a;
  a.vw = r * 4096 + (((c.lane & 7) ^ ((r >> 1) & 7)) << 4);
  a.bw = c.wsp + (size_t)rt * 64 * 4096;
  return a;
}
DEV void g3_prefetch_w(const Ctx& c, const G3A& a) {
#pragma unroll
  for (int s = 0; s < 7; ++s) dma16(a.vw, a.bw + s * 128, c.lb + s * G_SLOT + c.w * 1024);
}

DEV void tlb_touch(const Ctx& c) {
  const int n = c.tid;
  const char* p = c.ws + (size_t)n * 65536;
  if (n >= 224) p = c.wsp + (size_t)(n - 224) * 65536;
  if (n >= 352) p = c.out + (size_t)(n - 352) * 65536;
  if (n >= 384) p = c.wo + (size_t)(n - 384) * 65536;
  if (n >= 392) p = c.wout + (size_t)(n - 392) * 65536;
  if (n >= 403) p = c.bsp;
  Ctx0 k = {c.lb + T_DUMMY};
  touch(k, p);
}
DEV void l2_warm(const Ctx& c, int rt, int a16, int half, int xcol) {
  Ctx0 k = {c.lb + T_DUMMY};
#pragma unroll
  for (int j = 0; j < 2; ++j) {
    int row = rt * 64 + half * 32 + 4 * c.w + 2 * j + (c.lane >> 5);
    touch(k, c.wsp + (size_t)row * 4096 + (c.lane & 31) * 128);
  }
  int n = c.w * 64 + c.lane; n = n < 300 ? n : 299;
  int row = a16 * 75 + (n >> 2);
  const char* p = row < 512 ? c.wo + (size_t)row * 1024 : c.wout + (size_t)(row - 512) * 1024;
  if (c.w < 5) touch(k, p + xcol * 512 + (n & 3) * 128);
}

struct AAddr { unsigned vx0, vx1, vh, vw; const char *bx, *bh, *bw; u64 hmask; };
template <int H, int P> DEV void a_dma(const Ctx& c, const AAddr& a, int s, unsigned slot) {
  const unsigned l = c.lb + slot;
  if (H == 0) {
    if (P == 0) dma16(a.vx0, a.bx + s * 128, l + c.w * 2048);
    if (P == 1) dma16(a.vx1, a.bx + s * 128, l + c.w * 2048 + 1024);
    if (P == 2) dma16m(a.vh, a.bh + s * 128, l + 128 * 128 + c.w * 256, a.hmask);
  } else {
    dma16(a.vw, a.bw + (size_t)s * 64 * 3072 + P * 1024, l + A_W + P * 8192 + c.w * 1024);
  }
}
template <int H> DEV void a_half(const Ctx& c, const AAddr& a, int s, unsigned slot) {
  a_dma<H, 0>(c, a, s, slot); a_dma<H, 1>(c, a, s, slot); a_dma<H, 2>(c, a, s, slot);
}

struct AAcc { f32x4 q[2][2], k[2][2], v[2][2], h, qc; };
struct AF { u32x4 x0, x1, hx, qa, qb, ka, kb, va, vb; };
struct AOfs { unsigned xo, xh, wA, wB; };

DEV void a_read(const Ctx& c, AF& F, unsigned slot, int kk, const AOfs& o) {
  LAS char* L = c.L + slot;
  LAS char* pa = L + A_W + o.wA + kk * 4096;
  LAS char* pb = L + A_W + o.wB + kk * 4096;
  F.qa = trfrag(pa, 512);
  F.x0 = lds128(L + (o.xo ^ (kk * 64)));
  F.x1 = lds128(L + (o.xo ^ (kk * 64)) + 2048);
  F.qb = trfrag(pb, 512);
  F.ka = trfrag(pa + 8192, 512); F.kb = trfrag(pb + 8192, 512);
  F.va = trfrag(pa + 16384, 512); F.vb = trfrag(pb + 16384, 512);
  F.hx = lds128(L + (o.xh ^ (kk * 64)));
  sb();
}
template <bool HV, int H, bool NEXT> DEV void a_step(const Ctx& c, AAcc& A, const AF& F, AF& N, unsigned rslot, int kk, const AOfs& o,
                                                     const AAddr& a, int sn, unsigned slot) {
  LAS char* L = c.L + rslot;
  LAS char* pa = L + A_W + o.wA + kk * 4096;
  LAS char* pb = L + A_W + o.wB + kk * 4096;
  LAS char* px = L + (o.xo ^ (kk * 64));
  sb();
  A.q[0][0] = mfmaX(F.qa, F.x0, A.q[0][0]); sb();
  if (NEXT) N.qa = trfrag(pa, 512);
  sb();
  A.q[1][0] = mfmaX(F.qa, F.x1, A.q[1][0]); sb();
  if (NEXT) { N.x0 = lds128(px); N.x1 = lds128(px + 2048); }
  sb();
  A.q[0][1] = mfmaX(F.qb, F.x0, A.q[0][1]); sb();
  if (NEXT) N.qb = trfrag(pb, 512);
  sb();
  A.q[1][1] = mfmaX(F.qb, F.x1, A.q[1][1]); sb();
  if (NEXT) N.ka = trfrag(pa + 8192, 512);
  sb();
  A.k[0][0] = mfmaX(F.ka, F.x0, A.k[0][0]); sb();
  if (NEXT) N.kb = trfrag(pb + 8192, 512);
  sb();
  A.k[1][0] = mfmaX(F.ka, F.x1, A.k[1][0]); sb();
  if (NEXT) N.va = trfrag(pa + 16384, 512);
  sb();
  A.k[0][1] = mfmaX(F.kb, F.x0, A.k[0][1]); sb();
  if (NEXT) N.vb = trfrag(pb + 16384, 512);
  sb();
  A.k[1][1] = mfmaX(F.kb, F.x1, A.k[1][1]); sb();
  if (NEXT) N.hx = lds128(L + (o.xh ^ (kk * 64)));
  sb();
  A.v[0][0] = mfmaX(F.x0, F.va, A.v[0][0]); sb();
  if (H >= 0) a_dma<(H < 0 ? 0 : H), 0>(c, a, sn, slot);
  sb();
  A.v[1][0] = mfmaX(F.x1, F.va, A.v[1][0]);
  A.v[0][1] = mfmaX(F.x0, F.vb, A.v[0][1]); sb();
  if (H >= 0) a_dma<(H < 0 ? 0 : H), 1>(c, a, sn, slot);
  sb();
  A.v[1][1] = mfmaX(F.x1, F.vb, A.v[1][1]);
  if (HV) {
    A.h = mfmaX(F.hx, F.va, A.h);
  } else {
    A.h = mfmaX(F.ka, F.hx, A.h);
  }
  sb();
  if (H >= 0) a_dma<(H < 0 ? 0 : H), 2>(c, a, sn, slot);
  sb();
  if (!HV) A.qc = mfmaX(F.qa, F.hx, A.qc);
  sb();
}

template <bool HV> DEV void a_loop(const Ctx& c, AAcc& A, const AAddr& a, const AOfs& o, u32x4& g, unsigned gofs) {
  AF F0, F1;
  wait_vm<9 + (PF & 1)>();
  bar();
  a_read(c, F0, 0, 0, o);
  unsigned s0 = 0, s1 = A_SLOT, s2 = 2 * A_SLOT;
#pragma unroll 1
  for (int s = 0; s < 5; ++s) {
    a_step<HV, 1, true>(c, A, F0, F1, s0, 1, o, a, s + 2, s2);
    wait_vm<6>();
    bar();
    STAMP1(16 + s);
    a_step<HV, 0, true>(c, A, F1, F0, s1, 0, o, a, s + 3, s0);
    unsigned t = s0; s0 = s1; s1 = s2; s2 = t;
  }
  a_step<HV, 1, true>(c, A, F0, F1, 2 * A_SLOT, 1, o, a, 7, A_SLOT);
  wait_vm<6>();
  bar();
  g = aload128c(gofs, c.ws + WS_XS);
  a_step<HV, -1, true>(c, A, F1, F0, 0, 0, o, a, 0, 0);
  a_step<HV, -1, true>(c, A, F0, F1, 0, 1, o, a, 0, 0);
  asm volatile("s_waitcnt vmcnt(0) ; TIE %0" : "+v"(g) : : "memory");
  bar();
  a_step<HV, -1, true>(c, A, F1, F0, A_SLOT, 0, o, a, 0, 0);
  a_step<HV, -1, true>(c, A, F0, F1, A_SLOT, 1, o, a, 0, 0);
  a_step<HV, -1, false>(c, A, F1, F0, 0, 0, o, a, 0, 0);
}

DEV void job_a(Ctx& c, int i, int hc, int s0row, const G3A& g3, int rt, int half, int xcol) {
  u32x4 xr; u32x2 ev;
  s0_issue(c, xr, ev, s0row);
  char* ws = c.ws;
  const int lane = c.lane, w = c.w, rg = w & 3, cg = w >> 2;
  const int t = lane & 15, q = lane >> 4;
  AAddr a;
  {
    int r0 = 16 * w + (lane >> 3);
    a.vx0 = r0 * 1024 + (((lane & 7) ^ ((r0 >> 1) & 7)) << 4);
    int r1 = r0 + 8;
    a.vx1 = r1 * 1024 + (((lane & 7) ^ ((r1 >> 1) & 7)) << 4);
    a.bx = c.x + (size_t)i * 128 * 1024;
    int rh = 2 * w + ((lane >> 3) & 1);
    a.vh = rh * 1024 + (((lane & 7) ^ w) << 4);
    a.bh = c.x + (size_t)(i > 0 ? i * 128 - 16 : 0) * 1024;
    a.hmask = w == 0 ? 0xff00ull : 0xffffull;
    int k = 8 * w + (lane >> 3);
    int f = ((k >> 1) & 1) | (((k >> 3) & 1) << 1);
    a.vw = k * 3072 + (((lane & 7) ^ (f << 1)) << 4);
    a.bw = c.wqkv + hc * 128;
  }
  if (c.tid < 24) {
    u32x4 ones = {0x3f803f80u, 0x3f803f80u, 0x3f803f80u, 0x3f803f80u};
    *(LAS u32x4*)(c.L + (c.tid >> 3) * A_SLOT + 128 * 128 + (c.tid & 7) * 16) = ones;
  }
  a_half<0>(c, a, 0, 0); a_half<1>(c, a, 0, 0);
  a_half<0>(c, a, 1, A_SLOT); a_half<1>(c, a, 1, A_SLOT);
  s0_finish<12>(c, xr, ev, s0row);
  STAMP(15);
  a_half<0>(c, a, 2, 2 * A_SLOT);
  if (PF & 1) tlb_touch(c);

  const unsigned xlane = t * 128 + ((q ^ (t >> 1)) << 4);
  const unsigned xo = rg * 32 * 128 + xlane, xh = 128 * 128 + xlane;
  const int f = ((t >> 3) & 1) | ((q & 1) << 1);
  const int hA = 2 * cg + (rg & 1), hB = hA ^ 1;
  const unsigned wl = (8 * q + (t >> 2)) * 128 + (t & 3) * 8;
  const unsigned wA = wl + ((hA ^ f) << 5), wB = wl + ((hB ^ f) << 5);
  const int gj = c.tid < 144 ? c.tid : 143;
  const int gtok = gj < 128 ? i * 128 + gj : (i > 0 ? i * 128 - 16 + (gj - 128) : gj - 128);
  const unsigned gofs = gtok * 16;

  AAcc A;
  const f32x4 z4 = {0.f, 0.f, 0.f, 0.f};
#pragma unroll
  for (int x = 0; x < 2; ++x)
#pragma unroll
    for (int y = 0; y < 2; ++y) { A.q[x][y] = z4; A.k[x][y] = z4; A.v[x][y] = z4; }
  A.h = z4; A.qc = z4;
  u32x4 g = {0, 0, 0, 0};
  const bool hv = rg >= 2;
  const AOfs ao = {xo, xh, wA, wB};
  if (hv) a_loop<true>(c, A, a, ao, g, gofs);
  else a_loop<false>(c, A, a, ao, g, gofs);
  STAMP(2);
  g = settle_pair(g, c.rs, WS_XS + gofs, true, c.Tlo);
  if (c.tid < 144) { u32x2 mr = {g.x, g.z}; *(LAS u32x2*)(c.L + T_XS + c.tid * 8) = mr; }
  if (!hv) {
    if (t == 0) {
      *(LAS f32x4*)(c.L + T_CS + (64 + hA * 16 + 4 * q) * 4) = A.h;
      *(LAS f32x4*)(c.L + T_CS + (hA * 16 + 4 * q) * 4) = A.qc;
    }
  } else if (lane < 16) {
    *(LAS float*)(c.L + T_CS + (128 + hA * 16 + lane) * 4) = A.h[0];
  }
  bar();
  g3_prefetch_w(c, g3);
  if (PF & 2) l2_warm(c, rt, (rt & 7) * 2 + half, half, xcol);

  u32x2 Q[2][2], K[2][2], V[2][2], H;
  {
    f32x4 csq[2], csk[2]; float csv[2];
#pragma unroll
    for (int y = 0; y < 2; ++y) {
      int hh = y ? hB : hA;
      csq[y] = *(LAS f32x4*)(c.L + T_CS + (hh * 16 + 4 * q) * 4);
      csk[y] = *(LAS f32x4*)(c.L + T_CS + (64 + hh * 16 + 4 * q) * 4);
      csv[y] = *(LAS float*)(c.L + T_CS + (128 + hh * 16 + t) * 4);
    }
#pragma unroll
    for (int x = 0; x < 2; ++x) {
      f32x2 mr = *(LAS f32x2*)(c.L + T_XS + (rg * 32 + x * 16 + t) * 8);
      f32x4 m01 = *(LAS f32x4*)(c.L + T_XS + (rg * 32 + x * 16 + 4 * q) * 8);
      f32x4 m23 = *(LAS f32x4*)(c.L + T_XS + (rg * 32 + x * 16 + 4 * q + 2) * 8);
      const float rq = mr.y * 0.25f;
#pragma unroll
      for (int y = 0; y < 2; ++y) {
        f32x4 aq = A.q[x][y], ak = A.k[x][y], av = A.v[x][y];
        Q[x][y].x = pk2(rq * (aq[0] - mr.x * csq[y][0]), rq * (aq[1] - mr.x * csq[y][1]));
        Q[x][y].y = pk2(rq * (aq[2] - mr.x * csq[y][2]), rq * (aq[3] - mr.x * csq[y][3]));
        K[x][y].x = pk2(mr.y * (ak[0] - mr.x * csk[y][0]), mr.y * (ak[1] - mr.x * csk[y][1]));
        K[x][y].y = pk2(mr.y * (ak[2] - mr.x * csk[y][2]), mr.y * (ak[3] - mr.x * csk[y][3]));
        V[x][y].x = pk2(m01[1] * (av[0] - m01[0] * csv[y]), m01[3] * (av[1] - m01[2] * csv[y]));
        V[x][y].y = pk2(m23[1] * (av[2] - m23[0] * csv[y]), m23[3] * (av[3] - m23[2] * csv[y]));
      }
    }
    if (!hv) {
      f32x2 mr = *(LAS f32x2*)(c.L + T_XS + (128 + t) * 8);
      H.x = pk2(mr.y * (A.h[0] - mr.x * csk[0][0]), mr.y * (A.h[1] - mr.x * csk[0][1]));
      H.y = pk2(mr.y * (A.h[2] - mr.x * csk[0][2]), mr.y * (A.h[3] - mr.x * csk[0][3]));
    } else {
      f32x4 m01 = *(LAS f32x4*)(c.L + T_XS + (128 + 4 * q) * 8);
      f32x4 m23 = *(LAS f32x4*)(c.L + T_XS + (128 + 4 * q + 2) * 8);
      H.x = pk2(m01[1] * (A.h[0] - m01[0] * csv[0]), m01[3] * (A.h[1] - m01[2] * csv[0]));
      H.y = pk2(m23[1] * (A.h[2] - m23[0] * csv[0]), m23[3] * (A.h[3] - m23[2] * csv[0]));
    }
  }
  const unsigned KX = G_Z, VX = G_SLOT + G_Z;
  {
    *(LAS u32x2*)(c.L + (hv ? VX : KX) + hA * 512 + lane * 8) = H;
    if (rg < 3) {
      *(LAS u32x2*)(c.L + KX + ((rg + 1) * 4 + hA) * 512 + lane * 8) = K[1][0];
      *(LAS u32x2*)(c.L + KX + ((rg + 1) * 4 + hB) * 512 + lane * 8) = K[1][1];
      *(LAS u32x2*)(c.L + VX + ((rg + 1) * 4 + hA) * 512 + lane * 8) = V[1][0];
      *(LAS u32x2*)(c.L + VX + ((rg + 1) * 4 + hB) * 512 + lane * 8) = V[1][1];
    }
  }
  bar();
  u32x2 Kp[2], Vp[2];
  Kp[0] = lds64(c.L + KX + (rg * 4 + hA) * 512 + lane * 8);
  Kp[1] = lds64(c.L + KX + (rg * 4 + hB) * 512 + lane * 8);
  Vp[0] = lds64(c.L + VX + (rg * 4 + hA) * 512 + lane * 8);
  Vp[1] = lds64(c.L + VX + (rg * 4 + hB) * 512 + lane * 8);

  const float NEG = -1e30f, LOG2E = 1.4426950408889634f;
  float bc[4], bp[4];
#pragma unroll
  for (int j = 0; j < 4; ++j) {
    int d = t - (4 * q + j);
    bc[j] = (d >= 0 && !(d & 1)) ? 0.f : NEG;
    int dp = d + 16;
    bp[j] = (dp <= 14 && !(dp & 1)) ? 0.f : NEG;
  }
  const bool first = (i == 0 && rg == 0);
#pragma unroll
  for (int x = 1; x >= 0; --x) {
    u32x2 O[2];
#pragma unroll
    for (int y = 0; y < 2; ++y) {
      u32x2 kp = x ? K[0][y] : Kp[y], vp = x ? V[0][y] : Vp[y];
      f32x4 sp = mfma16(kp, Q[x][y], z4), sc = mfma16(K[x][y], Q[x][y], z4);
      float e[8];
#pragma unroll
      for (int j = 0; j < 4; ++j) {
        e[j] = __builtin_fmaf(sp[j], LOG2E, (x == 0 && first) ? NEG : bp[j]);
        e[4 + j] = __builtin_fmaf(sc[j], LOG2E, bc[j]);
      }
      float mx = fmaxf(fmaxf(fmaxf(e[0], e[1]), fmaxf(e[2], e[3])), fmaxf(fmaxf(e[4], e[5]), fmaxf(e[6], e[7])));
      mx = maxq(mx);
      float sum = 0.f;
#pragma unroll
      for (int j = 0; j < 8; ++j) { e[j] = __builtin_amdgcn_exp2f(e[j] - mx); sum += e[j]; }
      float inv = __builtin_amdgcn_rcpf(sumq(sum));
      u32x4 P = {pk2(e[0] * inv, e[1] * inv), pk2(e[2] * inv, e[3] * inv), pk2(e[4] * inv, e[5] * inv), pk2(e[6] * inv, e[7] * inv)};
      u32x4 VV = {vp.x, vp.y, V[x][y].x, V[x][y].y};
      f32x4 o = mfma32(VV, P, z4);
      O[y].x = pk2(o[0], o[1]); O[y].y = pk2(o[2], o[3]);
    }
    u32x4 fr = pairswap(O[0], O[1]);
    int qp = (((q & 1) << 1) | (q >> 1)) ^ ((rg & 1) << 1);
    unsigned off = WS_O + ((i * 8 + rg * 2 + x) * 16 + 2 * hc + cg) * 1024 + qp * 256 + t * 16;
    __builtin_amdgcn_raw_buffer_store_b128(fr, c.rs, off, 0, 16);
  }
  wait_vm<0>();
  bar();
  if (c.tid == 0) gstore64(ws + WS_FO + (i * 8 + hc) * 128, tagT(c));
  STAMP(3);
}

struct ZAddr { unsigned vx0, vx1, vw; const char *bx, *bw; };
template <int S, int H, int P> DEV void z_dma(const Ctx& c, const ZAddr& a) {
  if (S >= 16 || S < 0) return;
  const unsigned l = c.lb + (S % 5) * Z_SLOT;
  const char* bw = a.bw + (size_t)(S & 7) * 64 * 2752 + (S < 8 ? 1376 : 0);
  if (S < 8) {
    if (H == 0) dma16(P ? a.vx1 : a.vx0, a.bx + S * 128, l + c.w * 2048 + P * 1024);
    else dma16m(a.vw, bw + P * 4 * 2752, l + Z_W + c.w * 1536 + P * 768, 0xffffffffffffull);
  } else if (P == 0) {
    dma16m(a.vw, bw + H * 4 * 2752, l + Z_W + c.w * 1536 + H * 768, 0xffffffffffffull);
  }
}
template <int S, int H> DEV void z_half(const Ctx& c, const ZAddr& a) { z_dma<S, H, 0>(c, a); z_dma<S, H, 1>(c, a); }
constexpr int z_n(int s) { return s >= 16 ? 0 : (s < 8 ? 4 : 2); }
constexpr int z_wait(int s) { return z_n(s + 1) + z_n(s + 2) + z_n(s + 3); }

struct ZW { u32x4 f[3]; };
template <int S, int KK> DEV void z_read_w(const Ctx& c, ZW& F, const unsigned (&wb)[3]) {
  LAS char* L = c.L + (S % 5) * Z_SLOT;
#pragma unroll
  for (int j = 0; j < 3; ++j) F.f[j] = trfrag(L + Z_W + wb[j] + KK * 6144, 768);
}
template <int S, int KK> DEV void z_read_x(const Ctx& c, u32x4 (&xf)[2][16], unsigned xo) {
  LAS char* L = c.L + (S % 5) * Z_SLOT;
  xf[0][2 * S + KK] = lds128(L + (xo ^ (KK * 64)));
  xf[1][2 * S + KK] = lds128(L + (xo ^ (KK * 64)) + 2048);
}
template <bool SWAP, int J, int RS, int RK, int SN, int H>
DEV void z_step(const Ctx& c, const ZAddr& a, u32x4 (&xf)[2][16], const ZW& F, ZW& N, f32x4 (&acc)[2][3], f32x4& cs, unsigned xo,
                const unsigned (&wb)[3]) {
  const u32x4 ones = {0x3f803f80u, 0x3f803f80u, 0x3f803f80u, 0x3f803f80u};
  constexpr int R = RS < 0 ? 0 : RS, JN = (2 * R + RK) & 15;
  LAS char* L = c.L + (R % 5) * Z_SLOT;
  auto mm = [&](int x, int j) { acc[x][j] = SWAP ? mfmaX(F.f[j], xf[x][J], acc[x][j]) : mfmaX(xf[x][J], F.f[j], acc[x][j]); };
  sb();
  mm(0, 0); sb();
  if (RS >= 0) N.f[0] = trfrag(L + Z_W + wb[0] + RK * 6144, 768);
  sb();
  mm(1, 0); sb();
  if (RS >= 0 && !SWAP) { xf[0][JN] = lds128(L + (xo ^ (RK * 64))); xf[1][JN] = lds128(L + (xo ^ (RK * 64)) + 2048); }
  if (RS >= 0 && SWAP) N.f[1] = trfrag(L + Z_W + wb[1] + RK * 6144, 768);
  sb();
  mm(0, 1); sb();
  if (RS >= 0 && !SWAP) N.f[1] = trfrag(L + Z_W + wb[1] + RK * 6144, 768);
  if (RS >= 0 && SWAP) N.f[2] = trfrag(L + Z_W + wb[2] + RK * 6144, 768);
  sb();
  mm(1, 1); sb();
  if (RS >= 0 && !SWAP) N.f[2] = trfrag(L + Z_W + wb[2] + RK * 6144, 768);
  sb();
  mm(0, 2); sb();
  z_dma<SN, H, 0>(c, a);
  sb();
  mm(1, 2); sb();
  z_dma<SN, H, 1>(c, a);
  sb();
  cs = SWAP ? mfmaX(F.f[0], ones, cs) : mfmaX(ones, F.f[0], cs);
  sb();
}
template <int S> DEV void z_stage_a(const Ctx& c, const ZAddr& a, u32x4 (&xf)[2][16], ZW& F0, ZW& F1, f32x4 (&acc)[2][3], f32x4& cs,
                                    unsigned xo, const unsigned (&wb)[3], u32x4& g, unsigned gofs) {
  z_step<false, 2 * S, S, 1, (S == 0 ? -1 : S + 4), 1>(c, a, xf, F0, F1, acc, cs, xo, wb);
  if (S == 7) asm volatile("s_waitcnt vmcnt(4) ; TIE %0" : "+v"(g) : : "memory");
  else wait_vm<z_wait(S + 1)>();
  bar();
  STAMP1(16 + S);
  if (S == 5) g = aload128c(gofs, c.ws + WS_XS);
  z_step<false, 2 * S + 1, (S < 7 ? S + 1 : -1), 0, S + 5, 0>(c, a, xf, F1, F0, acc, cs, xo, wb);
}
template <int S> DEV void z_stage_b(const Ctx& c, const ZAddr& a, u32x4 (&xf)[2][16], ZW& F0, ZW& F1, f32x4 (&acc)[2][3], f32x4& cs,
                                    const unsigned (&wb)[3], u32x4 (&g)[2], unsigned gofs, const char* gbase) {
  z_step<true, 2 * (S - 8), S, 1, S + 4, 1>(c, a, xf, F0, F1, acc, cs, 0, wb);
  if (S < 15) {
    wait_vm<z_wait(S + 1)>();
    bar();
    if (S == 14) { g[0] = aload128c(gofs, gbase); g[1] = aload128c(gofs, gbase + 2048); }
  }
  z_step<true, 2 * (S - 8) + 1, (S < 15 ? S + 1 : -1), 0, S + 5, 0>(c, a, xf, F1, F0, acc, cs, 0, wb);
  if (S == 15) asm volatile("s_waitcnt vmcnt(0) ; TIE %0 %1" : "+v"(g[0]), "+v"(g[1]) : : "memory");
}

DEV int fb_start(int c) { return c < 3 ? 6 * c : 18 + 5 * (c - 3); }

DEV void job_z(Ctx& c, int i, int zc, int s0row, const G3A& g3) {
  u32x4 xr; u32x2 ev;
  s0_issue(c, xr, ev, s0row);
  char* ws = c.ws;
  const int lane = c.lane, w = c.w, rg = w & 3, cg = w >> 2;
  const int t = lane & 15, q = lane >> 4;
  const int fb0 = fb_start(zc), nb = zc < 3 ? 6 : 5;
  ZAddr a;
  {
    int r0 = 16 * w + (lane >> 3);
    a.vx0 = r0 * 1024 + (((lane & 7) ^ ((r0 >> 1) & 7)) << 4);
    int r1 = r0 + 8;
    a.vx1 = r1 * 1024 + (((lane & 7) ^ ((r1 >> 1) & 7)) << 4);
    a.bx = c.x + (size_t)i * 128 * 1024;
    int l48 = lane < 48 ? lane : 47;
    int kr = l48 / 12, cc = l48 % 12;
    int b = (cc >> 1) ^ (w & 1);
    int fbg = fb0 + b; fbg = fbg > 42 ? 42 : fbg;
    a.vw = (8 * w + kr) * 2752 + fbg * 32 + (cc & 1) * 16;
    a.bw = c.win;
  }
  z_half<0, 0>(c, a); z_half<0, 1>(c, a); z_half<1, 0>(c, a); z_half<1, 1>(c, a); z_half<2, 0>(c, a); z_half<2, 1>(c, a);
  s0_finish<12>(c, xr, ev, s0row);
  STAMP(15);
  z_half<3, 0>(c, a); z_half<3, 1>(c, a); z_half<4, 0>(c, a); z_half<4, 1>(c, a);
  if (PF & 1) tlb_touch(c);

  const unsigned xo = rg * 32 * 128 + t * 128 + ((q ^ (t >> 1)) << 4);
  unsigned wb[3]; int lb3[3];
#pragma unroll
  for (int j = 0; j < 3; ++j) {
    int jr = j + (rg == 3 ? 0 : rg); jr = jr >= 3 ? jr - 3 : jr;
    lb3[j] = 3 * cg + jr;
    wb[j] = (8 * q + (t >> 2)) * 192 + ((lb3[j] ^ (q & 1)) << 5) + (t & 3) * 8;
  }
  const int gj = c.tid < 128 ? c.tid : 127;
  const unsigned gofs = (i * 128 + gj) * 16;
  const f32x4 z4 = {0.f, 0.f, 0.f, 0.f};
  u32x4 xf[2][16];
  f32x4 acc[2][3], cs = z4;
#pragma unroll
  for (int j = 0; j < 3; ++j) { acc[0][j] = z4; acc[1][j] = z4; }
  u32x4 g = {0, 0, 0, 0};
  ZW F0, F1;
  wait_vm<16 + (PF & 1)>();
  bar();
  z_read_x<0, 0>(c, xf, xo); z_read_w<0, 0>(c, F0, wb);
  z_stage_a<0>(c, a, xf, F0, F1, acc, cs, xo, wb, g, gofs); z_stage_a<1>(c, a, xf, F0, F1, acc, cs, xo, wb, g, gofs);
  z_stage_a<2>(c, a, xf, F0, F1, acc, cs, xo, wb, g, gofs); z_stage_a<3>(c, a, xf, F0, F1, acc, cs, xo, wb, g, gofs);
  z_stage_a<4>(c, a, xf, F0, F1, acc, cs, xo, wb, g, gofs); z_stage_a<5>(c, a, xf, F0, F1, acc, cs, xo, wb, g, gofs);
  z_stage_a<6>(c, a, xf, F0, F1, acc, cs, xo, wb, g, gofs); z_stage_a<7>(c, a, xf, F0, F1, acc, cs, xo, wb, g, gofs);
  asm volatile("" : "+v"(acc[0][0]), "+v"(acc[0][1]), "+v"(acc[0][2]), "+v"(acc[1][0]), "+v"(acc[1][1]), "+v"(acc[1][2]), "+v"(cs));
  STAMP(2);
  g = settle_pair(g, c.rs, WS_XS + gofs, true, c.Tlo);
  if (c.tid < 128) { u32x2 mr = {g.x, g.z}; *(LAS u32x2*)(c.L + T_XS + c.tid * 8) = mr; }
  if (rg < 3 && lane < 16) *(LAS float*)(c.L + T_CS + (lb3[0] * 16 + lane) * 4) = cs[0];
  bar();
  u32x2 Z2[2][3];
  {
    float csf[3];
#pragma unroll
    for (int j = 0; j < 3; ++j) csf[j] = *(LAS float*)(c.L + T_CS + (lb3[j] * 16 + t) * 4);
#pragma unroll
    for (int x = 0; x < 2; ++x) {
      f32x4 m01 = *(LAS f32x4*)(c.L + T_XS + (rg * 32 + x * 16 + 4 * q) * 8);
      f32x4 m23 = *(LAS f32x4*)(c.L + T_XS + (rg * 32 + x * 16 + 4 * q + 2) * 8);
      float s1[4] = {0.f, 0.f, 0.f, 0.f}, s2[4] = {0.f, 0.f, 0.f, 0.f};
#pragma unroll
      for (int j = 0; j < 3; ++j) {
        f32x4 v = acc[x][j];
        float y0 = gelu_t(m01[1] * (v[0] - m01[0] * csf[j])), y1 = gelu_t(m01[3] * (v[1] - m01[2] * csf[j]));
        float y2 = gelu_t(m23[1] * (v[2] - m23[0] * csf[j])), y3 = gelu_t(m23[3] * (v[3] - m23[2] * csf[j]));
        unsigned p0 = pk2(y0, y1), p1 = pk2(y2, y3);
        Z2[x][j].x = p0; Z2[x][j].y = p1;
        if (lb3[j] < nb) {
          y0 = blo(p0); y1 = bhi(p0); y2 = blo(p1); y3 = bhi(p1);
          s1[0] += y0; s1[1] += y1; s1[2] += y2; s1[3] += y3;
          s2[0] = __builtin_fmaf(y0, y0, s2[0]); s2[1] = __builtin_fmaf(y1, y1, s2[1]);
          s2[2] = __builtin_fmaf(y2, y2, s2[2]); s2[3] = __builtin_fmaf(y3, y3, s2[3]);
        }
      }
#pragma unroll
      for (int r = 0; r < 4; ++r) { s1[r] = sum16(s1[r]); s2[r] = sum16(s2[r]); }
      if (t == 0) {
        f32x4 o0 = {s1[0], s2[0], s1[1], s2[1]}, o1 = {s1[2], s2[2], s1[3], s2[3]};
        LAS char* p = c.L + T_ZP + (cg * 128 + rg * 32 + x * 16 + 4 * q) * 8;
        *(LAS f32x4*)p = o0; *(LAS f32x4*)(p + 16) = o1;
      }
    }
  }
  bar();
  if (c.tid < 256) {
    int tok = c.tid >> 1, st = c.tid & 1;
    float v = *(LAS float*)(c.L + T_ZP + tok * 8 + st * 4) + *(LAS float*)(c.L + T_ZP + (128 + tok) * 8 + st * 4);
    gstore64(ws + WS_ZS + ((i * 8 + zc) * 128 + tok) * 16 + st * 8, ((u64)c.Tlo << 32) | __float_as_uint(v));
  }
  STAMP(3);
  cs = z4;
#pragma unroll
  for (int j = 0; j < 3; ++j) { acc[0][j] = z4; acc[1][j] = z4; }
  u32x4 gs[2] = {{0, 0, 0, 0}, {0, 0, 0, 0}};
  const int stok = c.tid & 127, pg = c.tid >> 7;
  const unsigned sofs = ((2 * pg) * 128 + stok) * 16;
  const char* sbase = ws + WS_ZS + (size_t)i * 8 * 128 * 16;
  z_read_w<8, 0>(c, F0, wb);
  z_stage_b<8>(c, a, xf, F0, F1, acc, cs, wb, gs, sofs, sbase);  z_stage_b<9>(c, a, xf, F0, F1, acc, cs, wb, gs, sofs, sbase);
  z_stage_b<10>(c, a, xf, F0, F1, acc, cs, wb, gs, sofs, sbase); z_stage_b<11>(c, a, xf, F0, F1, acc, cs, wb, gs, sofs, sbase);
  z_stage_b<12>(c, a, xf, F0, F1, acc, cs, wb, gs, sofs, sbase); z_stage_b<13>(c, a, xf, F0, F1, acc, cs, wb, gs, sofs, sbase);
  z_stage_b<14>(c, a, xf, F0, F1, acc, cs, wb, gs, sofs, sbase); z_stage_b<15>(c, a, xf, F0, F1, acc, cs, wb, gs, sofs, sbase);
  asm volatile("" : "+v"(acc[0][0]), "+v"(acc[0][1]), "+v"(acc[0][2]), "+v"(acc[1][0]), "+v"(acc[1][1]), "+v"(acc[1][2]), "+v"(cs));
  STAMP(4);
  {
    const unsigned so = WS_ZS + i * 8 * 128 * 16 + sofs;
    gs[0] = settle_pair(gs[0], c.rs, so, true, c.Tlo);
    gs[1] = settle_pair(gs[1], c.rs, so + 2048, true, c.Tlo);
    f32x2 p = {__uint_as_float(gs[0].x) + __uint_as_float(gs[1].x), __uint_as_float(gs[0].z) + __uint_as_float(gs[1].z)};
    *(LAS f32x2*)(c.L + T_ZQ + (pg * 128 + stok) * 8) = p;
  }
  if (rg < 3 && t == 0) *(LAS f32x4*)(c.L + T_CS + 512 + (lb3[0] * 16 + 4 * q) * 4) = cs;
  bar();
  g3_prefetch_w(c, g3);
  if (c.tid < 128) {
    f32x2 p0 = *(LAS f32x2*)(c.L + T_ZQ + c.tid * 8), p1 = *(LAS f32x2*)(c.L + T_ZQ + (128 + c.tid) * 8);
    f32x2 p2 = *(LAS f32x2*)(c.L + T_ZQ + (256 + c.tid) * 8), p3 = *(LAS f32x2*)(c.L + T_ZQ + (384 + c.tid) * 8);
    float s = (p0.x + p1.x) + (p2.x + p3.x), ss = (p0.y + p1.y) + (p2.y + p3.y);
    float m = s * (1.0f / 688.0f);
    float var = fmaxf(ss * (1.0f / 688.0f) - m * m, 0.f);
    f32x2 o = {m, 1.0f / sqrtf(var + 1e-5f)};
    *(LAS f32x2*)(c.L + T_ZF + c.tid * 8) = o;
  }
  bar();
  {
    const int qp = ((q & 1) << 1) | (q >> 1);
    f32x4 mr[2][2];
#pragma unroll
    for (int x = 0; x < 2; ++x) {
      mr[x][0] = *(LAS f32x4*)(c.L + T_ZF + (rg * 32 + x * 16 + 4 * q) * 8);
      mr[x][1] = *(LAS f32x4*)(c.L + T_ZF + (rg * 32 + x * 16 + 4 * q + 2) * 8);
    }
#pragma unroll
    for (int j = 0; j < 3; ++j) {
      u32x2 n[2];
#pragma unroll
      for (int x = 0; x < 2; ++x) {
        unsigned p0 = Z2[x][j].x, p1 = Z2[x][j].y;
        n[x].x = pk2((blo(p0) - mr[x][0][0]) * mr[x][0][1], (bhi(p0) - mr[x][0][2]) * mr[x][0][3]);
        n[x].y = pk2((blo(p1) - mr[x][1][0]) * mr[x][1][1], (bhi(p1) - mr[x][1][2]) * mr[x][1][3]);
      }
      u32x4 fr = pairswap(n[0], n[1]);
      if (lb3[j] < nb) {
        unsigned off = WS_Z2N + ((i * 4 + rg) * 44 + fb0 + lb3[j]) * 1024 + qp * 256 + t * 16;
        __builtin_amdgcn_raw_buffer_store_b128(fr, c.rs, off, 0, 16);
      }
    }
  }
  wait_vm<0>();
  bar();
  if (c.tid == 0) gstore64(ws + WS_FZ2 + (zc * 16 + i) * 128, tagT(c));
  STAMP(5);
  {
    f32x2 mr[2];
#pragma unroll
    for (int x = 0; x < 2; ++x) mr[x] = *(LAS f32x2*)(c.L + T_XS + (rg * 32 + x * 16 + t) * 8);
#pragma unroll
    for (int j = 0; j < 3; ++j) {
      f32x4 c4 = *(LAS f32x4*)(c.L + T_CS + 512 + (lb3[j] * 16 + 4 * q) * 4);
      u32x2 n[2];
#pragma unroll
      for (int x = 0; x < 2; ++x) {
        f32x4 v = acc[x][j];
        n[x].x = pk2(gelu_t(mr[x].y * (v[0] - mr[x].x * c4[0])), gelu_t(mr[x].y * (v[1] - mr[x].x * c4[1])));
        n[x].y = pk2(gelu_t(mr[x].y * (v[2] - mr[x].x * c4[2])), gelu_t(mr[x].y * (v[3] - mr[x].x * c4[3])));
      }
      u32x4 fr = pairswap(n[0], n[1]);
      if (lb3[j] < nb) {
        int fb = fb0 + lb3[j];
        unsigned off = WS_Z1 + ((i * 8 + rg * 2 + (q & 1)) * 22 + (fb >> 1)) * 1024 + (2 * (fb & 1) + (q >> 1)) * 256 + t * 16;
        __builtin_amdgcn_raw_buffer_store_b128(fr, c.rs, off, 0, 16);
      }
    }
  }
  wait_vm<0>();
  bar();
  if (c.tid == 0) gstore64(ws + WS_FZ1 + (i * 8 + zc) * 128, tagT(c));
  STAMP(6);
}

struct HAddr { unsigned va, vw; const char *bo, *bu, *bwo, *bwout; };
template <int T> DEV void h_issue_w(const Ctx& c, const HAddr& a) {
  const unsigned l = c.lb + ((32 + T) % 7) * G_SLOT + 8192 + c.w * 1024;
  if (T < 8) dma16(a.vw, a.bwo + (size_t)T * 64 * 1024, l);
  else if (T < 18) dma16(a.vw, a.bwout + (size_t)(T - 8) * 64 * 1024, l);
  else {
    int k = 8 * c.w + (c.lane >> 3);
    unsigned v = k > 47 ? a.vw - (k - 47) * 1024 : a.vw;
    dma16(v, a.bwout + (size_t)640 * 1024, l);
  }
}
template <int T> DEV void h_issue_a(const Ctx& c, const HAddr& a) {
  const unsigned l = c.lb + ((32 + T) % 7) * G_SLOT + c.w * 1024;
  if (T < 8) dma16c(a.va, a.bo + T * 2048, l);
  else dma16c(a.va, a.bu + (T - 8) * 2048, l);
}
struct HF { u32x4 b[2], f0[2], f1[2]; };
template <int T> DEV void h_read(const Ctx& c, HF& F, unsigned ao, unsigned w0, unsigned w1) {
  LAS char* L = c.L + ((32 + T) % 7) * G_SLOT;
#pragma unroll
  for (int kk = 0; kk < 2; ++kk) {
    F.b[kk] = lds128(L + ao + kk * 4096);
    F.f0[kk] = trfrag(L + 8192 + w0 + kk * 4096, 512);
    F.f1[kk] = trfrag(L + 8192 + w1 + kk * 4096, 512);
  }
  sb();
}
template <int T, int IA, int IW> DEV void h_mma(const Ctx& c, const HAddr& h, const HF& F, f32x4 (&acc)[2]) {
  if (IA >= 0) h_issue_a<(IA < 0 ? 0 : IA)>(c, h);
  sb();
  acc[0] = mfma32(F.f0[0], F.b[0], acc[0]);
  acc[1] = mfma32(F.f1[0], F.b[0], acc[1]);
  sb();
  if (IW >= 0) h_issue_w<(IW < 0 ? 0 : IW)>(c, h);
  sb();
  u32x4 b = F.b[1];
  if (T == 18) {
    if (c.lane >= 32) { u32x4 z = {0, 0, 0, 0}; b = z; }
  }
  acc[0] = mfma32(F.f0[1], b, acc[0]);
  acc[1] = mfma32(F.f1[1], b, acc[1]);
  sb();
}
template <int T, int IA, int IW, bool NEXT> DEV void h_it(const Ctx& c, const HAddr& h, HF& Fc, HF& Fn, f32x4 (&acc)[2],
                                                         unsigned ao, unsigned w0, unsigned w1) {
  bar();
  if (NEXT) h_read<T + 1>(c, Fn, ao, w0, w1);
  h_mma<T, IA, IW>(c, h, Fc, acc);
}

template <int V> struct IC { static constexpr int v = V; };
struct GF { u32x4 b0, b1, a0, a1, a2; };

DEV void job_g(Ctx& c, int rt, int gc, const G3A& g3, bool epoch_wg) {
  char* ws = c.ws;
  const int lane = c.lane, w = c.w;
  const int t = lane & 15, q = lane >> 4;
  const int rg = w & 1, cg = (w >> 1) & 1, kh = w >> 2;
  const int fb0 = fb_start(gc), nb = gc < 3 ? 6 : 5;
  const u64 T = tagT(c);
  float bias = blo(*(const GAS u16*)(size_t)(c.bsp + ((rt * 4 + rg * 2 + kh) * 16 + t) * 2));
  if (w == 0) poll_flags(ws + WS_FZ2 + (gc * 16 + (lane & 15)) * 128, lane < 16, T);
  asm volatile("" : "+v"(bias));
  bar();
  STAMP(8);
  const char* zb = ws + WS_Z2N + (size_t)fb0 * 1024;
  const unsigned vz1 = ((w / 6) * 44 + (w % 6)) * 1024 + lane * 16;
  const int u2 = 8 + (w >> 1);
  const unsigned vz2 = (44 + (u2 - 6)) * 1024 + (w & 1) * 512 + (lane & 31) * 16;
  const unsigned lz1 = c.lb + G_Z + w * 1024, lz2 = c.lb + G_Z + u2 * 1024 + (w & 1) * 512;
#pragma unroll
  for (int s = 0; s < 7; ++s) {
    dma16c(vz1, zb + (size_t)s * 88 * 1024, lz1 + s * G_SLOT);
    dma16mc(vz2, zb + (size_t)s * 88 * 1024, lz2 + s * G_SLOT, 0xffffffffull);
  }
  HAddr h;
  {
    h.va = ((w & 3) * 16 + (w >> 2)) * 1024 + lane * 16;
    int k = 8 * w + (lane >> 3);
    int f = ((k >> 1) & 1) | (((k >> 3) & 1) << 1);
    h.vw = k * 1024 + (((lane & 7) ^ (f << 1)) << 4);
    h.bo = ws + WS_O + (size_t)rt * 4 * 16 * 1024;
    h.bu = ws + WS_U + (size_t)rt * 4 * 22 * 1024;
    h.bwo = c.wo + gc * 128;
    h.bwout = c.wout + gc * 128;
  }
  const unsigned vau = ((w & 3) * 22 + (w >> 2)) * 1024 + lane * 16;
  const unsigned bo_ = rg * 32 * 128 + t * 128 + (((4 * kh + q) ^ (t >> 1)) << 4);
  const unsigned ao_ = G_Z + (kh * 6 + 3 * cg) * 1024 + lane * 16;
  const f32x4 z4 = {0.f, 0.f, 0.f, 0.f};
  f32x4 acc[2][3];
#pragma unroll
  for (int j = 0; j < 3; ++j) { acc[0][j] = z4; acc[1][j] = z4; }
  const int tbk = rt * 4 + rg * 2 + kh;
  const unsigned flofs = lane < 8 ? WS_FO + ((rt >> 1) * 8 + lane) * 128 : WS_FZ1 + ((rt >> 1) * 8 + gc) * 128;
  unsigned z1ofs[3];
#pragma unroll
  for (int j = 0; j < 3; ++j) {
    int fb = fb0 + 3 * cg + j; fb = fb > 42 ? 42 : fb;
    z1ofs[j] = (tbk * 22 + (fb >> 1)) * 1024 + (2 * (fb & 1) + (q >> 1)) * 256 + t * 16 + (q & 1) * 8;
  }
  unsigned slot = 0;
  const char* pw = g3.bw + 7 * 128;
  const char* pz = zb + (size_t)7 * 88 * 1024;
  auto rd = [&](GF& F, unsigned sl) {
    LAS char* L = c.L + sl;
    F.a0 = lds128(L + ao_); F.b0 = lds128(L + bo_); F.b1 = lds128(L + bo_ + 2048);
    F.a1 = lds128(L + ao_ + 1024); F.a2 = lds128(L + ao_ + 2048);
    sb();
  };
  auto it = [&](auto kind, auto ht, GF& Fc, GF& Fn, bool next) {
    constexpr int KIND = decltype(kind)::v, HT = decltype(ht)::v;
    bar();
    const unsigned nsl = slot == 6 * G_SLOT ? 0 : slot + G_SLOT;
    if (KIND == 0) dma16(g3.vw, pw, c.lb + slot + w * 1024);
    if (KIND == 1) h_issue_a<HT>(c, h);
    sb();
    if (next) rd(Fn, nsl);
    acc[0][0] = mfma32(Fc.a0, Fc.b0, acc[0][0]); acc[1][0] = mfma32(Fc.a0, Fc.b1, acc[1][0]);
    acc[0][1] = mfma32(Fc.a1, Fc.b0, acc[0][1]);
    sb();
    if (KIND == 0) dma16c(vz1, pz, lz1 + slot);
    if (KIND == 1) h_issue_w<HT>(c, h);
    sb();
    acc[1][1] = mfma32(Fc.a1, Fc.b1, acc[1][1]);
    acc[0][2] = mfma32(Fc.a2, Fc.b0, acc[0][2]); acc[1][2] = mfma32(Fc.a2, Fc.b1, acc[1][2]);
    sb();
    if (KIND == 0) { dma16mc(vz2, pz, lz2 + slot, 0xffffffffull); pw += 128; pz += 88 * 1024; }
    sb();
    slot = nsl;
  };
  const IC<0> K0; const IC<1> K1; const IC<2> K2;
  GF F0, F1;
  wait_vm<12>();
  bar();
  rd(F0, 0);
  wait_vm<10>(); it(K0, K0, F0, F1, true);
  wait_vm<11>(); it(K0, K0, F1, F0, true);
  wait_vm<12>(); it(K0, K0, F0, F1, true);
  wait_vm<13>(); it(K0, K0, F1, F0, true);
  wait_vm<14>(); it(K0, K0, F0, F1, true);
  wait_vm<15>(); it(K0, K0, F1, F0, true);
#pragma unroll 1
  for (int s = 6; s < 10; s += 2) { wait_vm<15>(); it(K0, K0, F0, F1, true); wait_vm<15>(); it(K0, K0, F1, F0, true); }
  u32x2 fl = aload64c(flofs, ws);
#pragma unroll 1
  for (int s = 10; s < 16; s += 2) { wait_vm<15>(); it(K0, K0, F0, F1, true); wait_vm<15>(); it(K0, K0, F1, F0, true); }
  asm volatile("; TIE %0" : "+v"(fl));
  if (!__all(fl.x == c.Tlo && fl.y == c.Thi)) poll_flags(ws + flofs, true, T);
  u32x2 z1v[3];
  z1v[0] = aload64c(z1ofs[0], ws + WS_Z1);
  z1v[1] = aload64c(z1ofs[1], ws + WS_Z1);
  z1v[2] = aload64c(z1ofs[2], ws + WS_Z1);
#pragma unroll 1
  for (int s = 16; s < 24; s += 2) { wait_vm<15>(); it(K0, K0, F0, F1, true); wait_vm<15>(); it(K0, K0, F1, F0, true); }
  wait_vm<15>(); it(K0, K0, F0, F1, true);
  wait_vm<15>(); it(K1, IC<0>(), F1, F0, true);
  wait_vm<14>(); it(K1, IC<1>(), F0, F1, true);
  wait_vm<13>(); it(K1, IC<2>(), F1, F0, true);
  wait_vm<12>(); it(K1, IC<3>(), F0, F1, true);
  wait_vm<11>(); it(K1, IC<4>(), F1, F0, true);
  wait_vm<10>(); it(K1, IC<5>(), F0, F1, true);
  it(K2, K0, F1, F0, false);
  asm volatile("; TIE %0 %1 %2" : "+v"(z1v[0]), "+v"(z1v[1]), "+v"(z1v[2]));
  STAMP(9);
  {
    const unsigned e01 = 3 * G_SLOT + w * 2048 + lane * 16, e2 = T_EX2 + w * 1024 + lane * 16;
    const unsigned p01 = 3 * G_SLOT + (w ^ 4) * 2048 + lane * 16, p2 = T_EX2 + (w ^ 4) * 1024 + lane * 16;
    f32x4 keep[3], send[3];
#pragma unroll
    for (int j = 0; j < 3; ++j) { keep[j] = kh ? acc[1][j] : acc[0][j]; send[j] = kh ? acc[0][j] : acc[1][j]; }
    *(LAS f32x4*)(c.L + e01) = send[0]; *(LAS f32x4*)(c.L + e01 + 1024) = send[1]; *(LAS f32x4*)(c.L + e2) = send[2];
    bar();
    keep[0] += *(LAS f32x4*)(c.L + p01); keep[1] += *(LAS f32x4*)(c.L + p01 + 1024); keep[2] += *(LAS f32x4*)(c.L + p2);
#pragma unroll
    for (int j = 0; j < 3; ++j) {
      unsigned g0 = pk2(keep[j][0] + bias, keep[j][1] + bias), g1 = pk2(keep[j][2] + bias, keep[j][3] + bias);
      unsigned a0 = z1v[j].x, a1 = z1v[j].y;
      u32x2 u;
      u.x = pk2(blo(a0) * blo(g0), bhi(a0) * bhi(g0));
      u.y = pk2(blo(a1) * blo(g1), bhi(a1) * bhi(g1));
      if (3 * cg + j < nb) __builtin_amdgcn_raw_buffer_store_b64(u, c.rs, WS_U + z1ofs[j], 0, 16);
    }
  }
  wait_vm<0>();
  bar();
  if (c.tid == 0) gstore64(ws + WS_FU + (rt * 8 + gc) * 128, T);
  STAMP(10);

  const int tb = w & 3, np = w >> 2;
  const unsigned ha = tb * 1024 + lane * 16;
  const int fw = ((t >> 3) & 1) | ((q & 1) << 1);
  const unsigned wl = (8 * q + (t >> 2)) * 128 + (t & 3) * 8;
  const unsigned hw0 = wl + (((2 * np) ^ fw) << 5), hw1 = wl + (((2 * np + 1) ^ fw) << 5);
  f32x4 sa[2] = {z4, z4};
  HF H0, H1;
  h_issue_a<6>(c, h); h_issue_w<6>(c, h);
  h_read<0>(c, H0, ha, hw0, hw1);
  h_it<0, 7, 7, true>(c, h, H0, H1, sa, ha, hw0, hw1);
  h_it<1, -1, 8, true>(c, h, H1, H0, sa, ha, hw0, hw1);
  h_it<2, -1, 9, true>(c, h, H0, H1, sa, ha, hw0, hw1);
  h_it<3, -1, 10, true>(c, h, H1, H0, sa, ha, hw0, hw1);
  h_it<4, -1, 11, true>(c, h, H0, H1, sa, ha, hw0, hw1);
  wait_vm<6>(); h_it<5, -1, 12, true>(c, h, H1, H0, sa, ha, hw0, hw1);
  wait_vm<5>(); h_it<6, -1, 13, true>(c, h, H0, H1, sa, ha, hw0, hw1);
  h_it<7, -1, 14, false>(c, h, H1, H0, sa, ha, hw0, hw1);
  asm volatile("" : "+v"(sa[0]), "+v"(sa[1]));
  STAMP(11);
  h.va = vau;
  if (w == 0) poll_flags(ws + WS_FU + (rt * 8 + (lane & 7)) * 128, lane < 8, T);
  if (epoch_wg && w == 1) {
#pragma unroll
    for (int j = 0; j < 2; ++j) {
      poll_flags(ws + WS_FZ2 + (j * 64 + lane) * 128, true, T);
      poll_flags(ws + WS_FO + (j * 64 + lane) * 128, true, T);
    }
    if (lane == 0) gstore64(ws + WS_EPOCH, T);
  }
  bar();
  STAMP(12);
  h_issue_a<8>(c, h); h_issue_a<9>(c, h); h_issue_a<10>(c, h); h_issue_a<11>(c, h); h_issue_a<12>(c, h); h_issue_a<13>(c, h);
  h_issue_a<14>(c, h);
  const int orow = rt * 64 + tb * 16 + t;
  const unsigned oofs = orow * 1024 + gc * 128 + np * 64 + (q & 1) * 32 + (q >> 1) * 16;
  f32x4 ga[2] = {z4, z4};
  wait_vm<6>();
  bar();
  h_read<8>(c, H0, ha, hw0, hw1);
  wait_vm<5>(); h_it<8, 15, 15, true>(c, h, H0, H1, ga, ha, hw0, hw1);
  wait_vm<6>(); h_it<9, 16, 16, true>(c, h, H1, H0, ga, ha, hw0, hw1);
  wait_vm<7>(); h_it<10, 17, 17, true>(c, h, H0, H1, ga, ha, hw0, hw1);
  wait_vm<8>(); h_it<11, 18, 18, true>(c, h, H1, H0, ga, ha, hw0, hw1);
  wait_vm<9>(); h_it<12, -1, -1, true>(c, h, H0, H1, ga, ha, hw0, hw1);
  u32x4 xres;
  asm volatile("global_load_dwordx4 %0, %1, %2" : "=v"(xres) : "v"(oofs), "s"(uni(c.x)) : "memory");
  wait_vm<9>(); h_it<13, -1, -1, true>(c, h, H1, H0, ga, ha, hw0, hw1);
  wait_vm<7>(); h_it<14, -1, -1, true>(c, h, H0, H1, ga, ha, hw0, hw1);
  wait_vm<5>(); h_it<15, -1, -1, true>(c, h, H1, H0, ga, ha, hw0, hw1);
  wait_vm<3>(); h_it<16, -1, -1, true>(c, h, H0, H1, ga, ha, hw0, hw1);
  wait_vm<1>(); h_it<17, -1, -1, true>(c, h, H1, H0, ga, ha, hw0, hw1);
  h_mma<18, -1, -1>(c, h, H0, ga);
  asm volatile("s_waitcnt vmcnt(0) ; TIE %0" : "+v"(xres) : : "memory");
  STAMP(13);
  {
    u32x2 S[2], G[2];
#pragma unroll
    for (int b = 0; b < 2; ++b) {
      S[b].x = pk2(sa[b][0], sa[b][1]); S[b].y = pk2(sa[b][2], sa[b][3]);
      G[b].x = pk2(ga[b][0], ga[b][1]); G[b].y = pk2(ga[b][2], ga[b][3]);
    }
    u32x4 s8 = pairswap(S[0], S[1]), g8 = pairswap(G[0], G[1]);
    u32x4 o;
#pragma unroll
    for (int j = 0; j < 4; ++j) {
      unsigned su = s8[j], gu = g8[j], xu = xres[j];
      float g0 = blo(gu), g1 = bhi(gu);
      float e0 = g0 * __builtin_amdgcn_rcpf(1.0f + __builtin_amdgcn_exp2f(g0 * -1.4426950408889634f));
      float e1 = g1 * __builtin_amdgcn_rcpf(1.0f + __builtin_amdgcn_exp2f(g1 * -1.4426950408889634f));
      o[j] = pk2(__builtin_fmaf(blo(su), e0, blo(xu)), __builtin_fmaf(bhi(su), e1, bhi(xu)));
    }
    if (OUTSC == 0) *(GAS u32x4*)(size_t)(c.out + oofs) = o;
    else {
      __amdgpu_buffer_rsrc_t ro = __builtin_amdgcn_make_buffer_rsrc((void*)c.out, 0, 2048 * 1024, 0x00020000);
      __builtin_amdgcn_raw_buffer_store_b128(o, ro, oofs, 0, OUTSC == 1 ? 16 : OUTSC == 2 ? 2 : 17);
    }
  }
  STAMP(14); STAMPC(31);
}

__global__ __launch_bounds__(512, 2) void fused_kernel(const char* x, const char* wqkv, const char* wo, const char* win, const char* wsp,
                                                       const char* bsp, const char* wout, char* out, char* ws) {
  extern __shared__ __attribute__((aligned(16))) char smem[];
  Ctx c;
  c.x = x; c.wqkv = wqkv; c.wo = wo; c.win = win; c.wsp = wsp; c.bsp = bsp; c.wout = wout; c.out = out; c.ws = ws;
  c.L = (LAS char*)smem;
  c.lb = (unsigned)(size_t)c.L;
  c.tid = threadIdx.x; c.lane = c.tid & 63; c.w = __builtin_amdgcn_readfirstlane(c.tid >> 6);
  c.rs = __builtin_amdgcn_make_buffer_rsrc((void*)ws, 0, WS_USED, 0x00020000);
  c.Tlo = 0; c.Thi = 0;
  c.bid = blockIdx.x;
  STAMP(0); STAMPC(30);
  const int bid = blockIdx.x, xcd = bid & 7, sl = bid >> 3;
  const int i = (xcd & 1) * 8 + (sl >> 2), rsel = sl & 3, col = 2 * (xcd >> 1) + (rsel & 1);
  const int r16 = (xcd >> 1) * 4 + rsel;
  const int rt = (xcd >> 1) * 8 + (sl >> 2), gc = (xcd & 1) * 4 + (sl & 3);
  const int s0row = i * 128 + r16 * 8 + c.w;
  const G3A g3 = g3_addr(c, rt);
  if (rsel < 2) job_a(c, i, col, s0row, g3, rt, rsel, xcd & 1);
  else job_z(c, i, col, s0row, g3);
  STAMP(7);
  job_g(c, rt, gc, g3, bid == 0);
  if (bid == MEASB && c.w == 0) {
    u64 d = MEAS == 99 ? (c.c1 - c.c0) / 10 : 8 * (c.tk - c.t0);
    u64 target = __builtin_amdgcn_s_memrealtime() + (d < 100000 ? d : 100000);
    for (int n = 0; n < 1000000; ++n) { if (__builtin_amdgcn_s_memrealtime() >= target) break; __builtin_amdgcn_s_sleep(4); }
  }
}

extern "C" void kernel_launch(void* const* d_in, const int* in_sizes, int n_in, void* d_out, int out_size, void* d_ws, size_t ws_size,
                              hipStream_t stream) {
  (void)in_sizes; (void)n_in; (void)out_size; (void)ws_size;
  (void)hipFuncSetAttribute((const void*)fused_kernel, hipFuncAttributeMaxDynamicSharedMemorySize, LDS_BYTES);
  fused_kernel<<<256, 512, LDS_BYTES, stream>>>((const char*)d_in[0], (const char*)d_in[1], (const char*)d_in[2], (const char*)d_in[4],
                                                (const char*)d_in[5], (const char*)d_in[6], (const char*)d_in[7], (char*)d_out, (char*)d_ws);
}
```

```cpp
#define PF 2
#include <hip/hip_runtime.h>
#define EXP 0
#define OUTSC 0

typedef unsigned short u16;
typedef unsigned long long u64;
typedef __bf16 bf16x8 __attribute__((ext_vector_type(8)));
typedef __bf16 bf16x2 __attribute__((ext_vector_type(2)));
typedef short s16x4 __attribute__((ext_vector_type(4)));
typedef float f32x4 __attribute__((ext_vector_type(4)));
typedef float f32x2 __attribute__((ext_vector_type(2)));
typedef unsigned u32x4 __attribute__((ext_vector_type(4)));
typedef unsigned u32x2 __attribute__((ext_vector_type(2)));
#define LAS __attribute__((address_space(3)))
#define GAS __attribute__((address_space(1)))
#define DEV __device__ __forceinline__

constexpr unsigned WS_EPOCH = 0;
constexpr unsigned WS_FZ2 = 4096;
constexpr unsigned WS_FZ1 = WS_FZ2 + 128 * 128;
constexpr unsigned WS_FO  = WS_FZ1 + 128 * 128;
constexpr unsigned WS_FU  = WS_FO + 128 * 128;
constexpr unsigned WS_XS  = 0x20000;
constexpr unsigned WS_ZS  = 0x30000;
constexpr unsigned WS_Z2N = 0x100000;
constexpr unsigned WS_Z1  = 0x400000;
constexpr unsigned WS_U   = 0x700000;
constexpr unsigned WS_O   = 0xA00000;
constexpr unsigned WS_DBG = 0xD00000;
constexpr unsigned WS_USED = 0xE00000;

constexpr unsigned LDS_BYTES = 160 * 1024;
constexpr unsigned A_SLOT = 43008, A_W = 18432;
constexpr unsigned Z_SLOT = 28672, Z_W = 16384;
constexpr unsigned G_SLOT = 20480, G_Z = 8192;
constexpr unsigned T_BASE = 143360;
constexpr unsigned T_EX2 = T_BASE;
constexpr unsigned T_XS = T_BASE + 8192;
constexpr unsigned T_CS = T_XS + 1280;
constexpr unsigned T_ZP = T_CS + 1024;
constexpr unsigned T_ZQ = T_ZP + 2048;
constexpr unsigned T_ZF = T_ZQ + 4096;
constexpr unsigned T_DUMMY = T_ZF + 1024;
static_assert(T_DUMMY + 256 <= LDS_BYTES, "lds");

constexpr unsigned SPIN_CAP = 200000;

#define STAMP1(k) do {} while (0)
#define STAMPC(k) do {} while (0)
#define STAMP(k) do {} while (0)

DEV const char* uni(const char* p) {
  u64 v = (u64)(size_t)p;
  unsigned lo = __builtin_amdgcn_readfirstlane((unsigned)v), hi = __builtin_amdgcn_readfirstlane((unsigned)(v >> 32));
  return (const char*)(size_t)(((u64)hi << 32) | lo);
}
DEV unsigned uni(unsigned v) { return __builtin_amdgcn_readfirstlane(v); }
struct Ctx0 { unsigned sink; };
DEV void dma16(unsigned voff, const char* sbase, unsigned lds) {
  asm volatile("s_mov_b32 m0, %2\n\ts_nop 0\n\tglobal_load_lds_dwordx4 %0, %1" ::"v"(voff), "s"(uni(sbase)), "s"(uni(lds)) : "memory");
}
DEV void dma16c(unsigned voff, const char* sbase, unsigned lds) {
  asm volatile("s_mov_b32 m0, %2\n\ts_nop 0\n\tglobal_load_lds_dwordx4 %0, %1 sc1" ::"v"(voff), "s"(uni(sbase)), "s"(uni(lds)) : "memory");
}
DEV void dma16m(unsigned voff, const char* sbase, unsigned lds, u64 mask) {
  asm volatile("s_mov_b32 m0, %2\n\ts_mov_b64 exec, %3\n\tglobal_load_lds_dwordx4 %0, %1\n\ts_mov_b64 exec, -1"
               ::"v"(voff), "s"(uni(sbase)), "s"(uni(lds)), "s"(mask) : "memory");
}
DEV void dma16mc(unsigned voff, const char* sbase, unsigned lds, u64 mask) {
  asm volatile("s_mov_b32 m0, %2\n\ts_mov_b64 exec, %3\n\tglobal_load_lds_dwordx4 %0, %1 sc1\n\ts_mov_b64 exec, -1"
               ::"v"(voff), "s"(uni(sbase)), "s"(uni(lds)), "s"(mask) : "memory");
}
DEV void touch(const Ctx0& c, const char* p) {
  asm volatile("s_mov_b32 m0, %1\n\ts_nop 0\n\tglobal_load_lds_dword %0, off" ::"v"(p), "s"(uni(c.sink)) : "memory");
}
template <int N> DEV void wait_vm() { asm volatile("s_waitcnt vmcnt(%0)" ::"n"(N) : "memory"); }
DEV void sb() { __builtin_amdgcn_sched_barrier(0); }
DEV void bar() { asm volatile("s_waitcnt lgkmcnt(0)\n\ts_barrier" ::: "memory"); }

template <int V = 0> DEV f32x4 mfmaX(u32x4 a, u32x4 b, f32x4 c) {
  return __builtin_amdgcn_mfma_f32_16x16x32_bf16(__builtin_bit_cast(bf16x8, a), __builtin_bit_cast(bf16x8, b), c, 0, 0, 0);
}
DEV f32x4 mfma32(u32x4 a, u32x4 b, f32x4 c) {
  return __builtin_amdgcn_mfma_f32_16x16x32_bf16(__builtin_bit_cast(bf16x8, a), __builtin_bit_cast(bf16x8, b), c, 0, 0, 0);
}
DEV f32x4 mfma16(u32x2 a, u32x2 b, f32x4 c) {
  return __builtin_amdgcn_mfma_f32_16x16x16bf16_1k(__builtin_bit_cast(s16x4, a), __builtin_bit_cast(s16x4, b), c, 0, 0, 0);
}
DEV u32x4 lds128(LAS char* p) { return *(LAS u32x4*)p; }
DEV u32x2 lds64(LAS char* p) { return *(LAS u32x2*)p; }
DEV u32x4 trfrag(LAS char* p, int off4) {
  s16x4 a = __builtin_amdgcn_ds_read_tr16_b64_v4i16((LAS s16x4*)p);
  s16x4 b = __builtin_amdgcn_ds_read_tr16_b64_v4i16((LAS s16x4*)(p + off4));
  u32x2 ua = __builtin_bit_cast(u32x2, a), ub = __builtin_bit_cast(u32x2, b);
  u32x4 r = {ua.x, ua.y, ub.x, ub.y};
  return r;
}
DEV unsigned pk2(float a, float b) {
  f32x2 v = {a, b};
  bf16x2 h = __builtin_convertvector(v, bf16x2);
  return __builtin_bit_cast(unsigned, h);
}
DEV float blo(unsigned u) { return __uint_as_float(u << 16); }
DEV float bhi(unsigned u) { return __uint_as_float(u & 0xffff0000u); }
template <int CTRL> DEV float dpp(float v) {
  return __uint_as_float(__builtin_amdgcn_update_dpp(0u, __float_as_uint(v), CTRL, 0xf, 0xf, true));
}
DEV float sum16(float v) {
  v += dpp<0x128>(v); v += dpp<0x124>(v); v += dpp<0x122>(v); v += dpp<0x121>(v);
  return v;
}
DEV float sumq(float v) {
  unsigned u = __float_as_uint(v);
  auto a = __builtin_amdgcn_permlane16_swap(u, u, false, false);
  v = __uint_as_float(a[0]) + __uint_as_float(a[1]);
  u = __float_as_uint(v);
  auto b = __builtin_amdgcn_permlane32_swap(u, u, false, false);
  return __uint_as_float(b[0]) + __uint_as_float(b[1]);
}
DEV float maxq(float v) {
  unsigned u = __float_as_uint(v);
  auto a = __builtin_amdgcn_permlane16_swap(u, u, false, false);
  v = fmaxf(__uint_as_float(a[0]), __uint_as_float(a[1]));
  u = __float_as_uint(v);
  auto b = __builtin_amdgcn_permlane32_swap(u, u, false, false);
  return fmaxf(__uint_as_float(b[0]), __uint_as_float(b[1]));
}
DEV u32x4 pairswap(u32x2 b0, u32x2 b1) {
  auto s0 = __builtin_amdgcn_permlane16_swap(b0.x, b1.x, false, false);
  auto s1 = __builtin_amdgcn_permlane16_swap(b0.y, b1.y, false, false);
  u32x4 r = {s0[0], s1[0], s0[1], s1[1]};
  return r;
}
DEV float gelu_t(float v) {
  float e = __builtin_amdgcn_exp2f(v * __builtin_fmaf(v * v, -0.10294324f, -2.3022082f));
  return v * __builtin_amdgcn_rcpf(1.0f + e);
}
DEV u64 gload64(const char* p) { return __hip_atomic_load((GAS u64*)(size_t)p, __ATOMIC_RELAXED, __HIP_MEMORY_SCOPE_AGENT); }
DEV void gstore64(const char* p, u64 v) { __hip_atomic_store((GAS u64*)(size_t)p, v, __ATOMIC_RELAXED, __HIP_MEMORY_SCOPE_AGENT); }
DEV void poll_flags(const char* p, bool active, u64 T) {
  for (unsigned it = 0; it < SPIN_CAP; ++it) {
    bool ok = !active || gload64(p) == T;
    if (__all(ok)) break;
    __builtin_amdgcn_s_sleep(1);
  }
}
DEV u32x4 settle_pair(u32x4 g, __amdgpu_buffer_rsrc_t rs, unsigned off, bool active, unsigned T32) {
  for (unsigned it = 0; it < SPIN_CAP; ++it) {
    bool ok = !active || (g.y == T32 && g.w == T32);
    if (__all(ok)) break;
    __builtin_amdgcn_s_sleep(1);
    g = __builtin_amdgcn_raw_buffer_load_b128(rs, off, 0, 16);
  }
  return g;
}
DEV u32x4 aload128c(unsigned voff, const char* sbase) {
  u32x4 r;
  asm volatile("global_load_dwordx4 %0, %1, %2 sc1" : "=v"(r) : "v"(voff), "s"(uni(sbase)) : "memory");
  return r;
}
DEV u32x2 aload64c(unsigned voff, const char* sbase) {
  u32x2 r;
  asm volatile("global_load_dwordx2 %0, %1, %2 sc1" : "=v"(r) : "v"(voff), "s"(uni(sbase)) : "memory");
  return r;
}

struct Ctx {
  const char *x, *wqkv, *wo, *win, *wsp, *bsp, *wout;
  char *out, *ws;
  LAS char* L;
  unsigned lb;
  int tid, lane, w, bid;
  unsigned Tlo, Thi;
  __amdgpu_buffer_rsrc_t rs;
};
DEV u64 tagT(const Ctx& c) { return ((u64)c.Thi << 32) | c.Tlo; }

DEV void s0_issue(const Ctx& c, u32x4& xr, u32x2& ev, int row) {
  ev = aload64c(0, c.ws + WS_EPOCH);
  asm volatile("global_load_dwordx4 %0, %1, %2" : "=v"(xr) : "v"(c.lane * 16), "s"(uni(c.x + (size_t)row * 1024)) : "memory");
}
template <int P> DEV void s0_finish(Ctx& c, u32x4 xr, u32x2 ev, int row) {
  asm volatile("s_waitcnt vmcnt(%2) ; TIE %0 %1" : "+v"(xr), "+v"(ev) : "n"(P) : "memory");
  u64 e = (((u64)(unsigned)__builtin_amdgcn_readfirstlane(ev.y)) << 32) | (unsigned)__builtin_amdgcn_readfirstlane(ev.x);
  e += 1;
  c.Tlo = (unsigned)e; c.Thi = (unsigned)(e >> 32);
  unsigned x0 = xr.x, x1 = xr.y, x2 = xr.z, x3 = xr.w;
  float v[8] = {blo(x0), bhi(x0), blo(x1), bhi(x1), blo(x2), bhi(x2), blo(x3), bhi(x3)};
  float s = ((v[0] + v[1]) + (v[2] + v[3])) + ((v[4] + v[5]) + (v[6] + v[7]));
  s = sumq(sum16(s));
  float m = s * (1.0f / 512.0f), q = 0.f;
#pragma unroll
  for (int j = 0; j < 8; ++j) { float d = v[j] - m; q = __builtin_fmaf(d, d, q); }
  q = sumq(sum16(q));
  float r = 1.0f / sqrtf(q * (1.0f / 512.0f) + 1e-5f);
  if (c.lane < 2)
    gstore64(c.ws + WS_XS + row * 16 + c.lane * 8, ((u64)c.Tlo << 32) | __float_as_uint(c.lane ? r : m));
}

struct G3A { unsigned vw; const char* bw; };
DEV G3A g3_addr(const Ctx& c, int rt) {
  int r = 8 * c.w + (c.lane >> 3);
  G3A a;
  a.vw = r * 4096 + (((c.lane & 7) ^ ((r >> 1) & 7)) << 4);
  a.bw = c.wsp + (size_t)rt * 64 * 4096;
  return a;
}
DEV void g3_prefetch_w(const Ctx& c, const G3A& a) {
#pragma unroll
  for (int s = 0; s < 7; ++s) dma16(a.vw, a.bw + s * 128, c.lb + s * G_SLOT + c.w * 1024);
}

DEV void tlb_touch(const Ctx& c) {
  const int n = c.tid;
  const char* p = c.ws + (size_t)n * 65536;
  if (n >= 224) p = c.wsp + (size_t)(n - 224) * 65536;
  if (n >= 352) p = c.out + (size_t)(n - 352) * 65536;
  if (n >= 384) p = c.wo + (size_t)(n - 384) * 65536;
  if (n >= 392) p = c.wout + (size_t)(n - 392) * 65536;
  if (n >= 403) p = c.bsp;
  Ctx0 k = {c.lb + T_DUMMY};
  touch(k, p);
}
DEV void l2_warm(const Ctx& c, int rt, int a16, int half, int xcol) {
  Ctx0 k = {c.lb + T_DUMMY};
#pragma unroll
  for (int j = 0; j < 2; ++j) {
    int row = rt * 64 + half * 32 + 4 * c.w + 2 * j + (c.lane >> 5);
    touch(k, c.wsp + (size_t)row * 4096 + (c.lane & 31) * 128);
  }
  int n = c.w * 64 + c.lane; n = n < 300 ? n : 299;
  int row = a16 * 75 + (n >> 2);
  const char* p = row < 512 ? c.wo + (size_t)row * 1024 : c.wout + (size_t)(row - 512) * 1024;
  if (c.w < 5) touch(k, p + xcol * 512 + (n & 3) * 128);
}

struct AAddr { unsigned vx0, vx1, vh, vw; const char *bx, *bh, *bw; u64 hmask; };
template <int H, int P> DEV void a_dma(const Ctx& c, const AAddr& a, int s, unsigned slot) {
  const unsigned l = c.lb + slot;
  if (H == 0) {
    if (P == 0) dma16(a.vx0, a.bx + s * 128, l + c.w * 2048);
    if (P == 1) dma16(a.vx1, a.bx + s * 128, l + c.w * 2048 + 1024);
    if (P == 2) dma16m(a.vh, a.bh + s * 128, l + 128 * 128 + c.w * 256, a.hmask);
  } else {
    dma16(a.vw, a.bw + (size_t)s * 64 * 3072 + P * 1024, l + A_W + P * 8192 + c.w * 1024);
  }
}
template <int H> DEV void a_half(const Ctx& c, const AAddr& a, int s, unsigned slot) {
  a_dma<H, 0>(c, a, s, slot); a_dma<H, 1>(c, a, s, slot); a_dma<H, 2>(c, a, s, slot);
}

struct AAcc { f32x4 q[2][2], k[2][2], v[2][2], h, qc; };
struct AF { u32x4 x0, x1, hx, qa, qb, ka, kb, va, vb; };
struct AOfs { unsigned xo, xh, wA, wB; };

DEV void a_read(const Ctx& c, AF& F, unsigned slot, int kk, const AOfs& o) {
  LAS char* L = c.L + slot;
  LAS char* pa = L + A_W + o.wA + kk * 4096;
  LAS char* pb = L + A_W + o.wB + kk * 4096;
  F.qa = trfrag(pa, 512);
  F.x0 = lds128(L + (o.xo ^ (kk * 64)));
  F.x1 = lds128(L + (o.xo ^ (kk * 64)) + 2048);
  F.qb = trfrag(pb, 512);
  F.ka = trfrag(pa + 8192, 512); F.kb = trfrag(pb + 8192, 512);
  F.va = trfrag(pa + 16384, 512); F.vb = trfrag(pb + 16384, 512);
  F.hx = lds128(L + (o.xh ^ (kk * 64)));
  sb();
}
template <bool HV, int H, bool NEXT> DEV void a_step(const Ctx& c, AAcc& A, const AF& F, AF& N, unsigned rslot, int kk, const AOfs& o,
                                                     const AAddr& a, int sn, unsigned slot) {
  LAS char* L = c.L + rslot;
  LAS char* pa = L + A_W + o.wA + kk * 4096;
  LAS char* pb = L + A_W + o.wB + kk * 4096;
  LAS char* px = L + (o.xo ^ (kk * 64));
  sb();
  A.q[0][0] = mfmaX(F.qa, F.x0, A.q[0][0]); sb();
  if (NEXT) N.qa = trfrag(pa, 512);
  sb();
  A.q[1][0] = mfmaX(F.qa, F.x1, A.q[1][0]); sb();
  if (NEXT) { N.x0 = lds128(px); N.x1 = lds128(px + 2048); }
  sb();
  A.q[0][1] = mfmaX(F.qb, F.x0, A.q[0][1]); sb();
  if (NEXT) N.qb = trfrag(pb, 512);
  sb();
  A.q[1][1] = mfmaX(F.qb, F.x1, A.q[1][1]); sb();
  if (NEXT) N.ka = trfrag(pa + 8192, 512);
  sb();
  A.k[0][0] = mfmaX(F.ka, F.x0, A.k[0][0]); sb();
  if (NEXT) N.kb = trfrag(pb + 8192, 512);
  sb();
  A.k[1][0] = mfmaX(F.ka, F.x1, A.k[1][0]); sb();
  if (NEXT) N.va = trfrag(pa + 16384, 512);
  sb();
  A.k[0][1] = mfmaX(F.kb, F.x0, A.k[0][1]); sb();
  if (NEXT) N.vb = trfrag(pb + 16384, 512);
  sb();
  A.k[1][1] = mfmaX(F.kb, F.x1, A.k[1][1]); sb();
  if (NEXT) N.hx = lds128(L + (o.xh ^ (kk * 64)));
  sb();
  A.v[0][0] = mfmaX(F.x0, F.va, A.v[0][0]); sb();
  if (H >= 0) a_dma<(H < 0 ? 0 : H), 0>(c, a, sn, slot);
  sb();
  A.v[1][0] = mfmaX(F.x1, F.va, A.v[1][0]);
  A.v[0][1] = mfmaX(F.x0, F.vb, A.v[0][1]); sb();
  if (H >= 0) a_dma<(H < 0 ? 0 : H), 1>(c, a, sn, slot);
  sb();
  A.v[1][1] = mfmaX(F.x1, F.vb, A.v[1][1]);
  if (HV) {
    A.h = mfmaX(F.hx, F.va, A.h);
  } else {
    A.h = mfmaX(F.ka, F.hx, A.h);
  }
  sb();
  if (H >= 0) a_dma<(H < 0 ? 0 : H), 2>(c, a, sn, slot);
  sb();
  if (!HV) A.qc = mfmaX(F.qa, F.hx, A.qc);
  sb();
}

template <bool HV> DEV void a_loop(const Ctx& c, AAcc& A, const AAddr& a, const AOfs& o, u32x4& g, unsigned gofs) {
  AF F0, F1;
  wait_vm<9 + (PF & 1)>();
  bar();
  a_read(c, F0, 0, 0, o);
  unsigned s0 = 0, s1 = A_SLOT, s2 = 2 * A_SLOT;
#pragma unroll 1
  for (int s = 0; s < 5; ++s) {
    a_step<HV, 1, true>(c, A, F0, F1, s0, 1, o, a, s + 2, s2);
    wait_vm<6>();
    bar();
    STAMP1(16 + s);
    a_step<HV, 0, true>(c, A, F1, F0, s1, 0, o, a, s + 3, s0);
    unsigned t = s0; s0 = s1; s1 = s2; s2 = t;
  }
  a_step<HV, 1, true>(c, A, F0, F1, 2 * A_SLOT, 1, o, a, 7, A_SLOT);
  wait_vm<6>();
  bar();
  g = aload128c(gofs, c.ws + WS_XS);
  a_step<HV, -1, true>(c, A, F1, F0, 0, 0, o, a, 0, 0);
  a_step<HV, -1, true>(c, A, F0, F1, 0, 1, o, a, 0, 0);
  asm volatile("s_waitcnt vmcnt(0) ; TIE %0" : "+v"(g) : : "memory");
  bar();
  a_step<HV, -1, true>(c, A, F1, F0, A_SLOT, 0, o, a, 0, 0);
  a_step<HV, -1, true>(c, A, F0, F1, A_SLOT, 1, o, a, 0, 0);
  a_step<HV, -1, false>(c, A, F1, F0, 0, 0, o, a, 0, 0);
}

DEV void job_a(Ctx& c, int i, int hc, int s0row, const G3A& g3, int rt, int half, int xcol) {
  u32x4 xr; u32x2 ev;
  s0_issue(c, xr, ev, s0row);
  char* ws = c.ws;
  const int lane = c.lane, w = c.w, rg = w & 3, cg = w >> 2;
  const int t = lane & 15, q = lane >> 4;
  AAddr a;
  {
    int r0 = 16 * w + (lane >> 3);
    a.vx0 = r0 * 1024 + (((lane & 7) ^ ((r0 >> 1) & 7)) << 4);
    int r1 = r0 + 8;
    a.vx1 = r1 * 1024 + (((lane & 7) ^ ((r1 >> 1) & 7)) << 4);
    a.bx = c.x + (size_t)i * 128 * 1024;
    int rh = 2 * w + ((lane >> 3) & 1);
    a.vh = rh * 1024 + (((lane & 7) ^ w) << 4);
    a.bh = c.x + (size_t)(i > 0 ? i * 128 - 16 : 0) * 1024;
    a.hmask = w == 0 ? 0xff00ull : 0xffffull;
    int k = 8 * w + (lane >> 3);
    int f = ((k >> 1) & 1) | (((k >> 3) & 1) << 1);
    a.vw = k * 3072 + (((lane & 7) ^ (f << 1)) << 4);
    a.bw = c.wqkv + hc * 128;
  }
  if (c.tid < 24) {
    u32x4 ones = {0x3f803f80u, 0x3f803f80u, 0x3f803f80u, 0x3f803f80u};
    *(LAS u32x4*)(c.L + (c.tid >> 3) * A_SLOT + 128 * 128 + (c.tid & 7) * 16) = ones;
  }
  a_half<0>(c, a, 0, 0); a_half<1>(c, a, 0, 0);
  a_half<0>(c, a, 1, A_SLOT); a_half<1>(c, a, 1, A_SLOT);
  s0_finish<12>(c, xr, ev, s0row);
  STAMP(15);
  a_half<0>(c, a, 2, 2 * A_SLOT);
  if (PF & 1) tlb_touch(c);

  const unsigned xlane = t * 128 + ((q ^ (t >> 1)) << 4);
  const unsigned xo = rg * 32 * 128 + xlane, xh = 128 * 128 + xlane;
  const int f = ((t >> 3) & 1) | ((q & 1) << 1);
  const int hA = 2 * cg + (rg & 1), hB = hA ^ 1;
  const unsigned wl = (8 * q + (t >> 2)) * 128 + (t & 3) * 8;
  const unsigned wA = wl + ((hA ^ f) << 5), wB = wl + ((hB ^ f) << 5);
  const int gj = c.tid < 144 ? c.tid : 143;
  const int gtok = gj < 128 ? i * 128 + gj : (i > 0 ? i * 128 - 16 + (gj - 128) : gj - 128);
  const unsigned gofs = gtok * 16;

  AAcc A;
  const f32x4 z4 = {0.f, 0.f, 0.f, 0.f};
#pragma unroll
  for (int x = 0; x < 2; ++x)
#pragma unroll
    for (int y = 0; y < 2; ++y) { A.q[x][y] = z4; A.k[x][y] = z4; A.v[x][y] = z4; }
  A.h = z4; A.qc = z4;
  u32x4 g = {0, 0, 0, 0};
  const bool hv = rg >= 2;
  const AOfs ao = {xo, xh, wA, wB};
  if (hv) a_loop<true>(c, A, a, ao, g, gofs);
  else a_loop<false>(c, A, a, ao, g, gofs);
  STAMP(2);
  g = settle_pair(g, c.rs, WS_XS + gofs, true, c.Tlo);
  if (c.tid < 144) { u32x2 mr = {g.x, g.z}; *(LAS u32x2*)(c.L + T_XS + c.tid * 8) = mr; }
  if (!hv) {
    if (t == 0) {
      *(LAS f32x4*)(c.L + T_CS + (64 + hA * 16 + 4 * q) * 4) = A.h;
      *(LAS f32x4*)(c.L + T_CS + (hA * 16 + 4 * q) * 4) = A.qc;
    }
  } else if (lane < 16) {
    *(LAS float*)(c.L + T_CS + (128 + hA * 16 + lane) * 4) = A.h[0];
  }
  bar();
  g3_prefetch_w(c, g3);
  if (PF & 2) l2_warm(c, rt, (rt & 7) * 2 + half, half, xcol);

  u32x2 Q[2][2], K[2][2], V[2][2], H;
  {
    f32x4 csq[2], csk[2]; float csv[2];
#pragma unroll
    for (int y = 0; y < 2; ++y) {
      int hh = y ? hB : hA;
      csq[y] = *(LAS f32x4*)(c.L + T_CS + (hh * 16 + 4 * q) * 4);
      csk[y] = *(LAS f32x4*)(c.L + T_CS + (64 + hh * 16 + 4 * q) * 4);
      csv[y] = *(LAS float*)(c.L + T_CS + (128 + hh * 16 + t) * 4);
    }
#pragma unroll
    for (int x = 0; x < 2; ++x) {
      f32x2 mr = *(LAS f32x2*)(c.L + T_XS + (rg * 32 + x * 16 + t) * 8);
      f32x4 m01 = *(LAS f32x4*)(c.L + T_XS + (rg * 32 + x * 16 + 4 * q) * 8);
      f32x4 m23 = *(LAS f32x4*)(c.L + T_XS + (rg * 32 + x * 16 + 4 * q + 2) * 8);
      const float rq = mr.y * 0.25f;
#pragma unroll
      for (int y = 0; y < 2; ++y) {
        f32x4 aq = A.q[x][y], ak = A.k[x][y], av = A.v[x][y];
        Q[x][y].x = pk2(rq * (aq[0] - mr.x * csq[y][0]), rq * (aq[1] - mr.x * csq[y][1]));
        Q[x][y].y = pk2(rq * (aq[2] - mr.x * csq[y][2]), rq * (aq[3] - mr.x * csq[y][3]));
        K[x][y].x = pk2(mr.y * (ak[0] - mr.x * csk[y][0]), mr.y * (ak[1] - mr.x * csk[y][1]));
        K[x][y].y = pk2(mr.y * (ak[2] - mr.x * csk[y][2]), mr.y * (ak[3] - mr.x * csk[y][3]));
        V[x][y].x = pk2(m01[1] * (av[0] - m01[0] * csv[y]), m01[3] * (av[1] - m01[2] * csv[y]));
        V[x][y].y = pk2(m23[1] * (av[2] - m23[0] * csv[y]), m23[3] * (av[3] - m23[2] * csv[y]));
      }
    }
    if (!hv) {
      f32x2 mr = *(LAS f32x2*)(c.L + T_XS + (128 + t) * 8);
      H.x = pk2(mr.y * (A.h[0] - mr.x * csk[0][0]), mr.y * (A.h[1] - mr.x * csk[0][1]));
      H.y = pk2(mr.y * (A.h[2] - mr.x * csk[0][2]), mr.y * (A.h[3] - mr.x * csk[0][3]));
    } else {
      f32x4 m01 = *(LAS f32x4*)(c.L + T_XS + (128 + 4 * q) * 8);
      f32x4 m23 = *(LAS f32x4*)(c.L + T_XS + (128 + 4 * q + 2) * 8);
      H.x = pk2(m01[1] * (A.h[0] - m01[0] * csv[0]), m01[3] * (A.h[1] - m01[2] * csv[0]));
      H.y = pk2(m23[1] * (A.h[2] - m23[0] * csv[0]), m23[3] * (A.h[3] - m23[2] * csv[0]));
    }
  }
  const unsigned KX = G_Z, VX = G_SLOT + G_Z;
  {
    *(LAS u32x2*)(c.L + (hv ? VX : KX) + hA * 512 + lane * 8) = H;
    if (rg < 3) {
      *(LAS u32x2*)(c.L + KX + ((rg + 1) * 4 + hA) * 512 + lane * 8) = K[1][0];
      *(LAS u32x2*)(c.L + KX + ((rg + 1) * 4 + hB) * 512 + lane * 8) = K[1][1];
      *(LAS u32x2*)(c.L + VX + ((rg + 1) * 4 + hA) * 512 + lane * 8) = V[1][0];
      *(LAS u32x2*)(c.L + VX + ((rg + 1) * 4 + hB) * 512 + lane * 8) = V[1][1];
    }
  }
  bar();
  u32x2 Kp[2], Vp[2];
  Kp[0] = lds64(c.L + KX + (rg * 4 + hA) * 512 + lane * 8);
  Kp[1] = lds64(c.L + KX + (rg * 4 + hB) * 512 + lane * 8);
  Vp[0] = lds64(c.L + VX + (rg * 4 + hA) * 512 + lane * 8);
  Vp[1] = lds64(c.L + VX + (rg * 4 + hB) * 512 + lane * 8);

  const float NEG = -1e30f, LOG2E = 1.4426950408889634f;
  float bc[4], bp[4];
#pragma unroll
  for (int j = 0; j < 4; ++j) {
    int d = t - (4 * q + j);
    bc[j] = (d >= 0 && !(d & 1)) ? 0.f : NEG;
    int dp = d + 16;
    bp[j] = (dp <= 14 && !(dp & 1)) ? 0.f : NEG;
  }
  const bool first = (i == 0 && rg == 0);
#pragma unroll
  for (int x = 1; x >= 0; --x) {
    u32x2 O[2];
#pragma unroll
    for (int y = 0; y < 2; ++y) {
      u32x2 kp = x ? K[0][y] : Kp[y], vp = x ? V[0][y] : Vp[y];
      f32x4 sp = mfma16(kp, Q[x][y], z4), sc = mfma16(K[x][y], Q[x][y], z4);
      float e[8];
#pragma unroll
      for (int j = 0; j < 4; ++j) {
        e[j] = __builtin_fmaf(sp[j], LOG2E, (x == 0 && first) ? NEG : bp[j]);
        e[4 + j] = __builtin_fmaf(sc[j], LOG2E, bc[j]);
      }
      float mx = fmaxf(fmaxf(fmaxf(e[0], e[1]), fmaxf(e[2], e[3])), fmaxf(fmaxf(e[4], e[5]), fmaxf(e[6], e[7])));
      mx = maxq(mx);
      float sum = 0.f;
#pragma unroll
      for (int j = 0; j < 8; ++j) { e[j] = __builtin_amdgcn_exp2f(e[j] - mx); sum += e[j]; }
      float inv = __builtin_amdgcn_rcpf(sumq(sum));
      u32x4 P = {pk2(e[0] * inv, e[1] * inv), pk2(e[2] * inv, e[3] * inv), pk2(e[4] * inv, e[5] * inv), pk2(e[6] * inv, e[7] * inv)};
      u32x4 VV = {vp.x, vp.y, V[x][y].x, V[x][y].y};
      f32x4 o = mfma32(VV, P, z4);
      O[y].x = pk2(o[0], o[1]); O[y].y = pk2(o[2], o[3]);
    }
    u32x4 fr = pairswap(O[0], O[1]);
    int qp = (((q & 1) << 1) | (q >> 1)) ^ ((rg & 1) << 1);
    unsigned off = WS_O + ((i * 8 + rg * 2 + x) * 16 + 2 * hc + cg) * 1024 + qp * 256 + t * 16;
    __builtin_amdgcn_raw_buffer_store_b128(fr, c.rs, off, 0, 16);
  }
  wait_vm<0>();
  bar();
  if (c.tid == 0) gstore64(ws + WS_FO + (i * 8 + hc) * 128, tagT(c));
  STAMP(3);
}

struct ZAddr { unsigned vx0, vx1, vw; const char *bx, *bw; };
template <int S, int H, int P> DEV void z_dma(const Ctx& c, const ZAddr& a) {
  if (S >= 16 || S < 0) return;
  const unsigned l = c.lb + (S % 5) * Z_SLOT;
  const char* bw = a.bw + (size_t)(S & 7) * 64 * 2752 + (S < 8 ? 1376 : 0);
  if (S < 8) {
    if (H == 0) dma16(P ? a.vx1 : a.vx0, a.bx + S * 128, l + c.w * 2048 + P * 1024);
    else dma16m(a.vw, bw + P * 4 * 2752, l + Z_W + c.w * 1536 + P * 768, 0xffffffffffffull);
  } else if (P == 0) {
    dma16m(a.vw, bw + H * 4 * 2752, l + Z_W + c.w * 1536 + H * 768, 0xffffffffffffull);
  }
}
template <int S, int H> DEV void z_half(const Ctx& c, const ZAddr& a) { z_dma<S, H, 0>(c, a); z_dma<S, H, 1>(c, a); }
constexpr int z_n(int s) { return s >= 16 ? 0 : (s < 8 ? 4 : 2); }
constexpr int z_wait(int s) { return z_n(s + 1) + z_n(s + 2) + z_n(s + 3); }

struct ZW { u32x4 f[3]; };
template <int S, int KK> DEV void z_read_w(const Ctx& c, ZW& F, const unsigned (&wb)[3]) {
  LAS char* L = c.L + (S % 5) * Z_SLOT;
#pragma unroll
  for (int j = 0; j < 3; ++j) F.f[j] = trfrag(L + Z_W + wb[j] + KK * 6144, 768);
}
template <int S, int KK> DEV void z_read_x(const Ctx& c, u32x4 (&xf)[2][16], unsigned xo) {
  LAS char* L = c.L + (S % 5) * Z_SLOT;
  xf[0][2 * S + KK] = lds128(L + (xo ^ (KK * 64)));
  xf[1][2 * S + KK] = lds128(L + (xo ^ (KK * 64)) + 2048);
}
template <bool SWAP, int J, int RS, int RK, int SN, int H>
DEV void z_step(const Ctx& c, const ZAddr& a, u32x4 (&xf)[2][16], const ZW& F, ZW& N, f32x4 (&acc)[2][3], f32x4& cs, unsigned xo,
                const unsigned (&wb)[3]) {
  const u32x4 ones = {0x3f803f80u, 0x3f803f80u, 0x3f803f80u, 0x3f803f80u};
  constexpr int R = RS < 0 ? 0 : RS, JN = (2 * R + RK) & 15;
  LAS char* L = c.L + (R % 5) * Z_SLOT;
  auto mm = [&](int x, int j) { acc[x][j] = SWAP ? mfmaX(F.f[j], xf[x][J], acc[x][j]) : mfmaX(xf[x][J], F.f[j], acc[x][j]); };
  sb();
  mm(0, 0); sb();
  if (RS >= 0) N.f[0] = trfrag(L + Z_W + wb[0] + RK * 6144, 768);
  sb();
  mm(1, 0); sb();
  if (RS >= 0 && !SWAP) { xf[0][JN] = lds128(L + (xo ^ (RK * 64))); xf[1][JN] = lds128(L + (xo ^ (RK * 64)) + 2048); }
  if (RS >= 0 && SWAP) N.f[1] = trfrag(L + Z_W + wb[1] + RK * 6144, 768);
  sb();
  mm(0, 1); sb();
  if (RS >= 0 && !SWAP) N.f[1] = trfrag(L + Z_W + wb[1] + RK * 6144, 768);
  if (RS >= 0 && SWAP) N.f[2] = trfrag(L + Z_W + wb[2] + RK * 6144, 768);
  sb();
  mm(1, 1); sb();
  if (RS >= 0 && !SWAP) N.f[2] = trfrag(L + Z_W + wb[2] + RK * 6144, 768);
  sb();
  mm(0, 2); sb();
  z_dma<SN, H, 0>(c, a);
  sb();
  mm(1, 2); sb();
  z_dma<SN, H, 1>(c, a);
  sb();
  cs = SWAP ? mfmaX(F.f[0], ones, cs) : mfmaX(ones, F.f[0], cs);
  sb();
}
template <int S> DEV void z_stage_a(const Ctx& c, const ZAddr& a, u32x4 (&xf)[2][16], ZW& F0, ZW& F1, f32x4 (&acc)[2][3], f32x4& cs,
                                    unsigned xo, const unsigned (&wb)[3], u32x4& g, unsigned gofs) {
  z_step<false, 2 * S, S, 1, (S == 0 ? -1 : S + 4), 1>(c, a, xf, F0, F1, acc, cs, xo, wb);
  if (S == 7) asm volatile("s_waitcnt vmcnt(4) ; TIE %0" : "+v"(g) : : "memory");
  else wait_vm<z_wait(S + 1)>();
  bar();
  STAMP1(16 + S);
  if (S == 5) g = aload128c(gofs, c.ws + WS_XS);
  z_step<false, 2 * S + 1, (S < 7 ? S + 1 : -1), 0, S + 5, 0>(c, a, xf, F1, F0, acc, cs, xo, wb);
}
template <int S> DEV void z_stage_b(const Ctx& c, const ZAddr& a, u32x4 (&xf)[2][16], ZW& F0, ZW& F1, f32x4 (&acc)[2][3], f32x4& cs,
                                    const unsigned (&wb)[3], u32x4 (&g)[2], unsigned gofs, const char* gbase) {
  z_step<true, 2 * (S - 8), S, 1, S + 4, 1>(c, a, xf, F0, F1, acc, cs, 0, wb);
  if (S < 15) {
    wait_vm<z_wait(S + 1)>();
    bar();
    if (S == 14) { g[0] = aload128c(gofs, gbase); g[1] = aload128c(gofs, gbase + 2048); }
  }
  z_step<true, 2 * (S - 8) + 1, (S < 15 ? S + 1 : -1), 0, S + 5, 0>(c, a, xf, F1, F0, acc, cs, 0, wb);
  if (S == 15) asm volatile("s_waitcnt vmcnt(0) ; TIE %0 %1" : "+v"(g[0]), "+v"(g[1]) : : "memory");
}

DEV int fb_start(int c) { return c < 3 ? 6 * c : 18 + 5 * (c - 3); }

DEV void job_z(Ctx& c, int i, int zc, int s0row, const G3A& g3) {
  u32x4 xr; u32x2 ev;
  s0_issue(c, xr, ev, s0row);
  char* ws = c.ws;
  const int lane = c.lane, w = c.w, rg = w & 3, cg = w >> 2;
  const int t = lane & 15, q = lane >> 4;
  const int fb0 = fb_start(zc), nb = zc < 3 ? 6 : 5;
  ZAddr a;
  {
    int r0 = 16 * w + (lane >> 3);
    a.vx0 = r0 * 1024 + (((lane & 7) ^ ((r0 >> 1) & 7)) << 4);
    int r1 = r0 + 8;
    a.vx1 = r1 * 1024 + (((lane & 7) ^ ((r1 >> 1) & 7)) << 4);
    a.bx = c.x + (size_t)i * 128 * 1024;
    int l48 = lane < 48 ? lane : 47;
    int kr = l48 / 12, cc = l48 % 12;
    int b = (cc >> 1) ^ (w & 1);
    int fbg = fb0 + b; fbg = fbg > 42 ? 42 : fbg;
    a.vw = (8 * w + kr) * 2752 + fbg * 32 + (cc & 1) * 16;
    a.bw = c.win;
  }
  z_half<0, 0>(c, a); z_half<0, 1>(c, a); z_half<1, 0>(c, a); z_half<1, 1>(c, a); z_half<2, 0>(c, a); z_half<2, 1>(c, a);
  s0_finish<12>(c, xr, ev, s0row);
  STAMP(15);
  z_half<3, 0>(c, a); z_half<3, 1>(c, a); z_half<4, 0>(c, a); z_half<4, 1>(c, a);
  if (PF & 1) tlb_touch(c);

  const unsigned xo = rg * 32 * 128 + t * 128 + ((q ^ (t >> 1)) << 4);
  unsigned wb[3]; int lb3[3];
#pragma unroll
  for (int j = 0; j < 3; ++j) {
    int jr = j + (rg == 3 ? 0 : rg); jr = jr >= 3 ? jr - 3 : jr;
    lb3[j] = 3 * cg + jr;
    wb[j] = (8 * q + (t >> 2)) * 192 + ((lb3[j] ^ (q & 1)) << 5) + (t & 3) * 8;
  }
  const int gj = c.tid < 128 ? c.tid : 127;
  const unsigned gofs = (i * 128 + gj) * 16;
  const f32x4 z4 = {0.f, 0.f, 0.f, 0.f};
  u32x4 xf[2][16];
  f32x4 acc[2][3], cs = z4;
#pragma unroll
  for (int j = 0; j < 3; ++j) { acc[0][j] = z4; acc[1][j] = z4; }
  u32x4 g = {0, 0, 0, 0};
  ZW F0, F1;
  wait_vm<16 + (PF & 1)>();
  bar();
  z_read_x<0, 0>(c, xf, xo); z_read_w<0, 0>(c, F0, wb);
  z_stage_a<0>(c, a, xf, F0, F1, acc, cs, xo, wb, g, gofs); z_stage_a<1>(c, a, xf, F0, F1, acc, cs, xo, wb, g, gofs);
  z_stage_a<2>(c, a, xf, F0, F1, acc, cs, xo, wb, g, gofs); z_stage_a<3>(c, a, xf, F0, F1, acc, cs, xo, wb, g, gofs);
  z_stage_a<4>(c, a, xf, F0, F1, acc, cs, xo, wb, g, gofs); z_stage_a<5>(c, a, xf, F0, F1, acc, cs, xo, wb, g, gofs);
  z_stage_a<6>(c, a, xf, F0, F1, acc, cs, xo, wb, g, gofs); z_stage_a<7>(c, a, xf, F0, F1, acc, cs, xo, wb, g, gofs);
  asm volatile("" : "+v"(acc[0][0]), "+v"(acc[0][1]), "+v"(acc[0][2]), "+v"(acc[1][0]), "+v"(acc[1][1]), "+v"(acc[1][2]), "+v"(cs));
  STAMP(2);
  g = settle_pair(g, c.rs, WS_XS + gofs, true, c.Tlo);
  if (c.tid < 128) { u32x2 mr = {g.x, g.z}; *(LAS u32x2*)(c.L + T_XS + c.tid * 8) = mr; }
  if (rg < 3 && lane < 16) *(LAS float*)(c.L + T_CS + (lb3[0] * 16 + lane) * 4) = cs[0];
  bar();
  u32x2 Z2[2][3];
  {
    float csf[3];
#pragma unroll
    for (int j = 0; j < 3; ++j) csf[j] = *(LAS float*)(c.L + T_CS + (lb3[j] * 16 + t) * 4);
#pragma unroll
    for (int x = 0; x < 2; ++x) {
      f32x4 m01 = *(LAS f32x4*)(c.L + T_XS + (rg * 32 + x * 16 + 4 * q) * 8);
      f32x4 m23 = *(LAS f32x4*)(c.L + T_XS + (rg * 32 + x * 16 + 4 * q + 2) * 8);
      float s1[4] = {0.f, 0.f, 0.f, 0.f}, s2[4] = {0.f, 0.f, 0.f, 0.f};
#pragma unroll
      for (int j = 0; j < 3; ++j) {
        f32x4 v = acc[x][j];
        float y0 = gelu_t(m01[1] * (v[0] - m01[0] * csf[j])), y1 = gelu_t(m01[3] * (v[1] - m01[2] * csf[j]));
        float y2 = gelu_t(m23[1] * (v[2] - m23[0] * csf[j])), y3 = gelu_t(m23[3] * (v[3] - m23[2] * csf[j]));
        unsigned p0 = pk2(y0, y1), p1 = pk2(y2, y3);
        Z2[x][j].x = p0; Z2[x][j].y = p1;
        if (lb3[j] < nb) {
          y0 = blo(p0); y1 = bhi(p0); y2 = blo(p1); y3 = bhi(p1);
          s1[0] += y0; s1[1] += y1; s1[2] += y2; s1[3] += y3;
          s2[0] = __builtin_fmaf(y0, y0, s2[0]); s2[1] = __builtin_fmaf(y1, y1, s2[1]);
          s2[2] = __builtin_fmaf(y2, y2, s2[2]); s2[3] = __builtin_fmaf(y3, y3, s2[3]);
        }
      }
#pragma unroll
      for (int r = 0; r < 4; ++r) { s1[r] = sum16(s1[r]); s2[r] = sum16(s2[r]); }
      if (t == 0) {
        f32x4 o0 = {s1[0], s2[0], s1[1], s2[1]}, o1 = {s1[2], s2[2], s1[3], s2[3]};
        LAS char* p = c.L + T_ZP + (cg * 128 + rg * 32 + x * 16 + 4 * q) * 8;
        *(LAS f32x4*)p = o0; *(LAS f32x4*)(p + 16) = o1;
      }
    }
  }
  bar();
  if (c.tid < 256) {
    int tok = c.tid >> 1, st = c.tid & 1;
    float v = *(LAS float*)(c.L + T_ZP + tok * 8 + st * 4) + *(LAS float*)(c.L + T_ZP + (128 + tok) * 8 + st * 4);
    gstore64(ws + WS_ZS + ((i * 8 + zc) * 128 + tok) * 16 + st * 8, ((u64)c.Tlo << 32) | __float_as_uint(v));
  }
  STAMP(3);
  cs = z4;
#pragma unroll
  for (int j = 0; j < 3; ++j) { acc[0][j] = z4; acc[1][j] = z4; }
  u32x4 gs[2] = {{0, 0, 0, 0}, {0, 0, 0, 0}};
  const int stok = c.tid & 127, pg = c.tid >> 7;
  const unsigned sofs = ((2 * pg) * 128 + stok) * 16;
  const char* sbase = ws + WS_ZS + (size_t)i * 8 * 128 * 16;
  z_read_w<8, 0>(c, F0, wb);
  z_stage_b<8>(c, a, xf, F0, F1, acc, cs, wb, gs, sofs, sbase);  z_stage_b<9>(c, a, xf, F0, F1, acc, cs, wb, gs, sofs, sbase);
  z_stage_b<10>(c, a, xf, F0, F1, acc, cs, wb, gs, sofs, sbase); z_stage_b<11>(c, a, xf, F0, F1, acc, cs, wb, gs, sofs, sbase);
  z_stage_b<12>(c, a, xf, F0, F1, acc, cs, wb, gs, sofs, sbase); z_stage_b<13>(c, a, xf, F0, F1, acc, cs, wb, gs, sofs, sbase);
  z_stage_b<14>(c, a, xf, F0, F1, acc, cs, wb, gs, sofs, sbase); z_stage_b<15>(c, a, xf, F0, F1, acc, cs, wb, gs, sofs, sbase);
  asm volatile("" : "+v"(acc[0][0]), "+v"(acc[0][1]), "+v"(acc[0][2]), "+v"(acc[1][0]), "+v"(acc[1][1]), "+v"(acc[1][2]), "+v"(cs));
  STAMP(4);
  {
    const unsigned so = WS_ZS + i * 8 * 128 * 16 + sofs;
    gs[0] = settle_pair(gs[0], c.rs, so, true, c.Tlo);
    gs[1] = settle_pair(gs[1], c.rs, so + 2048, true, c.Tlo);
    f32x2 p = {__uint_as_float(gs[0].x) + __uint_as_float(gs[1].x), __uint_as_float(gs[0].z) + __uint_as_float(gs[1].z)};
    *(LAS f32x2*)(c.L + T_ZQ + (pg * 128 + stok) * 8) = p;
  }
  if (rg < 3 && t == 0) *(LAS f32x4*)(c.L + T_CS + 512 + (lb3[0] * 16 + 4 * q) * 4) = cs;
  bar();
  g3_prefetch_w(c, g3);
  if (c.tid < 128) {
    f32x2 p0 = *(LAS f32x2*)(c.L + T_ZQ + c.tid * 8), p1 = *(LAS f32x2*)(c.L + T_ZQ + (128 + c.tid) * 8);
    f32x2 p2 = *(LAS f32x2*)(c.L + T_ZQ + (256 + c.tid) * 8), p3 = *(LAS f32x2*)(c.L + T_ZQ + (384 + c.tid) * 8);
    float s = (p0.x + p1.x) + (p2.x + p3.x), ss = (p0.y + p1.y) + (p2.y + p3.y);
    float m = s * (1.0f / 688.0f);
    float var = fmaxf(ss * (1.0f / 688.0f) - m * m, 0.f);
    f32x2 o = {m, 1.0f / sqrtf(var + 1e-5f)};
    *(LAS f32x2*)(c.L + T_ZF + c.tid * 8) = o;
  }
  bar();
  {
    const int qp = ((q & 1) << 1) | (q >> 1);
    f32x4 mr[2][2];
#pragma unroll
    for (int x = 0; x < 2; ++x) {
      mr[x][0] = *(LAS f32x4*)(c.L + T_ZF + (rg * 32 + x * 16 + 4 * q) * 8);
      mr[x][1] = *(LAS f32x4*)(c.L + T_ZF + (rg * 32 + x * 16 + 4 * q + 2) * 8);
    }
#pragma unroll
    for (int j = 0; j < 3; ++j) {
      u32x2 n[2];
#pragma unroll
      for (int x = 0; x < 2; ++x) {
        unsigned p0 = Z2[x][j].x, p1 = Z2[x][j].y;
        n[x].x = pk2((blo(p0) - mr[x][0][0]) * mr[x][0][1], (bhi(p0) - mr[x][0][2]) * mr[x][0][3]);
        n[x].y = pk2((blo(p1) - mr[x][1][0]) * mr[x][1][1], (bhi(p1) - mr[x][1][2]) * mr[x][1][3]);
      }
      u32x4 fr = pairswap(n[0], n[1]);
      if (lb3[j] < nb) {
        unsigned off = WS_Z2N + ((i * 4 + rg) * 44 + fb0 + lb3[j]) * 1024 + qp * 256 + t * 16;
        __builtin_amdgcn_raw_buffer_store_b128(fr, c.rs, off, 0, 16);
      }
    }
  }
  wait_vm<0>();
  bar();
  if (c.tid == 0) gstore64(ws + WS_FZ2 + (zc * 16 + i) * 128, tagT(c));
  STAMP(5);
  {
    f32x2 mr[2];
#pragma unroll
    for (int x = 0; x < 2; ++x) mr[x] = *(LAS f32x2*)(c.L + T_XS + (rg * 32 + x * 16 + t) * 8);
#pragma unroll
    for (int j = 0; j < 3; ++j) {
      f32x4 c4 = *(LAS f32x4*)(c.L + T_CS + 512 + (lb3[j] * 16 + 4 * q) * 4);
      u32x2 n[2];
#pragma unroll
      for (int x = 0; x < 2; ++x) {
        f32x4 v = acc[x][j];
        n[x].x = pk2(gelu_t(mr[x].y * (v[0] - mr[x].x * c4[0])), gelu_t(mr[x].y * (v[1] - mr[x].x * c4[1])));
        n[x].y = pk2(gelu_t(mr[x].y * (v[2] - mr[x].x * c4[2])), gelu_t(mr[x].y * (v[3] - mr[x].x * c4[3])));
      }
      u32x4 fr = pairswap(n[0], n[1]);
      if (lb3[j] < nb) {
        int fb = fb0 + lb3[j];
        unsigned off = WS_Z1 + ((i * 8 + rg * 2 + (q & 1)) * 22 + (fb >> 1)) * 1024 + (2 * (fb & 1) + (q >> 1)) * 256 + t * 16;
        __builtin_amdgcn_raw_buffer_store_b128(fr, c.rs, off, 0, 16);
      }
    }
  }
  wait_vm<0>();
  bar();
  if (c.tid == 0) gstore64(ws + WS_FZ1 + (i * 8 + zc) * 128, tagT(c));
  STAMP(6);
}

struct HAddr { unsigned va, vw; const char *bo, *bu, *bwo, *bwout; };
template <int T> DEV void h_issue_w(const Ctx& c, const HAddr& a) {
  const unsigned l = c.lb + ((32 + T) % 7) * G_SLOT + 8192 + c.w * 1024;
  if (T < 8) dma16(a.vw, a.bwo + (size_t)T * 64 * 1024, l);
  else if (T < 18) dma16(a.vw, a.bwout + (size_t)(T - 8) * 64 * 1024, l);
  else {
    int k = 8 * c.w + (c.lane >> 3);
    unsigned v = k > 47 ? a.vw - (k - 47) * 1024 : a.vw;
    dma16(v, a.bwout + (size_t)640 * 1024, l);
  }
}
template <int T> DEV void h_issue_a(const Ctx& c, const HAddr& a) {
  const unsigned l = c.lb + ((32 + T) % 7) * G_SLOT + c.w * 1024;
  if (T < 8) dma16c(a.va, a.bo + T * 2048, l);
  else dma16c(a.va, a.bu + (T - 8) * 2048, l);
}
struct HF { u32x4 b[2], f0[2], f1[2]; };
template <int T> DEV void h_read(const Ctx& c, HF& F, unsigned ao, unsigned w0, unsigned w1) {
  LAS char* L = c.L + ((32 + T) % 7) * G_SLOT;
#pragma unroll
  for (int kk = 0; kk < 2; ++kk) {
    F.b[kk] = lds128(L + ao + kk * 4096);
    F.f0[kk] = trfrag(L + 8192 + w0 + kk * 4096, 512);
    F.f1[kk] = trfrag(L + 8192 + w1 + kk * 4096, 512);
  }
  sb();
}
template <int T, int IA, int IW> DEV void h_mma(const Ctx& c, const HAddr& h, const HF& F, f32x4 (&acc)[2]) {
  if (IA >= 0) h_issue_a<(IA < 0 ? 0 : IA)>(c, h);
  sb();
  acc[0] = mfma32(F.f0[0], F.b[0], acc[0]);
  acc[1] = mfma32(F.f1[0], F.b[0], acc[1]);
  sb();
  if (IW >= 0) h_issue_w<(IW < 0 ? 0 : IW)>(c, h);
  sb();
  u32x4 b = F.b[1];
  if (T == 18) {
    if (c.lane >= 32) { u32x4 z = {0, 0, 0, 0}; b = z; }
  }
  acc[0] = mfma32(F.f0[1], b, acc[0]);
  acc[1] = mfma32(F.f1[1], b, acc[1]);
  sb();
}
template <int T, int IA, int IW, bool NEXT> DEV void h_it(const Ctx& c, const HAddr& h, HF& Fc, HF& Fn, f32x4 (&acc)[2],
                                                         unsigned ao, unsigned w0, unsigned w1) {
  bar();
  if (NEXT) h_read<T + 1>(c, Fn, ao, w0, w1);
  h_mma<T, IA, IW>(c, h, Fc, acc);
}

template <int V> struct IC { static constexpr int v = V; };
struct GF { u32x4 b0, b1, a0, a1, a2; };

DEV void job_g(Ctx& c, int rt, int gc, const G3A& g3, bool epoch_wg) {
  char* ws = c.ws;
  const int lane = c.lane, w = c.w;
  const int t = lane & 15, q = lane >> 4;
  const int rg = w & 1, cg = (w >> 1) & 1, kh = w >> 2;
  const int fb0 = fb_start(gc), nb = gc < 3 ? 6 : 5;
  const u64 T = tagT(c);
  float bias = blo(*(const GAS u16*)(size_t)(c.bsp + ((rt * 4 + rg * 2 + kh) * 16 + t) * 2));
  if (w == 0) poll_flags(ws + WS_FZ2 + (gc * 16 + (lane & 15)) * 128, lane < 16, T);
  asm volatile("" : "+v"(bias));
  bar();
  STAMP(8);
  const char* zb = ws + WS_Z2N + (size_t)fb0 * 1024;
  const unsigned vz1 = ((w / 6) * 44 + (w % 6)) * 1024 + lane * 16;
  const int u2 = 8 + (w >> 1);
  const unsigned vz2 = (44 + (u2 - 6)) * 1024 + (w & 1) * 512 + (lane & 31) * 16;
  const unsigned lz1 = c.lb + G_Z + w * 1024, lz2 = c.lb + G_Z + u2 * 1024 + (w & 1) * 512;
#pragma unroll
  for (int s = 0; s < 7; ++s) {
    dma16c(vz1, zb + (size_t)s * 88 * 1024, lz1 + s * G_SLOT);
    dma16mc(vz2, zb + (size_t)s * 88 * 1024, lz2 + s * G_SLOT, 0xffffffffull);
  }
  HAddr h;
  {
    h.va = ((w & 3) * 16 + (w >> 2)) * 1024 + lane * 16;
    int k = 8 * w + (lane >> 3);
    int f = ((k >> 1) & 1) | (((k >> 3) & 1) << 1);
    h.vw = k * 1024 + (((lane & 7) ^ (f << 1)) << 4);
    h.bo = ws + WS_O + (size_t)rt * 4 * 16 * 1024;
    h.bu = ws + WS_U + (size_t)rt * 4 * 22 * 1024;
    h.bwo = c.wo + gc * 128;
    h.bwout = c.wout + gc * 128;
  }
  const unsigned vau = ((w & 3) * 22 + (w >> 2)) * 1024 + lane * 16;
  const unsigned bo_ = rg * 32 * 128 + t * 128 + (((4 * kh + q) ^ (t >> 1)) << 4);
  const unsigned ao_ = G_Z + (kh * 6 + 3 * cg) * 1024 + lane * 16;
  const f32x4 z4 = {0.f, 0.f, 0.f, 0.f};
  f32x4 acc[2][3];
#pragma unroll
  for (int j = 0; j < 3; ++j) { acc[0][j] = z4; acc[1][j] = z4; }
  const int tbk = rt * 4 + rg * 2 + kh;
  const unsigned flofs = lane < 8 ? WS_FO + ((rt >> 1) * 8 + lane) * 128 : WS_FZ1 + ((rt >> 1) * 8 + gc) * 128;
  unsigned z1ofs[3];
#pragma unroll
  for (int j = 0; j < 3; ++j) {
    int fb = fb0 + 3 * cg + j; fb = fb > 42 ? 42 : fb;
    z1ofs[j] = (tbk * 22 + (fb >> 1)) * 1024 + (2 * (fb & 1) + (q >> 1)) * 256 + t * 16 + (q & 1) * 8;
  }
  unsigned slot = 0;
  const char* pw = g3.bw + 7 * 128;
  const char* pz = zb + (size_t)7 * 88 * 1024;
  auto rd = [&](GF& F, unsigned sl) {
    LAS char* L = c.L + sl;
    F.a0 = lds128(L + ao_); F.b0 = lds128(L + bo_); F.b1 = lds128(L + bo_ + 2048);
    F.a1 = lds128(L + ao_ + 1024); F.a2 = lds128(L + ao_ + 2048);
    sb();
  };
  auto it = [&](auto kind, auto ht, GF& Fc, GF& Fn, bool next) {
    constexpr int KIND = decltype(kind)::v, HT = decltype(ht)::v;
    bar();
    const unsigned nsl = slot == 6 * G_SLOT ? 0 : slot + G_SLOT;
    if (KIND == 0) dma16(g3.vw, pw, c.lb + slot + w * 1024);
    if (KIND == 1) h_issue_a<HT>(c, h);
    sb();
    if (next) rd(Fn, nsl);
    acc[0][0] = mfma32(Fc.a0, Fc.b0, acc[0][0]); acc[1][0] = mfma32(Fc.a0, Fc.b1, acc[1][0]);
    acc[0][1] = mfma32(Fc.a1, Fc.b0, acc[0][1]);
    sb();
    if (KIND == 0) dma16c(vz1, pz, lz1 + slot);
    if (KIND == 1) h_issue_w<HT>(c, h);
    sb();
    acc[1][1] = mfma32(Fc.a1, Fc.b1, acc[1][1]);
    acc[0][2] = mfma32(Fc.a2, Fc.b0, acc[0][2]); acc[1][2] = mfma32(Fc.a2, Fc.b1, acc[1][2]);
    sb();
    if (KIND == 0) { dma16mc(vz2, pz, lz2 + slot, 0xffffffffull); pw += 128; pz += 88 * 1024; }
    sb();
    slot = nsl;
  };
  const IC<0> K0; const IC<1> K1; const IC<2> K2;
  GF F0, F1;
  wait_vm<12>();
  bar();
  rd(F0, 0);
  wait_vm<10>(); it(K0, K0, F0, F1, true);
  wait_vm<11>(); it(K0, K0, F1, F0, true);
  wait_vm<12>(); it(K0, K0, F0, F1, true);
  wait_vm<13>(); it(K0, K0, F1, F0, true);
  wait_vm<14>(); it(K0, K0, F0, F1, true);
  wait_vm<15>(); it(K0, K0, F1, F0, true);
#pragma unroll 1
  for (int s = 6; s < 10; s += 2) { wait_vm<15>(); it(K0, K0, F0, F1, true); wait_vm<15>(); it(K0, K0, F1, F0, true); }
  u32x2 fl = aload64c(flofs, ws);
#pragma unroll 1
  for (int s = 10; s < 16; s += 2) { wait_vm<15>(); it(K0, K0, F0, F1, true); wait_vm<15>(); it(K0, K0, F1, F0, true); }
  asm volatile("; TIE %0" : "+v"(fl));
  if (!__all(fl.x == c.Tlo && fl.y == c.Thi)) poll_flags(ws + flofs, true, T);
  u32x2 z1v[3];
  z1v[0] = aload64c(z1ofs[0], ws + WS_Z1);
  z1v[1] = aload64c(z1ofs[1], ws + WS_Z1);
  z1v[2] = aload64c(z1ofs[2], ws + WS_Z1);
#pragma unroll 1
  for (int s = 16; s < 24; s += 2) { wait_vm<15>(); it(K0, K0, F0, F1, true); wait_vm<15>(); it(K0, K0, F1, F0, true); }
  wait_vm<15>(); it(K0, K0, F0, F1, true);
  wait_vm<15>(); it(K1, IC<0>(), F1, F0, true);
  wait_vm<14>(); it(K1, IC<1>(), F0, F1, true);
  wait_vm<13>(); it(K1, IC<2>(), F1, F0, true);
  wait_vm<12>(); it(K1, IC<3>(), F0, F1, true);
  wait_vm<11>(); it(K1, IC<4>(), F1, F0, true);
  wait_vm<10>(); it(K1, IC<5>(), F0, F1, true);
  it(K2, K0, F1, F0, false);
  asm volatile("; TIE %0 %1 %2" : "+v"(z1v[0]), "+v"(z1v[1]), "+v"(z1v[2]));
  STAMP(9);
  {
    const unsigned e01 = 3 * G_SLOT + w * 2048 + lane * 16, e2 = T_EX2 + w * 1024 + lane * 16;
    const unsigned p01 = 3 * G_SLOT + (w ^ 4) * 2048 + lane * 16, p2 = T_EX2 + (w ^ 4) * 1024 + lane * 16;
    f32x4 keep[3], send[3];
#pragma unroll
    for (int j = 0; j < 3; ++j) { keep[j] = kh ? acc[1][j] : acc[0][j]; send[j] = kh ? acc[0][j] : acc[1][j]; }
    *(LAS f32x4*)(c.L + e01) = send[0]; *(LAS f32x4*)(c.L + e01 + 1024) = send[1]; *(LAS f32x4*)(c.L + e2) = send[2];
    bar();
    keep[0] += *(LAS f32x4*)(c.L + p01); keep[1] += *(LAS f32x4*)(c.L + p01 + 1024); keep[2] += *(LAS f32x4*)(c.L + p2);
#pragma unroll
    for (int j = 0; j < 3; ++j) {
      unsigned g0 = pk2(keep[j][0] + bias, keep[j][1] + bias), g1 = pk2(keep[j][2] + bias, keep[j][3] + bias);
      unsigned a0 = z1v[j].x, a1 = z1v[j].y;
      u32x2 u;
      u.x = pk2(blo(a0) * blo(g0), bhi(a0) * bhi(g0));
      u.y = pk2(blo(a1) * blo(g1), bhi(a1) * bhi(g1));
      if (3 * cg + j < nb) __builtin_amdgcn_raw_buffer_store_b64(u, c.rs, WS_U + z1ofs[j], 0, 16);
    }
  }
  wait_vm<0>();
  bar();
  if (c.tid == 0) gstore64(ws + WS_FU + (rt * 8 + gc) * 128, T);
  STAMP(10);

  const int tb = w & 3, np = w >> 2;
  const unsigned ha = tb * 1024 + lane * 16;
  const int fw = ((t >> 3) & 1) | ((q & 1) << 1);
  const unsigned wl = (8 * q + (t >> 2)) * 128 + (t & 3) * 8;
  const unsigned hw0 = wl + (((2 * np) ^ fw) << 5), hw1 = wl + (((2 * np + 1) ^ fw) << 5);
  f32x4 sa[2] = {z4, z4};
  HF H0, H1;
  h_issue_a<6>(c, h); h_issue_w<6>(c, h);
  h_read<0>(c, H0, ha, hw0, hw1);
  h_it<0, 7, 7, true>(c, h, H0, H1, sa, ha, hw0, hw1);
  h_it<1, -1, 8, true>(c, h, H1, H0, sa, ha, hw0, hw1);
  h_it<2, -1, 9, true>(c, h, H0, H1, sa, ha, hw0, hw1);
  h_it<3, -1, 10, true>(c, h, H1, H0, sa, ha, hw0, hw1);
  h_it<4, -1, 11, true>(c, h, H0, H1, sa, ha, hw0, hw1);
  wait_vm<6>(); h_it<5, -1, 12, true>(c, h, H1, H0, sa, ha, hw0, hw1);
  wait_vm<5>(); h_it<6, -1, 13, true>(c, h, H0, H1, sa, ha, hw0, hw1);
  h_it<7, -1, 14, false>(c, h, H1, H0, sa, ha, hw0, hw1);
  asm volatile("" : "+v"(sa[0]), "+v"(sa[1]));
  STAMP(11);
  h.va = vau;
  if (w == 0) poll_flags(ws + WS_FU + (rt * 8 + (lane & 7)) * 128, lane < 8, T);
  if (epoch_wg && w == 1) {
#pragma unroll
    for (int j = 0; j < 2; ++j) {
      poll_flags(ws + WS_FZ2 + (j * 64 + lane) * 128, true, T);
      poll_flags(ws + WS_FO + (j * 64 + lane) * 128, true, T);
    }
    if (lane == 0) gstore64(ws + WS_EPOCH, T);
  }
  bar();
  STAMP(12);
  h_issue_a<8>(c, h); h_issue_a<9>(c, h); h_issue_a<10>(c, h); h_issue_a<11>(c, h); h_issue_a<12>(c, h); h_issue_a<13>(c, h);
  h_issue_a<14>(c, h);
  const int orow = rt * 64 + tb * 16 + t;
  const unsigned oofs = orow * 1024 + gc * 128 + np * 64 + (q & 1) * 32 + (q >> 1) * 16;
  f32x4 ga[2] = {z4, z4};
  wait_vm<6>();
  bar();
  h_read<8>(c, H0, ha, hw0, hw1);
  wait_vm<5>(); h_it<8, 15, 15, true>(c, h, H0, H1, ga, ha, hw0, hw1);
  wait_vm<6>(); h_it<9, 16, 16, true>(c, h, H1, H0, ga, ha, hw0, hw1);
  wait_vm<7>(); h_it<10, 17, 17, true>(c, h, H0, H1, ga, ha, hw0, hw1);
  wait_vm<8>(); h_it<11, 18, 18, true>(c, h, H1, H0, ga, ha, hw0, hw1);
  wait_vm<9>(); h_it<12, -1, -1, true>(c, h, H0, H1, ga, ha, hw0, hw1);
  u32x4 xres;
  asm volatile("global_load_dwordx4 %0, %1, %2" : "=v"(xres) : "v"(oofs), "s"(uni(c.x)) : "memory");
  wait_vm<9>(); h_it<13, -1, -1, true>(c, h, H1, H0, ga, ha, hw0, hw1);
  wait_vm<7>(); h_it<14, -1, -1, true>(c, h, H0, H1, ga, ha, hw0, hw1);
  wait_vm<5>(); h_it<15, -1, -1, true>(c, h, H1, H0, ga, ha, hw0, hw1);
  wait_vm<3>(); h_it<16, -1, -1, true>(c, h, H0, H1, ga, ha, hw0, hw1);
  wait_vm<1>(); h_it<17, -1, -1, true>(c, h, H1, H0, ga, ha, hw0, hw1);
  h_mma<18, -1, -1>(c, h, H0, ga);
  asm volatile("s_waitcnt vmcnt(0) ; TIE %0" : "+v"(xres) : : "memory");
  STAMP(13);
  {
    u32x2 S[2], G[2];
#pragma unroll
    for (int b = 0; b < 2; ++b) {
      S[b].x = pk2(sa[b][0], sa[b][1]); S[b].y = pk2(sa[b][2], sa[b][3]);
      G[b].x = pk2(ga[b][0], ga[b][1]); G[b].y = pk2(ga[b][2], ga[b][3]);
    }
    u32x4 s8 = pairswap(S[0], S[1]), g8 = pairswap(G[0], G[1]);
    u32x4 o;
#pragma unroll
    for (int j = 0; j < 4; ++j) {
      unsigned su = s8[j], gu = g8[j], xu = xres[j];
      float g0 = blo(gu), g1 = bhi(gu);
      float e0 = g0 * __builtin_amdgcn_rcpf(1.0f + __builtin_amdgcn_exp2f(g0 * -1.4426950408889634f));
      float e1 = g1 * __builtin_amdgcn_rcpf(1.0f + __builtin_amdgcn_exp2f(g1 * -1.4426950408889634f));
      o[j] = pk2(__builtin_fmaf(blo(su), e0, blo(xu)), __builtin_fmaf(bhi(su), e1, bhi(xu)));
    }
    if (OUTSC == 0) *(GAS u32x4*)(size_t)(c.out + oofs) = o;
    else {
      __amdgpu_buffer_rsrc_t ro = __builtin_amdgcn_make_buffer_rsrc((void*)c.out, 0, 2048 * 1024, 0x00020000);
      __builtin_amdgcn_raw_buffer_store_b128(o, ro, oofs, 0, OUTSC == 1 ? 16 : OUTSC == 2 ? 2 : 17);
    }
  }
  STAMP(14); STAMPC(31);
}

__global__ __launch_bounds__(512, 2) void fused_kernel(const char* x, const char* wqkv, const char* wo, const char* win, const char* wsp,
                                                       const char* bsp, const char* wout, char* out, char* ws) {
  extern __shared__ __attribute__((aligned(16))) char smem[];
  Ctx c;
  c.x = x; c.wqkv = wqkv; c.wo = wo; c.win = win; c.wsp = wsp; c.bsp = bsp; c.wout = wout; c.out = out; c.ws = ws;
  c.L = (LAS char*)smem;
  c.lb = (unsigned)(size_t)c.L;
  c.tid = threadIdx.x; c.lane = c.tid & 63; c.w = __builtin_amdgcn_readfirstlane(c.tid >> 6);
  c.rs = __builtin_amdgcn_make_buffer_rsrc((void*)ws, 0, WS_USED, 0x00020000);
  c.Tlo = 0; c.Thi = 0;
  c.bid = blockIdx.x;
  STAMP(0); STAMPC(30);
  const int bid = blockIdx.x, xcd = bid & 7, sl = bid >> 3;
  const int i = (xcd & 1) * 8 + (sl >> 2), rsel = sl & 3, col = 2 * (xcd >> 1) + (rsel & 1);
  const int r16 = (xcd >> 1) * 4 + rsel;
  const int rt = (xcd >> 1) * 8 + (sl >> 2), gc = (xcd & 1) * 4 + (sl & 3);
  const int s0row = i * 128 + r16 * 8 + c.w;
  const G3A g3 = g3_addr(c, rt);
  if (rsel < 2) job_a(c, i, col, s0row, g3, rt, rsel, xcd & 1);
  else job_z(c, i, col, s0row, g3);
  STAMP(7);
  job_g(c, rt, gc, g3, bid == 0);
}

extern "C" void kernel_launch(void* const* d_in, const int* in_sizes, int n_in, void* d_out, int out_size, void* d_ws, size_t ws_size,
                              hipStream_t stream) {
  (void)in_sizes; (void)n_in; (void)out_size; (void)ws_size;
  (void)hipFuncSetAttribute((const void*)fused_kernel, hipFuncAttributeMaxDynamicSharedMemorySize, LDS_BYTES);
  fused_kernel<<<256, 512, LDS_BYTES, stream>>>((const char*)d_in[0], (const char*)d_in[1], (const char*)d_in[2], (const char*)d_in[4],
                                                (const char*)d_in[5], (const char*)d_in[6], (const char*)d_in[7], (char*)d_out, (char*)d_ws);
}
```

```cpp
#include <hip/hip_runtime.h>
#define EXP 0
#define PF 2
#define ROT 0
#define GX 0
#define OUTSC 0

typedef unsigned short u16;
typedef unsigned long long u64;
typedef __bf16 bf16x8 __attribute__((ext_vector_type(8)));
typedef __bf16 bf16x2 __attribute__((ext_vector_type(2)));
typedef short s16x4 __attribute__((ext_vector_type(4)));
typedef float f32x4 __attribute__((ext_vector_type(4)));
typedef float f32x2 __attribute__((ext_vector_type(2)));
typedef unsigned u32x4 __attribute__((ext_vector_type(4)));
typedef unsigned u32x2 __attribute__((ext_vector_type(2)));
#define LAS __attribute__((address_space(3)))
#define GAS __attribute__((address_space(1)))
#define DEV __device__ __forceinline__

constexpr unsigned WS_EPOCH = 0;
constexpr unsigned WS_FZ2 = 4096;
constexpr unsigned WS_FZ1 = WS_FZ2 + 128 * 128;
constexpr unsigned WS_FO  = WS_FZ1 + 128 * 128;
constexpr unsigned WS_FU  = WS_FO + 128 * 128;
constexpr unsigned WS_XS  = 0x20000;
constexpr unsigned WS_ZS  = 0x30000;
constexpr unsigned WS_Z2N = 0x100000;
constexpr unsigned WS_Z1  = 0x400000;
constexpr unsigned WS_U   = 0x700000;
constexpr unsigned WS_O   = 0xA00000;
constexpr unsigned WS_DBG = 0xD00000;
constexpr unsigned WS_USED = 0xE00000;

constexpr unsigned LDS_BYTES = 160 * 1024;
constexpr unsigned A_SLOT = 43008, A_W = 18432;
constexpr unsigned Z_SLOT = 28672, Z_W = 16384;
constexpr unsigned G_SLOT = 20480, G_Z = 8192;
constexpr unsigned T_BASE = 143360;
constexpr unsigned T_EX2 = T_BASE;
constexpr unsigned T_XM = T_BASE + 8192;
constexpr unsigned T_XR = T_XM + 640;
constexpr unsigned T_CS = T_XM + 1280;
constexpr unsigned T_ZP = T_CS + 1024;
constexpr unsigned T_ZR = T_ZP + 2048;
constexpr unsigned T_ZB = T_ZR + 512;
constexpr unsigned T_DUMMY = T_ZB + 512;
static_assert(T_DUMMY + 256 <= LDS_BYTES, "lds");

constexpr unsigned SPIN_CAP = 200000;

#define STAMP1(k) do {} while (0)
#define STAMPC(k) do {} while (0)
#define STAMP(k) do {} while (0)

DEV const char* uni(const char* p) {
  u64 v = (u64)(size_t)p;
  unsigned lo = __builtin_amdgcn_readfirstlane((unsigned)v), hi = __builtin_amdgcn_readfirstlane((unsigned)(v >> 32));
  return (const char*)(size_t)(((u64)hi << 32) | lo);
}
DEV unsigned uni(unsigned v) { return __builtin_amdgcn_readfirstlane(v); }
struct Ctx0 { unsigned sink; };
DEV void dma16(unsigned voff, const char* sbase, unsigned lds) {
  asm volatile("s_mov_b32 m0, %2\n\ts_nop 0\n\tglobal_load_lds_dwordx4 %0, %1" ::"v"(voff), "s"(uni(sbase)), "s"(uni(lds)) : "memory");
}
DEV void dma16c(unsigned voff, const char* sbase, unsigned lds) {
  asm volatile("s_mov_b32 m0, %2\n\ts_nop 0\n\tglobal_load_lds_dwordx4 %0, %1 sc1" ::"v"(voff), "s"(uni(sbase)), "s"(uni(lds)) : "memory");
}
DEV void dma16m(unsigned voff, const char* sbase, unsigned lds, u64 mask) {
  asm volatile("s_mov_b32 m0, %2\n\ts_mov_b64 exec, %3\n\tglobal_load_lds_dwordx4 %0, %1\n\ts_mov_b64 exec, -1"
               ::"v"(voff), "s"(uni(sbase)), "s"(uni(lds)), "s"(mask) : "memory");
}
DEV void dma16mc(unsigned voff, const char* sbase, unsigned lds, u64 mask) {
  asm volatile("s_mov_b32 m0, %2\n\ts_mov_b64 exec, %3\n\tglobal_load_lds_dwordx4 %0, %1 sc1\n\ts_mov_b64 exec, -1"
               ::"v"(voff), "s"(uni(sbase)), "s"(uni(lds)), "s"(mask) : "memory");
}
DEV void touch(const Ctx0& c, const char* p) {
  asm volatile("s_mov_b32 m0, %1\n\ts_nop 0\n\tglobal_load_lds_dword %0, off" ::"v"(p), "s"(uni(c.sink)) : "memory");
}
template <int N> DEV void wait_vm() { asm volatile("s_waitcnt vmcnt(%0)" ::"n"(N) : "memory"); }
DEV void sb() { __builtin_amdgcn_sched_barrier(0); }
DEV void bar() { asm volatile("s_waitcnt lgkmcnt(0)\n\ts_barrier" ::: "memory"); }

template <int V = 0> DEV f32x4 mfmaX(u32x4 a, u32x4 b, f32x4 c) {
  return __builtin_amdgcn_mfma_f32_16x16x32_bf16(__builtin_bit_cast(bf16x8, a), __builtin_bit_cast(bf16x8, b), c, 0, 0, 0);
}
DEV f32x4 mfma32(u32x4 a, u32x4 b, f32x4 c) {
  return __builtin_amdgcn_mfma_f32_16x16x32_bf16(__builtin_bit_cast(bf16x8, a), __builtin_bit_cast(bf16x8, b), c, 0, 0, 0);
}
DEV f32x4 mfma16(u32x2 a, u32x2 b, f32x4 c) {
  return __builtin_amdgcn_mfma_f32_16x16x16bf16_1k(__builtin_bit_cast(s16x4, a), __builtin_bit_cast(s16x4, b), c, 0, 0, 0);
}
DEV u32x4 lds128(LAS char* p) { return *(LAS u32x4*)p; }
DEV u32x2 lds64(LAS char* p) { return *(LAS u32x2*)p; }
DEV u32x4 trfrag(LAS char* p, int off4) {
  s16x4 a = __builtin_amdgcn_ds_read_tr16_b64_v4i16((LAS s16x4*)p);
  s16x4 b = __builtin_amdgcn_ds_read_tr16_b64_v4i16((LAS s16x4*)(p + off4));
  u32x2 ua = __builtin_bit_cast(u32x2, a), ub = __builtin_bit_cast(u32x2, b);
  u32x4 r = {ua.x, ua.y, ub.x, ub.y};
  return r;
}
DEV unsigned pk2(float a, float b) {
  f32x2 v = {a, b};
  bf16x2 h = __builtin_convertvector(v, bf16x2);
  return __builtin_bit_cast(unsigned, h);
}
DEV float blo(unsigned u) { return __uint_as_float(u << 16); }
DEV float bhi(unsigned u) { return __uint_as_float(u & 0xffff0000u); }
template <int CTRL> DEV float dpp(float v) {
  return __uint_as_float(__builtin_amdgcn_update_dpp(0u, __float_as_uint(v), CTRL, 0xf, 0xf, true));
}
DEV float sum16(float v) {
  v += dpp<0x128>(v); v += dpp<0x124>(v); v += dpp<0x122>(v); v += dpp<0x121>(v);
  return v;
}
DEV float sumq(float v) {
  unsigned u = __float_as_uint(v);
  auto a = __builtin_amdgcn_permlane16_swap(u, u, false, false);
  v = __uint_as_float(a[0]) + __uint_as_float(a[1]);
  u = __float_as_uint(v);
  auto b = __builtin_amdgcn_permlane32_swap(u, u, false, false);
  return __uint_as_float(b[0]) + __uint_as_float(b[1]);
}
DEV float maxq(float v) {
  unsigned u = __float_as_uint(v);
  auto a = __builtin_amdgcn_permlane16_swap(u, u, false, false);
  v = fmaxf(__uint_as_float(a[0]), __uint_as_float(a[1]));
  u = __float_as_uint(v);
  auto b = __builtin_amdgcn_permlane32_swap(u, u, false, false);
  return fmaxf(__uint_as_float(b[0]), __uint_as_float(b[1]));
}
template <bool MX> DEV float op2(float a, float b) { return MX ? fmaxf(a, b) : a + b; }
template <bool MX> DEV void rows4(float (&v)[4]) {
  auto f = [](float x) { return __float_as_uint(x); };
  auto g = [](unsigned x) { return __uint_as_float(x); };
  auto a = __builtin_amdgcn_permlane16_swap(f(v[0]), f(v[1]), false, false);
  auto b = __builtin_amdgcn_permlane16_swap(f(v[2]), f(v[3]), false, false);
  float m1 = op2<MX>(g(a[0]), g(a[1])), m2 = op2<MX>(g(b[0]), g(b[1]));
  auto d = __builtin_amdgcn_permlane32_swap(f(m1), f(m2), false, false);
  unsigned r = f(op2<MX>(g(d[0]), g(d[1])));
  auto e = __builtin_amdgcn_permlane16_swap(r, r, false, false);
  auto ac = __builtin_amdgcn_permlane32_swap(e[0], e[0], false, false);
  auto bd = __builtin_amdgcn_permlane32_swap(e[1], e[1], false, false);
  v[0] = g(ac[0]); v[2] = g(ac[1]); v[1] = g(bd[0]); v[3] = g(bd[1]);
}
DEV u32x4 pairswap(u32x2 b0, u32x2 b1) {
  auto s0 = __builtin_amdgcn_permlane16_swap(b0.x, b1.x, false, false);
  auto s1 = __builtin_amdgcn_permlane16_swap(b0.y, b1.y, false, false);
  u32x4 r = {s0[0], s1[0], s0[1], s1[1]};
  return r;
}
DEV f32x2 fma2(f32x2 a, f32x2 b, f32x2 c) { return __builtin_elementwise_fma(a, b, c); }
DEV f32x2 splat(float v) { f32x2 r = {v, v}; return r; }
DEV f32x2 lo2(f32x4 v) { return __builtin_shufflevector(v, v, 0, 1); }
DEV f32x2 hi2(f32x4 v) { return __builtin_shufflevector(v, v, 2, 3); }
DEV unsigned pk2(f32x2 v) { return __builtin_bit_cast(unsigned, __builtin_convertvector(v, bf16x2)); }
DEV f32x2 unpk(unsigned u) { f32x2 r = {blo(u), bhi(u)}; return r; }
DEV f32x2 gelu2(f32x2 v) {
  f32x2 a = v * fma2(v * v, splat(-0.10294324f), splat(-2.3022082f));
  f32x2 e = {__builtin_amdgcn_exp2f(a.x), __builtin_amdgcn_exp2f(a.y)};
  e = e + 1.0f;
  f32x2 r = {__builtin_amdgcn_rcpf(e.x), __builtin_amdgcn_rcpf(e.y)};
  return v * r;
}
DEV void red16(const float (&a)[2][8], float (&r)[2][2]) {
  float m0, m1, m2, m3, m4, m5, m6, m7;
  asm volatile(
      "s_nop 1\n\t"
      "v_add_f32_dpp %0, %4, %4 row_ror:8 row_mask:0xf bank_mask:0x3\n\t"
      "v_add_f32_dpp %1, %5, %5 row_ror:8 row_mask:0xf bank_mask:0x3\n\t"
      "v_add_f32_dpp %2, %8, %8 row_ror:8 row_mask:0xf bank_mask:0x3\n\t"
      "v_add_f32_dpp %3, %9, %9 row_ror:8 row_mask:0xf bank_mask:0x3\n\t"
      "v_add_f32_dpp %0, %6, %6 row_ror:8 row_mask:0xf bank_mask:0xc\n\t"
      "v_add_f32_dpp %1, %7, %7 row_ror:8 row_mask:0xf bank_mask:0xc\n\t"
      "v_add_f32_dpp %2, %10, %10 row_ror:8 row_mask:0xf bank_mask:0xc\n\t"
      "v_add_f32_dpp %3, %11, %11 row_ror:8 row_mask:0xf bank_mask:0xc"
      : "=&v"(m0), "=&v"(m1), "=&v"(m2), "=&v"(m3)
      : "v"(a[0][0]), "v"(a[0][1]), "v"(a[0][2]), "v"(a[0][3]), "v"(a[0][4]), "v"(a[0][5]), "v"(a[0][6]), "v"(a[0][7]));
  asm volatile(
      "s_nop 1\n\t"
      "v_add_f32_dpp %4, %8, %8 row_ror:8 row_mask:0xf bank_mask:0x3\n\t"
      "v_add_f32_dpp %5, %9, %9 row_ror:8 row_mask:0xf bank_mask:0x3\n\t"
      "v_add_f32_dpp %6, %12, %12 row_ror:8 row_mask:0xf bank_mask:0x3\n\t"
      "v_add_f32_dpp %7, %13, %13 row_ror:8 row_mask:0xf bank_mask:0x3\n\t"
      "v_add_f32_dpp %4, %10, %10 row_ror:8 row_mask:0xf bank_mask:0xc\n\t"
      "v_add_f32_dpp %5, %11, %11 row_ror:8 row_mask:0xf bank_mask:0xc\n\t"
      "v_add_f32_dpp %6, %14, %14 row_ror:8 row_mask:0xf bank_mask:0xc\n\t"
      "v_add_f32_dpp %7, %15, %15 row_ror:8 row_mask:0xf bank_mask:0xc\n\t"
      "v_add_f32_dpp %0, %0, %0 row_ror:12 row_mask:0xf bank_mask:0x5\n\t"
      "v_add_f32_dpp %2, %2, %2 row_ror:12 row_mask:0xf bank_mask:0x5\n\t"
      "v_add_f32_dpp %4, %4, %4 row_ror:12 row_mask:0xf bank_mask:0x5\n\t"
      "v_add_f32_dpp %6, %6, %6 row_ror:12 row_mask:0xf bank_mask:0x5\n\t"
      "v_add_f32_dpp %0, %1, %1 row_ror:4 row_mask:0xf bank_mask:0xa\n\t"
      "v_add_f32_dpp %2, %3, %3 row_ror:4 row_mask:0xf bank_mask:0xa\n\t"
      "v_add_f32_dpp %4, %5, %5 row_ror:4 row_mask:0xf bank_mask:0xa\n\t"
      "v_add_f32_dpp %6, %7, %7 row_ror:4 row_mask:0xf bank_mask:0xa\n\t"
      "v_add_f32_dpp %0, %0, %0 quad_perm:[1,0,3,2] row_mask:0xf bank_mask:0xf\n\t"
      "v_add_f32_dpp %2, %2, %2 quad_perm:[1,0,3,2] row_mask:0xf bank_mask:0xf\n\t"
      "v_add_f32_dpp %4, %4, %4 quad_perm:[1,0,3,2] row_mask:0xf bank_mask:0xf\n\t"
      "v_add_f32_dpp %6, %6, %6 quad_perm:[1,0,3,2] row_mask:0xf bank_mask:0xf\n\t"
      "v_add_f32_dpp %0, %0, %0 quad_perm:[2,3,0,1] row_mask:0xf bank_mask:0xf\n\t"
      "v_add_f32_dpp %2, %2, %2 quad_perm:[2,3,0,1] row_mask:0xf bank_mask:0xf\n\t"
      "v_add_f32_dpp %4, %4, %4 quad_perm:[2,3,0,1] row_mask:0xf bank_mask:0xf\n\t"
      "v_add_f32_dpp %6, %6, %6 quad_perm:[2,3,0,1] row_mask:0xf bank_mask:0xf\n\t"
      "s_nop 1"
      : "+v"(m0), "+v"(m1), "+v"(m2), "+v"(m3), "=&v"(m4), "=&v"(m5), "=&v"(m6), "=&v"(m7)
      : "v"(a[1][0]), "v"(a[1][1]), "v"(a[1][2]), "v"(a[1][3]), "v"(a[1][4]), "v"(a[1][5]), "v"(a[1][6]), "v"(a[1][7]));
  r[0][0] = m0; r[0][1] = m2; r[1][0] = m4; r[1][1] = m6;
}
DEV u64 gload64(const char* p) { return __hip_atomic_load((GAS u64*)(size_t)p, __ATOMIC_RELAXED, __HIP_MEMORY_SCOPE_AGENT); }
DEV void gstore64(const char* p, u64 v) { __hip_atomic_store((GAS u64*)(size_t)p, v, __ATOMIC_RELAXED, __HIP_MEMORY_SCOPE_AGENT); }
DEV void poll_flags(const char* p, bool active, u64 T) {
  for (unsigned it = 0; it < SPIN_CAP; ++it) {
    bool ok = !active || gload64(p) == T;
    if (__all(ok)) break;
    __builtin_amdgcn_s_sleep(1);
  }
}
DEV u32x4 settle_pair(u32x4 g, __amdgpu_buffer_rsrc_t rs, unsigned off, bool active, unsigned T32) {
  for (unsigned it = 0; it < SPIN_CAP; ++it) {
    bool ok = !active || (g.y == T32 && g.w == T32);
    if (__all(ok)) break;
    __builtin_amdgcn_s_sleep(1);
    g = __builtin_amdgcn_raw_buffer_load_b128(rs, off, 0, 16);
  }
  return g;
}
DEV u32x4 aload128c(unsigned voff, const char* sbase) {
  u32x4 r;
  asm volatile("global_load_dwordx4 %0, %1, %2 sc1" : "=v"(r) : "v"(voff), "s"(uni(sbase)) : "memory");
  return r;
}
DEV u32x2 aload64c(unsigned voff, const char* sbase) {
  u32x2 r;
  asm volatile("global_load_dwordx2 %0, %1, %2 sc1" : "=v"(r) : "v"(voff), "s"(uni(sbase)) : "memory");
  return r;
}

struct Ctx {
  const char *x, *wqkv, *wo, *win, *wsp, *bsp, *wout;
  char *out, *ws;
  LAS char* L;
  unsigned lb;
  int tid, lane, w, bid;
  unsigned Tlo, Thi;
  __amdgpu_buffer_rsrc_t rs;
};
DEV u64 tagT(const Ctx& c) { return ((u64)c.Thi << 32) | c.Tlo; }

DEV void s0_issue(const Ctx& c, u32x4& xr, u32x2& ev, int row) {
  ev = aload64c(0, c.ws + WS_EPOCH);
  asm volatile("global_load_dwordx4 %0, %1, %2" : "=v"(xr) : "v"(c.lane * 16), "s"(uni(c.x + (size_t)row * 1024)) : "memory");
}
template <int P> DEV void s0_finish(Ctx& c, u32x4 xr, u32x2 ev, int row) {
  asm volatile("s_waitcnt vmcnt(%2) ; TIE %0 %1" : "+v"(xr), "+v"(ev) : "n"(P) : "memory");
  u64 e = (((u64)(unsigned)__builtin_amdgcn_readfirstlane(ev.y)) << 32) | (unsigned)__builtin_amdgcn_readfirstlane(ev.x);
  e += 1;
  c.Tlo = (unsigned)e; c.Thi = (unsigned)(e >> 32);
  unsigned x0 = xr.x, x1 = xr.y, x2 = xr.z, x3 = xr.w;
  float v[8] = {blo(x0), bhi(x0), blo(x1), bhi(x1), blo(x2), bhi(x2), blo(x3), bhi(x3)};
  float s = ((v[0] + v[1]) + (v[2] + v[3])) + ((v[4] + v[5]) + (v[6] + v[7]));
  s = sumq(sum16(s));
  float m = s * (1.0f / 512.0f), q = 0.f;
#pragma unroll
  for (int j = 0; j < 8; ++j) { float d = v[j] - m; q = __builtin_fmaf(d, d, q); }
  q = sumq(sum16(q));
  float r = __builtin_amdgcn_rsqf(q * (1.0f / 512.0f) + 1e-5f);
  if (c.lane < 2)
    gstore64(c.ws + WS_XS + row * 16 + c.lane * 8, ((u64)c.Tlo << 32) | __float_as_uint(c.lane ? r : m));
}

struct G3A { unsigned vw; const char* bw; unsigned k0; };
DEV G3A g3_addr(const Ctx& c, int rt, int sl) {
  int r = 8 * c.w + (c.lane >> 3);
  G3A a;
  a.vw = r * 4096 + (((c.lane & 7) ^ ((r >> 1) & 7)) << 4);
  a.bw = c.wsp + (size_t)rt * 64 * 4096;
  a.k0 = ROT == 0 ? 0 : ROT == 1 ? sl : ROT == 2 ? (sl >> 2) * 4 : (sl & 3) * 8;
  return a;
}
DEV void g3_prefetch_w(const Ctx& c, const G3A& a) {
#pragma unroll
  for (int s = 0; s < 7; ++s) dma16(a.vw, a.bw + ((a.k0 + s) & 31) * 128, c.lb + s * G_SLOT + c.w * 1024);
}

DEV void tlb_touch(const Ctx& c) {
  const int n = c.tid;
  const char* p = c.ws + (size_t)n * 65536;
  if (n >= 224) p = c.wsp + (size_t)(n - 224) * 65536;
  if (n >= 352) p = c.out + (size_t)(n - 352) * 65536;
  if (n >= 384) p = c.wo + (size_t)(n - 384) * 65536;
  if (n >= 392) p = c.wout + (size_t)(n - 392) * 65536;
  if (n >= 403) p = c.bsp;
  Ctx0 k = {c.lb + T_DUMMY};
  touch(k, p);
}
DEV void l2_warm(const Ctx& c, int rt, int a16, int half, int xcol) {
  Ctx0 k = {c.lb + T_DUMMY};
#pragma unroll
  for (int j = 0; j < 2; ++j) {
    int row = rt * 64 + half * 32 + 4 * c.w + 2 * j + (c.lane >> 5);
    touch(k, c.wsp + (size_t)row * 4096 + (c.lane & 31) * 128);
  }
  int n = c.w * 64 + c.lane; n = n < 300 ? n : 299;
  int row = a16 * 75 + (n >> 2);
  const char* p = row < 512 ? c.wo + (size_t)row * 1024 : c.wout + (size_t)(row - 512) * 1024;
  if (c.w < 5) touch(k, p + xcol * 512 + (n & 3) * 128);
}

struct AAddr { unsigned vx0, vx1, vh, vw; const char *bx, *bh, *bw; u64 hmask; };
template <int H, int P> DEV void a_dma(const Ctx& c, const AAddr& a, int s, unsigned slot) {
  const unsigned l = c.lb + slot;
  if (H == 0) {
    if (P == 0) dma16(a.vx0, a.bx + s * 128, l + c.w * 2048);
    if (P == 1) dma16(a.vx1, a.bx + s * 128, l + c.w * 2048 + 1024);
    if (P == 2) dma16m(a.vh, a.bh + s * 128, l + 128 * 128 + c.w * 256, a.hmask);
  } else {
    dma16(a.vw, a.bw + (size_t)s * 64 * 3072 + P * 1024, l + A_W + P * 8192 + c.w * 1024);
  }
}
template <int H> DEV void a_half(const Ctx& c, const AAddr& a, int s, unsigned slot) {
  a_dma<H, 0>(c, a, s, slot); a_dma<H, 1>(c, a, s, slot); a_dma<H, 2>(c, a, s, slot);
}

struct AAcc { f32x4 q[2][2], k[2][2], v[2][2], h, qc; };
struct AF { u32x4 x0, x1, hx, qa, qb, ka, kb, va, vb; };
struct AOfs { unsigned xo, xh, wA, wB; };

DEV void a_read(const Ctx& c, AF& F, unsigned slot, int kk, const AOfs& o) {
  LAS char* L = c.L + slot;
  LAS char* pa = L + A_W + o.wA + kk * 4096;
  LAS char* pb = L + A_W + o.wB + kk * 4096;
  F.qa = trfrag(pa, 512);
  F.x0 = lds128(L + (o.xo ^ (kk * 64)));
  F.x1 = lds128(L + (o.xo ^ (kk * 64)) + 2048);
  F.qb = trfrag(pb, 512);
  F.ka = trfrag(pa + 8192, 512); F.kb = trfrag(pb + 8192, 512);
  F.va = trfrag(pa + 16384, 512); F.vb = trfrag(pb + 16384, 512);
  F.hx = lds128(L + (o.xh ^ (kk * 64)));
  sb();
}
template <bool HV, int H, bool NEXT> DEV void a_step(const Ctx& c, AAcc& A, const AF& F, AF& N, unsigned rslot, int kk, const AOfs& o,
                                                     const AAddr& a, int sn, unsigned slot) {
  LAS char* L = c.L + rslot;
  LAS char* pa = L + A_W + o.wA + kk * 4096;
  LAS char* pb = L + A_W + o.wB + kk * 4096;
  LAS char* px = L + (o.xo ^ (kk * 64));
  sb();
  A.q[0][0] = mfmaX(F.qa, F.x0, A.q[0][0]); sb();
  if (NEXT) N.qa = trfrag(pa, 512);
  sb();
  A.q[1][0] = mfmaX(F.qa, F.x1, A.q[1][0]); sb();
  if (NEXT) { N.x0 = lds128(px); N.x1 = lds128(px + 2048); }
  sb();
  A.q[0][1] = mfmaX(F.qb, F.x0, A.q[0][1]); sb();
  if (NEXT) N.qb = trfrag(pb, 512);
  sb();
  A.q[1][1] = mfmaX(F.qb, F.x1, A.q[1][1]); sb();
  if (NEXT) N.ka = trfrag(pa + 8192, 512);
  sb();
  A.k[0][0] = mfmaX(F.ka, F.x0, A.k[0][0]); sb();
  if (NEXT) N.kb = trfrag(pb + 8192, 512);
  sb();
  A.k[1][0] = mfmaX(F.ka, F.x1, A.k[1][0]); sb();
  if (NEXT) N.va = trfrag(pa + 16384, 512);
  sb();
  A.k[0][1] = mfmaX(F.kb, F.x0, A.k[0][1]); sb();
  if (NEXT) N.vb = trfrag(pb + 16384, 512);
  sb();
  A.k[1][1] = mfmaX(F.kb, F.x1, A.k[1][1]); sb();
  if (NEXT) N.hx = lds128(L + (o.xh ^ (kk * 64)));
  sb();
  A.v[0][0] = mfmaX(F.x0, F.va, A.v[0][0]); sb();
  if (H >= 0) a_dma<(H < 0 ? 0 : H), 0>(c, a, sn, slot);
  sb();
  A.v[1][0] = mfmaX(F.x1, F.va, A.v[1][0]);
  A.v[0][1] = mfmaX(F.x0, F.vb, A.v[0][1]); sb();
  if (H >= 0) a_dma<(H < 0 ? 0 : H), 1>(c, a, sn, slot);
  sb();
  A.v[1][1] = mfmaX(F.x1, F.vb, A.v[1][1]);
  if (HV) {
    A.h = mfmaX(F.hx, F.va, A.h);
  } else {
    A.h = mfmaX(F.ka, F.hx, A.h);
  }
  sb();
  if (H >= 0) a_dma<(H < 0 ? 0 : H), 2>(c, a, sn, slot);
  sb();
  if (!HV) A.qc = mfmaX(F.qa, F.hx, A.qc);
  sb();
}

template <bool HV> DEV void a_loop(const Ctx& c, AAcc& A, const AAddr& a, const AOfs& o, u32x4& g, unsigned gofs) {
  AF F0, F1;
  wait_vm<9 + (PF & 1)>();
  bar();
  a_read(c, F0, 0, 0, o);
  unsigned s0 = 0, s1 = A_SLOT, s2 = 2 * A_SLOT;
#pragma unroll 1
  for (int s = 0; s < 5; ++s) {
    a_step<HV, 1, true>(c, A, F0, F1, s0, 1, o, a, s + 2, s2);
    wait_vm<6>();
    bar();
    STAMP1(16 + s);
    a_step<HV, 0, true>(c, A, F1, F0, s1, 0, o, a, s + 3, s0);
    unsigned t = s0; s0 = s1; s1 = s2; s2 = t;
  }
  a_step<HV, 1, true>(c, A, F0, F1, 2 * A_SLOT, 1, o, a, 7, A_SLOT);
  wait_vm<6>();
  bar();
  g = aload128c(gofs, c.ws + WS_XS);
  a_step<HV, -1, true>(c, A, F1, F0, 0, 0, o, a, 0, 0);
  a_step<HV, -1, true>(c, A, F0, F1, 0, 1, o, a, 0, 0);
  asm volatile("s_waitcnt vmcnt(0) ; TIE %0" : "+v"(g) : : "memory");
  bar();
  a_step<HV, -1, true>(c, A, F1, F0, A_SLOT, 0, o, a, 0, 0);
  a_step<HV, -1, true>(c, A, F0, F1, A_SLOT, 1, o, a, 0, 0);
  a_step<HV, -1, false>(c, A, F1, F0, 0, 0, o, a, 0, 0);
}

DEV void job_a(Ctx& c, int i, int hc, int s0row, const G3A& g3, int rt, int half, int xcol) {
  u32x4 xr; u32x2 ev;
  s0_issue(c, xr, ev, s0row);
  char* ws = c.ws;
  const int lane = c.lane, w = c.w, rg = w & 3, cg = w >> 2;
  const int t = lane & 15, q = lane >> 4;
  AAddr a;
  {
    int r0 = 16 * w + (lane >> 3);
    a.vx0 = r0 * 1024 + (((lane & 7) ^ ((r0 >> 1) & 7)) << 4);
    int r1 = r0 + 8;
    a.vx1 = r1 * 1024 + (((lane & 7) ^ ((r1 >> 1) & 7)) << 4);
    a.bx = c.x + (size_t)i * 128 * 1024;
    int rh = 2 * w + ((lane >> 3) & 1);
    a.vh = rh * 1024 + (((lane & 7) ^ w) << 4);
    a.bh = c.x + (size_t)(i > 0 ? i * 128 - 16 : 0) * 1024;
    a.hmask = w == 0 ? 0xff00ull : 0xffffull;
    int k = 8 * w + (lane >> 3);
    int f = ((k >> 1) & 1) | (((k >> 3) & 1) << 1);
    a.vw = k * 3072 + (((lane & 7) ^ (f << 1)) << 4);
    a.bw = c.wqkv + hc * 128;
  }
  if (c.tid < 24) {
    u32x4 ones = {0x3f803f80u, 0x3f803f80u, 0x3f803f80u, 0x3f803f80u};
    *(LAS u32x4*)(c.L + (c.tid >> 3) * A_SLOT + 128 * 128 + (c.tid & 7) * 16) = ones;
  }
  a_half<0>(c, a, 0, 0); a_half<1>(c, a, 0, 0);
  a_half<0>(c, a, 1, A_SLOT); a_half<1>(c, a, 1, A_SLOT);
  s0_finish<12>(c, xr, ev, s0row);
  STAMP(15);
  a_half<0>(c, a, 2, 2 * A_SLOT);
  if (PF & 1) tlb_touch(c);

  const unsigned xlane = t * 128 + ((q ^ (t >> 1)) << 4);
  const unsigned xo = rg * 32 * 128 + xlane, xh = 128 * 128 + xlane;
  const int f = ((t >> 3) & 1) | ((q & 1) << 1);
  const int hA = 2 * cg + (rg & 1), hB = hA ^ 1;
  const unsigned wl = (8 * q + (t >> 2)) * 128 + (t & 3) * 8;
  const unsigned wA = wl + ((hA ^ f) << 5), wB = wl + ((hB ^ f) << 5);
  const int gj = c.tid < 144 ? c.tid : 143;
  const int gtok = gj < 128 ? i * 128 + gj : (i > 0 ? i * 128 - 16 + (gj - 128) : gj - 128);
  const unsigned gofs = gtok * 16;

  AAcc A;
  const f32x4 z4 = {0.f, 0.f, 0.f, 0.f};
#pragma unroll
  for (int x = 0; x < 2; ++x)
#pragma unroll
    for (int y = 0; y < 2; ++y) { A.q[x][y] = z4; A.k[x][y] = z4; A.v[x][y] = z4; }
  A.h = z4; A.qc = z4;
  u32x4 g = {0, 0, 0, 0};
  const bool hv = rg >= 2;
  const AOfs ao = {xo, xh, wA, wB};
  if (hv) a_loop<true>(c, A, a, ao, g, gofs);
  else a_loop<false>(c, A, a, ao, g, gofs);
  STAMP(2);
  g = settle_pair(g, c.rs, WS_XS + gofs, true, c.Tlo);
  if (c.tid < 144) {
    *(LAS unsigned*)(c.L + T_XM + c.tid * 4) = g.x;
    *(LAS unsigned*)(c.L + T_XR + c.tid * 4) = g.z;
  }
  if (!hv) {
    if (t == 0) {
      *(LAS f32x4*)(c.L + T_CS + (64 + hA * 16 + 4 * q) * 4) = A.h;
      *(LAS f32x4*)(c.L + T_CS + (hA * 16 + 4 * q) * 4) = A.qc;
    }
  } else if (lane < 16) {
    *(LAS float*)(c.L + T_CS + (128 + hA * 16 + lane) * 4) = A.h[0];
  }
  bar();
  STAMP(18);
  g3_prefetch_w(c, g3);
  if (PF & 2) l2_warm(c, rt, (rt & 7) * 2 + half, half, xcol);

  STAMP(19);
  u32x2 Q[2][2], K[2][2], V[2][2], H;
  {
    f32x4 csq[2], csk[2]; float csv[2];
#pragma unroll
    for (int y = 0; y < 2; ++y) {
      int hh = y ? hB : hA;
      csq[y] = *(LAS f32x4*)(c.L + T_CS + (hh * 16 + 4 * q) * 4);
      csk[y] = *(LAS f32x4*)(c.L + T_CS + (64 + hh * 16 + 4 * q) * 4);
      csv[y] = *(LAS float*)(c.L + T_CS + (128 + hh * 16 + t) * 4);
    }
#pragma unroll
    for (int x = 0; x < 2; ++x) {
      const int tk = rg * 32 + x * 16;
      const float m1 = -*(LAS float*)(c.L + T_XM + (tk + t) * 4), r1 = *(LAS float*)(c.L + T_XR + (tk + t) * 4);
      const f32x4 m4 = *(LAS f32x4*)(c.L + T_XM + (tk + 4 * q) * 4), r4 = *(LAS f32x4*)(c.L + T_XR + (tk + 4 * q) * 4);
      const f32x2 nm = splat(m1), rk = splat(r1), rq = splat(r1 * 0.25f);
#pragma unroll
      for (int y = 0; y < 2; ++y) {
        f32x4 aq = A.q[x][y], ak = A.k[x][y], av = A.v[x][y];
        const f32x2 ncv = splat(-csv[y]);
        Q[x][y].x = pk2(rq * fma2(nm, lo2(csq[y]), lo2(aq)));
        Q[x][y].y = pk2(rq * fma2(nm, hi2(csq[y]), hi2(aq)));
        K[x][y].x = pk2(rk * fma2(nm, lo2(csk[y]), lo2(ak)));
        K[x][y].y = pk2(rk * fma2(nm, hi2(csk[y]), hi2(ak)));
        V[x][y].x = pk2(lo2(r4) * fma2(lo2(m4), ncv, lo2(av)));
        V[x][y].y = pk2(hi2(r4) * fma2(hi2(m4), ncv, hi2(av)));
      }
    }
    if (!hv) {
      const f32x2 nm = splat(-*(LAS float*)(c.L + T_XM + (128 + t) * 4)), rk = splat(*(LAS float*)(c.L + T_XR + (128 + t) * 4));
      H.x = pk2(rk * fma2(nm, lo2(csk[0]), lo2(A.h)));
      H.y = pk2(rk * fma2(nm, hi2(csk[0]), hi2(A.h)));
    } else {
      const f32x4 m4 = *(LAS f32x4*)(c.L + T_XM + (128 + 4 * q) * 4), r4 = *(LAS f32x4*)(c.L + T_XR + (128 + 4 * q) * 4);
      const f32x2 ncv = splat(-csv[0]);
      H.x = pk2(lo2(r4) * fma2(lo2(m4), ncv, lo2(A.h)));
      H.y = pk2(hi2(r4) * fma2(hi2(m4), ncv, hi2(A.h)));
    }
  }
  const unsigned KX = G_Z, VX = G_SLOT + G_Z;
  {
    *(LAS u32x2*)(c.L + (hv ? VX : KX) + hA * 512 + lane * 8) = H;
    if (rg < 3) {
      *(LAS u32x2*)(c.L + KX + ((rg + 1) * 4 + hA) * 512 + lane * 8) = K[1][0];
      *(LAS u32x2*)(c.L + KX + ((rg + 1) * 4 + hB) * 512 + lane * 8) = K[1][1];
      *(LAS u32x2*)(c.L + VX + ((rg + 1) * 4 + hA) * 512 + lane * 8) = V[1][0];
      *(LAS u32x2*)(c.L + VX + ((rg + 1) * 4 + hB) * 512 + lane * 8) = V[1][1];
    }
  }
  bar();
  STAMP(20);
  u32x2 Kp[2], Vp[2];
  Kp[0] = lds64(c.L + KX + (rg * 4 + hA) * 512 + lane * 8);
  Kp[1] = lds64(c.L + KX + (rg * 4 + hB) * 512 + lane * 8);
  Vp[0] = lds64(c.L + VX + (rg * 4 + hA) * 512 + lane * 8);
  Vp[1] = lds64(c.L + VX + (rg * 4 + hB) * 512 + lane * 8);

  const float NEG = -1e30f, LOG2E = 1.4426950408889634f;
  const bool odd = t & 1;
  const bool cur0 = 4 * q + (t & 1) <= t, cur1 = 4 * q + (t & 1) + 2 <= t;
  const bool first = (i == 0 && rg == 0);
  const float nb0 = (first && !cur0) ? NEG : 0.f, nb1 = (first && !cur1) ? NEG : 0.f;
  const unsigned s0 = odd ? 0x01000c0cu : 0x0c0c0100u, s1 = odd ? 0x03020c0cu : 0x0c0c0302u;
  const unsigned sPx = cur0 ? 0x0c0c0c0cu : s0, sPz = cur0 ? s0 : 0x0c0c0c0cu;
  const unsigned sPy = cur1 ? 0x0c0c0c0cu : s1, sPw = cur1 ? s1 : 0x0c0c0c0cu;
  float e0[2][2], e1[2][2];
#pragma unroll
  for (int x = 0; x < 2; ++x)
#pragma unroll
    for (int y = 0; y < 2; ++y) {
      u32x2 kp = x ? K[0][y] : Kp[y];
      f32x4 sp = mfma16(kp, Q[x][y], z4), sc = mfma16(K[x][y], Q[x][y], z4);
      float c0 = odd ? sc[1] : sc[0], p0 = odd ? sp[1] : sp[0], c1 = odd ? sc[3] : sc[2], p1 = odd ? sp[3] : sp[2];
      float a0 = cur0 ? c0 : p0, a1 = cur1 ? c1 : p1;
      e0[x][y] = x ? a0 * LOG2E : __builtin_fmaf(a0, LOG2E, nb0);
      e1[x][y] = x ? a1 * LOG2E : __builtin_fmaf(a1, LOG2E, nb1);
    }
  float red[4];
  red[0] = fmaxf(e0[0][0], e1[0][0]); red[1] = fmaxf(e0[0][1], e1[0][1]);
  red[2] = fmaxf(e0[1][0], e1[1][0]); red[3] = fmaxf(e0[1][1], e1[1][1]);
  rows4<true>(red);
#pragma unroll
  for (int k = 0; k < 4; ++k) {
    e0[k >> 1][k & 1] = __builtin_amdgcn_exp2f(e0[k >> 1][k & 1] - red[k]);
    e1[k >> 1][k & 1] = __builtin_amdgcn_exp2f(e1[k >> 1][k & 1] - red[k]);
  }
#pragma unroll
  for (int k = 0; k < 4; ++k) red[k] = e0[k >> 1][k & 1] + e1[k >> 1][k & 1];
  rows4<false>(red);
#pragma unroll
  for (int x = 1; x >= 0; --x) {
    u32x2 O[2];
#pragma unroll
    for (int y = 0; y < 2; ++y) {
      u32x2 vp = x ? V[0][y] : Vp[y];
      float inv = __builtin_amdgcn_rcpf(red[2 * x + y]);
      unsigned pw = pk2(e0[x][y] * inv, e1[x][y] * inv);
      u32x4 P = {__builtin_amdgcn_perm(0u, pw, sPx), __builtin_amdgcn_perm(0u, pw, sPy), __builtin_amdgcn_perm(0u, pw, sPz),
                 __builtin_amdgcn_perm(0u, pw, sPw)};
      u32x4 VV = {vp.x, vp.y, V[x][y].x, V[x][y].y};
      f32x4 o = mfma32(VV, P, z4);
      O[y].x = pk2(o[0], o[1]); O[y].y = pk2(o[2], o[3]);
    }
    u32x4 fr = pairswap(O[0], O[1]);
    int qp = (((q & 1) << 1) | (q >> 1)) ^ ((rg & 1) << 1);
    unsigned off = WS_O + ((i * 8 + rg * 2 + x) * 16 + 2 * hc + cg) * 1024 + qp * 256 + t * 16;
    __builtin_amdgcn_raw_buffer_store_b128(fr, c.rs, off, 0, 16);
  }
  STAMP(3);
}

struct ZAddr { unsigned vx0, vx1, vw; const char *bx, *bw; };
template <int S, int H, int P> DEV void z_dma(const Ctx& c, const ZAddr& a) {
  if (S >= 16 || S < 0) return;
  const unsigned l = c.lb + (S % 5) * Z_SLOT;
  const char* bw = a.bw + (size_t)(S & 7) * 64 * 2752 + (S < 8 ? 1376 : 0);
  if (S < 8) {
    if (H == 0) dma16(P ? a.vx1 : a.vx0, a.bx + S * 128, l + c.w * 2048 + P * 1024);
    else dma16m(a.vw, bw + P * 4 * 2752, l + Z_W + c.w * 1536 + P * 768, 0xffffffffffffull);
  } else if (P == 0) {
    dma16m(a.vw, bw + H * 4 * 2752, l + Z_W + c.w * 1536 + H * 768, 0xffffffffffffull);
  }
}
template <int S, int H> DEV void z_half(const Ctx& c, const ZAddr& a) { z_dma<S, H, 0>(c, a); z_dma<S, H, 1>(c, a); }
constexpr int z_n(int s) { return s >= 16 ? 0 : (s < 8 ? 4 : 2); }
constexpr int z_wait(int s) { return z_n(s + 1) + z_n(s + 2) + z_n(s + 3); }

struct ZW { u32x4 f[3]; };
template <int S, int KK> DEV void z_read_w(const Ctx& c, ZW& F, const unsigned (&wb)[3]) {
  LAS char* L = c.L + (S % 5) * Z_SLOT;
#pragma unroll
  for (int j = 0; j < 3; ++j) F.f[j] = trfrag(L + Z_W + wb[j] + KK * 6144, 768);
}
template <int S, int KK> DEV void z_read_x(const Ctx& c, u32x4 (&xf)[2][16], unsigned xo) {
  LAS char* L = c.L + (S % 5) * Z_SLOT;
  xf[0][2 * S + KK] = lds128(L + (xo ^ (KK * 64)));
  xf[1][2 * S + KK] = lds128(L + (xo ^ (KK * 64)) + 2048);
}
template <bool SWAP, int J, int RS, int RK, int SN, int H>
DEV void z_step(const Ctx& c, const ZAddr& a, u32x4 (&xf)[2][16], const ZW& F, ZW& N, f32x4 (&acc)[2][3], f32x4& cs, unsigned xo,
                const unsigned (&wb)[3]) {
  const u32x4 ones = {0x3f803f80u, 0x3f803f80u, 0x3f803f80u, 0x3f803f80u};
  constexpr int R = RS < 0 ? 0 : RS, JN = (2 * R + RK) & 15;
  LAS char* L = c.L + (R % 5) * Z_SLOT;
  auto mm = [&](int x, int j) { acc[x][j] = SWAP ? mfmaX(F.f[j], xf[x][J], acc[x][j]) : mfmaX(xf[x][J], F.f[j], acc[x][j]); };
  sb();
  mm(0, 0); sb();
  if (RS >= 0) N.f[0] = trfrag(L + Z_W + wb[0] + RK * 6144, 768);
  sb();
  mm(1, 0); sb();
  if (RS >= 0 && !SWAP) { xf[0][JN] = lds128(L + (xo ^ (RK * 64))); xf[1][JN] = lds128(L + (xo ^ (RK * 64)) + 2048); }
  if (RS >= 0 && SWAP) N.f[1] = trfrag(L + Z_W + wb[1] + RK * 6144, 768);
  sb();
  mm(0, 1); sb();
  if (RS >= 0 && !SWAP) N.f[1] = trfrag(L + Z_W + wb[1] + RK * 6144, 768);
  if (RS >= 0 && SWAP) N.f[2] = trfrag(L + Z_W + wb[2] + RK * 6144, 768);
  sb();
  mm(1, 1); sb();
  if (RS >= 0 && !SWAP) N.f[2] = trfrag(L + Z_W + wb[2] + RK * 6144, 768);
  sb();
  mm(0, 2); sb();
  z_dma<SN, H, 0>(c, a);
  sb();
  mm(1, 2); sb();
  z_dma<SN, H, 1>(c, a);
  sb();
  cs = SWAP ? mfmaX(F.f[0], ones, cs) : mfmaX(ones, F.f[0], cs);
  sb();
}
template <int S> DEV void z_stage_a(const Ctx& c, const ZAddr& a, u32x4 (&xf)[2][16], ZW& F0, ZW& F1, f32x4 (&acc)[2][3], f32x4& cs,
                                    unsigned xo, const unsigned (&wb)[3], u32x4& g, unsigned gofs) {
  z_step<false, 2 * S, S, 1, (S == 0 ? -1 : S + 4), 1>(c, a, xf, F0, F1, acc, cs, xo, wb);
  if (S == 7) asm volatile("s_waitcnt vmcnt(4) ; TIE %0" : "+v"(g) : : "memory");
  else wait_vm<z_wait(S + 1)>();
  bar();
  STAMP1(16 + S);
  if (S == 5) g = aload128c(gofs, c.ws + WS_XS);
  z_step<false, 2 * S + 1, (S < 7 ? S + 1 : -1), 0, S + 5, 0>(c, a, xf, F1, F0, acc, cs, xo, wb);
}
template <int S> DEV void z_stage_b(const Ctx& c, const ZAddr& a, u32x4 (&xf)[2][16], ZW& F0, ZW& F1, f32x4 (&acc)[2][3], f32x4& cs,
                                    const unsigned (&wb)[3], u32x4 (&g)[2], unsigned gofs, const char* gbase) {
  z_step<true, 2 * (S - 8), S, 1, S + 4, 1>(c, a, xf, F0, F1, acc, cs, 0, wb);
  if (S < 15) {
    wait_vm<z_wait(S + 1)>();
    bar();
    if (S == 14) { g[0] = aload128c(gofs, gbase); g[1] = aload128c(gofs, gbase + 2048); }
  }
  z_step<true, 2 * (S - 8) + 1, (S < 15 ? S + 1 : -1), 0, S + 5, 0>(c, a, xf, F1, F0, acc, cs, 0, wb);
  if (S == 15) asm volatile("s_waitcnt vmcnt(0) ; TIE %0 %1" : "+v"(g[0]), "+v"(g[1]) : : "memory");
}

DEV int fb_start(int c) { return c < 3 ? 6 * c : 18 + 5 * (c - 3); }

struct Z1Keep { f32x4 acc[2][3]; };
DEV void job_z(Ctx& c, int i, int zc, int s0row, const G3A& g3, Z1Keep& z1) {
  u32x4 xr; u32x2 ev;
  s0_issue(c, xr, ev, s0row);
  char* ws = c.ws;
  const int lane = c.lane, w = c.w, rg = w & 3, cg = w >> 2;
  const int t = lane & 15, q = lane >> 4;
  const int fb0 = fb_start(zc), nb = zc < 3 ? 6 : 5;
  ZAddr a;
  {
    int r0 = 16 * w + (lane >> 3);
    a.vx0 = r0 * 1024 + (((lane & 7) ^ ((r0 >> 1) & 7)) << 4);
    int r1 = r0 + 8;
    a.vx1 = r1 * 1024 + (((lane & 7) ^ ((r1 >> 1) & 7)) << 4);
    a.bx = c.x + (size_t)i * 128 * 1024;
    int l48 = lane < 48 ? lane : 47;
    int kr = l48 / 12, cc = l48 % 12;
    int b = (cc >> 1) ^ (w & 1);
    int fbg = fb0 + b; fbg = fbg > 42 ? 42 : fbg;
    a.vw = (8 * w + kr) * 2752 + fbg * 32 + (cc & 1) * 16;
    a.bw = c.win;
  }
  z_half<0, 0>(c, a); z_half<0, 1>(c, a); z_half<1, 0>(c, a); z_half<1, 1>(c, a); z_half<2, 0>(c, a); z_half<2, 1>(c, a);
  s0_finish<12>(c, xr, ev, s0row);
  STAMP(15);
  z_half<3, 0>(c, a); z_half<3, 1>(c, a); z_half<4, 0>(c, a); z_half<4, 1>(c, a);
  if (PF & 1) tlb_touch(c);

  const unsigned xo = rg * 32 * 128 + t * 128 + ((q ^ (t >> 1)) << 4);
  unsigned wb[3]; int lb3[3];
#pragma unroll
  for (int j = 0; j < 3; ++j) {
    int jr = j + (rg == 3 ? 0 : rg); jr = jr >= 3 ? jr - 3 : jr;
    lb3[j] = 3 * cg + jr;
    wb[j] = (8 * q + (t >> 2)) * 192 + ((lb3[j] ^ (q & 1)) << 5) + (t & 3) * 8;
  }
  const int gj = c.tid < 128 ? c.tid : 127;
  const unsigned gofs = (i * 128 + gj) * 16;
  const f32x4 z4 = {0.f, 0.f, 0.f, 0.f};
  u32x4 xf[2][16];
  f32x4 acc[2][3], cs = z4;
#pragma unroll
  for (int j = 0; j < 3; ++j) { acc[0][j] = z4; acc[1][j] = z4; }
  u32x4 g = {0, 0, 0, 0};
  ZW F0, F1;
  wait_vm<16 + (PF & 1)>();
  bar();
  z_read_x<0, 0>(c, xf, xo); z_read_w<0, 0>(c, F0, wb);
  z_stage_a<0>(c, a, xf, F0, F1, acc, cs, xo, wb, g, gofs); z_stage_a<1>(c, a, xf, F0, F1, acc, cs, xo, wb, g, gofs);
  z_stage_a<2>(c, a, xf, F0, F1, acc, cs, xo, wb, g, gofs); z_stage_a<3>(c, a, xf, F0, F1, acc, cs, xo, wb, g, gofs);
  z_stage_a<4>(c, a, xf, F0, F1, acc, cs, xo, wb, g, gofs); z_stage_a<5>(c, a, xf, F0, F1, acc, cs, xo, wb, g, gofs);
  z_stage_a<6>(c, a, xf, F0, F1, acc, cs, xo, wb, g, gofs); z_stage_a<7>(c, a, xf, F0, F1, acc, cs, xo, wb, g, gofs);
  asm volatile("" : "+v"(acc[0][0]), "+v"(acc[0][1]), "+v"(acc[0][2]), "+v"(acc[1][0]), "+v"(acc[1][1]), "+v"(acc[1][2]), "+v"(cs));
  STAMP(2);
  g = settle_pair(g, c.rs, WS_XS + gofs, true, c.Tlo);
  if (c.tid < 128) {
    *(LAS unsigned*)(c.L + T_XM + c.tid * 4) = g.x;
    *(LAS unsigned*)(c.L + T_XR + c.tid * 4) = g.z;
  }
  if (rg < 3 && lane < 16) *(LAS float*)(c.L + T_CS + (lb3[0] * 16 + lane) * 4) = cs[0];
  bar();
  u32x2 Z2[2][3];
  {
    float ncs[3], on[3];
#pragma unroll
    for (int j = 0; j < 3; ++j) {
      ncs[j] = -*(LAS float*)(c.L + T_CS + (lb3[j] * 16 + t) * 4);
      on[j] = lb3[j] < nb ? 1.0f : 0.0f;
    }
    float st[2][8];
#pragma unroll
    for (int x = 0; x < 2; ++x) {
      const f32x4 m4 = *(LAS f32x4*)(c.L + T_XM + (rg * 32 + x * 16 + 4 * q) * 4);
      const f32x4 r4 = *(LAS f32x4*)(c.L + T_XR + (rg * 32 + x * 16 + 4 * q) * 4);
      f32x2 s1a = {0.f, 0.f}, s1b = s1a, s2a = s1a, s2b = s1a;
#pragma unroll
      for (int j = 0; j < 3; ++j) {
        const f32x4 v = acc[x][j];
        const f32x2 ya = gelu2(lo2(r4) * fma2(lo2(m4), splat(ncs[j]), lo2(v)));
        const f32x2 yb = gelu2(hi2(r4) * fma2(hi2(m4), splat(ncs[j]), hi2(v)));
        Z2[x][j].x = pk2(ya); Z2[x][j].y = pk2(yb);
        const f32x2 ma = ya * on[j], mb = yb * on[j];
        s1a += ma; s1b += mb;
        s2a = fma2(ma, ya, s2a); s2b = fma2(mb, yb, s2b);
      }
      st[x][0] = s1a.x; st[x][1] = s1a.y; st[x][2] = s1b.x; st[x][3] = s1b.y;
      st[x][4] = s2a.x; st[x][5] = s2a.y; st[x][6] = s2b.x; st[x][7] = s2b.y;
    }
    float rs[2][2];
    red16(st, rs);
    if ((t & 3) == 0) {
#pragma unroll
      for (int x = 0; x < 2; ++x) {
        f32x2 o = {rs[x][0], rs[x][1]};
        *(LAS f32x2*)(c.L + T_ZP + (cg * 128 + rg * 32 + x * 16 + 4 * q + (t >> 2)) * 8) = o;
      }
    }
  }
  bar();
  if (c.tid < 256) {
    int tok = c.tid >> 1, st = c.tid & 1;
    float v = *(LAS float*)(c.L + T_ZP + tok * 8 + st * 4) + *(LAS float*)(c.L + T_ZP + (128 + tok) * 8 + st * 4);
    gstore64(ws + WS_ZS + ((i * 8 + zc) * 128 + tok) * 16 + st * 8, ((u64)c.Tlo << 32) | __float_as_uint(v));
  }
  STAMP(3);
  cs = z4;
#pragma unroll
  for (int j = 0; j < 3; ++j) { acc[0][j] = z4; acc[1][j] = z4; }
  u32x4 gs[2] = {{0, 0, 0, 0}, {0, 0, 0, 0}};
  const unsigned sofs = ((2 * q) * 128 + 16 * w + t) * 16;
  const char* sbase = ws + WS_ZS + (size_t)i * 8 * 128 * 16;
  z_read_w<8, 0>(c, F0, wb);
  z_stage_b<8>(c, a, xf, F0, F1, acc, cs, wb, gs, sofs, sbase);  z_stage_b<9>(c, a, xf, F0, F1, acc, cs, wb, gs, sofs, sbase);
  z_stage_b<10>(c, a, xf, F0, F1, acc, cs, wb, gs, sofs, sbase); z_stage_b<11>(c, a, xf, F0, F1, acc, cs, wb, gs, sofs, sbase);
  z_stage_b<12>(c, a, xf, F0, F1, acc, cs, wb, gs, sofs, sbase); z_stage_b<13>(c, a, xf, F0, F1, acc, cs, wb, gs, sofs, sbase);
  z_stage_b<14>(c, a, xf, F0, F1, acc, cs, wb, gs, sofs, sbase); z_stage_b<15>(c, a, xf, F0, F1, acc, cs, wb, gs, sofs, sbase);
  asm volatile("" : "+v"(acc[0][0]), "+v"(acc[0][1]), "+v"(acc[0][2]), "+v"(acc[1][0]), "+v"(acc[1][1]), "+v"(acc[1][2]), "+v"(cs));
  STAMP(4);
  {
    const unsigned so = WS_ZS + i * 8 * 128 * 16 + sofs;
    gs[0] = settle_pair(gs[0], c.rs, so, true, c.Tlo);
    gs[1] = settle_pair(gs[1], c.rs, so + 2048, true, c.Tlo);
    const float sm = sumq(__uint_as_float(gs[0].x) + __uint_as_float(gs[1].x));
    const float ss = sumq(__uint_as_float(gs[0].z) + __uint_as_float(gs[1].z));
    const float m = sm * (1.0f / 688.0f);
    const float r = __builtin_amdgcn_rsqf(fmaxf(ss * (1.0f / 688.0f) - m * m, 0.f) + 1e-5f);
    if (lane < 16) {
      *(LAS float*)(c.L + T_ZR + (16 * w + lane) * 4) = r;
      *(LAS float*)(c.L + T_ZB + (16 * w + lane) * 4) = -m * r;
    }
  }
  if (rg < 3 && t == 0) *(LAS f32x4*)(c.L + T_CS + 512 + (lb3[0] * 16 + 4 * q) * 4) = cs;
  bar();
  STAMP(18);
  {
    const int qp = ((q & 1) << 1) | (q >> 1);
    f32x4 r4[2], b4[2];
#pragma unroll
    for (int x = 0; x < 2; ++x) {
      r4[x] = *(LAS f32x4*)(c.L + T_ZR + (rg * 32 + x * 16 + 4 * q) * 4);
      b4[x] = *(LAS f32x4*)(c.L + T_ZB + (rg * 32 + x * 16 + 4 * q) * 4);
    }
#pragma unroll
    for (int j = 0; j < 3; ++j) {
      u32x2 n[2];
#pragma unroll
      for (int x = 0; x < 2; ++x) {
        n[x].x = pk2(fma2(unpk(Z2[x][j].x), lo2(r4[x]), lo2(b4[x])));
        n[x].y = pk2(fma2(unpk(Z2[x][j].y), hi2(r4[x]), hi2(b4[x])));
      }
      u32x4 fr = pairswap(n[0], n[1]);
      if (lb3[j] < nb) {
        unsigned off = WS_Z2N + ((i * 4 + rg) * 44 + fb0 + lb3[j]) * 1024 + qp * 256 + t * 16;
        __builtin_amdgcn_raw_buffer_store_b128(fr, c.rs, off, 0, 16);
      }
    }
  }
  STAMP(19);
  g3_prefetch_w(c, g3);
  wait_vm<7>();
  bar();
  if (c.tid == 0) gstore64(ws + WS_FZ2 + (zc * 16 + i) * 128, tagT(c));
  STAMP(5);
#pragma unroll
  for (int j = 0; j < 3; ++j) { z1.acc[0][j] = acc[0][j]; z1.acc[1][j] = acc[1][j]; }
}

DEV void z1_epilogue(const Ctx& c, const Z1Keep& z1, int i, int zc) {
  const int lane = c.lane, rg = c.w & 3, cg = c.w >> 2;
  const int t = lane & 15, q = lane >> 4;
  const int fb0 = fb_start(zc), nb = zc < 3 ? 6 : 5;
  f32x2 nm[2], rs[2];
#pragma unroll
  for (int x = 0; x < 2; ++x) {
    nm[x] = splat(-*(LAS float*)(c.L + T_XM + (rg * 32 + x * 16 + t) * 4));
    rs[x] = splat(*(LAS float*)(c.L + T_XR + (rg * 32 + x * 16 + t) * 4));
  }
#pragma unroll
  for (int j = 0; j < 3; ++j) {
    int jr = j + (rg == 3 ? 0 : rg); jr = jr >= 3 ? jr - 3 : jr;
    const int lb = 3 * cg + jr;
    const f32x4 c4 = *(LAS f32x4*)(c.L + T_CS + 512 + (lb * 16 + 4 * q) * 4);
    u32x2 n[2];
#pragma unroll
    for (int x = 0; x < 2; ++x) {
      const f32x4 v = z1.acc[x][j];
      n[x].x = pk2(gelu2(rs[x] * fma2(nm[x], lo2(c4), lo2(v))));
      n[x].y = pk2(gelu2(rs[x] * fma2(nm[x], hi2(c4), hi2(v))));
    }
    u32x4 fr = pairswap(n[0], n[1]);
    if (lb < nb) {
      int fb = fb0 + lb;
      unsigned off = WS_Z1 + ((i * 8 + rg * 2 + (q & 1)) * 22 + (fb >> 1)) * 1024 + (2 * (fb & 1) + (q >> 1)) * 256 + t * 16;
      __builtin_amdgcn_raw_buffer_store_b128(fr, c.rs, off, 0, 16);
    }
  }
}

struct HAddr { unsigned va, vw; const char *bo, *bu, *bwo, *bwout; };
template <int T> DEV void h_issue_w(const Ctx& c, const HAddr& a) {
  const unsigned l = c.lb + ((32 + T) % 7) * G_SLOT + 8192 + c.w * 1024;
  if (T < 8) dma16(a.vw, a.bwo + (size_t)T * 64 * 1024, l);
  else if (T < 18) dma16(a.vw, a.bwout + (size_t)(T - 8) * 64 * 1024, l);
  else {
    int k = 8 * c.w + (c.lane >> 3);
    unsigned v = k > 47 ? a.vw - (k - 47) * 1024 : a.vw;
    dma16(v, a.bwout + (size_t)640 * 1024, l);
  }
}
template <int T> DEV void h_issue_a(const Ctx& c, const HAddr& a) {
  const unsigned l = c.lb + ((32 + T) % 7) * G_SLOT + c.w * 1024;
  if (T < 8) dma16c(a.va, a.bo + T * 2048, l);
  else dma16c(a.va, a.bu + (T - 8) * 2048, l);
}
struct HF { u32x4 b[2], f0[2], f1[2]; };
template <int T> DEV void h_read(const Ctx& c, HF& F, unsigned ao, unsigned w0, unsigned w1) {
  LAS char* L = c.L + ((32 + T) % 7) * G_SLOT;
#pragma unroll
  for (int kk = 0; kk < 2; ++kk) {
    F.b[kk] = lds128(L + ao + kk * 4096);
    F.f0[kk] = trfrag(L + 8192 + w0 + kk * 4096, 512);
    F.f1[kk] = trfrag(L + 8192 + w1 + kk * 4096, 512);
  }
  sb();
}
template <int T, int IA, int IW> DEV void h_mma(const Ctx& c, const HAddr& h, const HF& F, f32x4 (&acc)[2]) {
  if (IA >= 0) h_issue_a<(IA < 0 ? 0 : IA)>(c, h);
  sb();
  acc[0] = mfma32(F.f0[0], F.b[0], acc[0]);
  acc[1] = mfma32(F.f1[0], F.b[0], acc[1]);
  sb();
  if (IW >= 0) h_issue_w<(IW < 0 ? 0 : IW)>(c, h);
  sb();
  u32x4 b = F.b[1];
  if (T == 18) {
    if (c.lane >= 32) { u32x4 z = {0, 0, 0, 0}; b = z; }
  }
  acc[0] = mfma32(F.f0[1], b, acc[0]);
  acc[1] = mfma32(F.f1[1], b, acc[1]);
  sb();
}
template <int T, int IA, int IW, bool NEXT> DEV void h_it(const Ctx& c, const HAddr& h, HF& Fc, HF& Fn, f32x4 (&acc)[2],
                                                         unsigned ao, unsigned w0, unsigned w1) {
  bar();
  if (NEXT) h_read<T + 1>(c, Fn, ao, w0, w1);
  h_mma<T, IA, IW>(c, h, Fc, acc);
}

template <int V> struct IC { static constexpr int v = V; };
struct GF { u32x4 b0, b1, a0, a1, a2; };

DEV void job_g(Ctx& c, int rt, int gc, const G3A& g3, bool epoch_wg, bool isz, const Z1Keep& z1, int pi, int pc) {
  char* ws = c.ws;
  const int lane = c.lane, w = c.w;
  const int t = lane & 15, q = lane >> 4;
  const int rg = w & 1, cg = (w >> 1) & 1, kh = w >> 2;
  const int fb0 = fb_start(gc), nb = gc < 3 ? 6 : 5;
  const u64 T = tagT(c);
  if (w == 0) poll_flags(ws + WS_FZ2 + (gc * 16 + (lane & 15)) * 128, lane < 16, T);
  bar();
  STAMP(8);
  const char* zb = ws + WS_Z2N + (size_t)fb0 * 1024;
  const unsigned vz1 = ((w / 6) * 44 + (w % 6)) * 1024 + lane * 16;
  const int u2 = 8 + (w >> 1);
  const unsigned vz2 = (44 + (u2 - 6)) * 1024 + (w & 1) * 512 + (lane & 31) * 16;
  const unsigned vz8 = (44 + (8 + (w & 3) - 6)) * 1024 + lane * 16, lz8 = c.lb + G_Z + (8 + (w & 3)) * 1024;
  const unsigned lz1 = c.lb + G_Z + w * 1024, lz2 = c.lb + G_Z + u2 * 1024 + (w & 1) * 512;
#pragma unroll
  for (int s = 0; s < 7; ++s) {
    const unsigned ks = (g3.k0 + s) & 31;
    dma16c(vz1, zb + ks * (88 * 1024), lz1 + s * G_SLOT);
    dma16mc(vz2, zb + ks * (88 * 1024), lz2 + s * G_SLOT, 0xffffffffull);
  }
  if (isz) z1_epilogue(c, z1, pi, pc);
  STAMP(6);
  HAddr h;
  {
    h.va = ((w & 3) * 16 + (w >> 2)) * 1024 + lane * 16;
    int k = 8 * w + (lane >> 3);
    int f = ((k >> 1) & 1) | (((k >> 3) & 1) << 1);
    h.vw = k * 1024 + (((lane & 7) ^ (f << 1)) << 4);
    h.bo = ws + WS_O + (size_t)rt * 4 * 16 * 1024;
    h.bu = ws + WS_U + (size_t)rt * 4 * 22 * 1024;
    h.bwo = c.wo + gc * 128;
    h.bwout = c.wout + gc * 128;
  }
  const unsigned vau = ((w & 3) * 22 + (w >> 2)) * 1024 + lane * 16;
  const unsigned bo_ = rg * 32 * 128 + t * 128 + (((4 * kh + q) ^ (t >> 1)) << 4);
  const unsigned ao_ = G_Z + (kh * 6 + 3 * cg) * 1024 + lane * 16;
  const f32x4 z4 = {0.f, 0.f, 0.f, 0.f};
  f32x4 acc[2][3];
#pragma unroll
  for (int j = 0; j < 3; ++j) { acc[0][j] = z4; acc[1][j] = z4; }
  const int tbk = rt * 4 + rg * 2 + kh;
  const unsigned flofs = lane < 8 ? WS_FO + ((rt >> 1) * 8 + lane) * 128 : WS_FZ1 + ((rt >> 1) * 8 + gc) * 128;
  unsigned z1ofs[3];
#pragma unroll
  for (int j = 0; j < 3; ++j) {
    int fb = fb0 + 3 * cg + j; fb = fb > 42 ? 42 : fb;
    z1ofs[j] = (tbk * 22 + (fb >> 1)) * 1024 + (2 * (fb & 1) + (q >> 1)) * 256 + t * 16 + (q & 1) * 8;
  }
  unsigned slot = 0;
  unsigned ks = (g3.k0 + 7) & 31;
  auto rd = [&](GF& F, unsigned sl) {
    LAS char* L = c.L + sl;
    F.a0 = lds128(L + ao_); F.b0 = lds128(L + bo_); F.b1 = lds128(L + bo_ + 2048);
    F.a1 = lds128(L + ao_ + 1024); F.a2 = lds128(L + ao_ + 2048);
    sb();
  };
  auto it = [&](auto kind, auto ht, GF& Fc, GF& Fn, bool next) {
    constexpr int KIND = decltype(kind)::v, HT = decltype(ht)::v;
    if (GX != 6 || KIND != 0) bar();
    const unsigned nsl = slot == 6 * G_SLOT ? 0 : slot + G_SLOT;
    const char* pz = zb + ks * (88 * 1024);
    if (KIND == 0 && GX != 1 && GX != 4) dma16(g3.vw, g3.bw + ks * 128, c.lb + slot + w * 1024);
    if (KIND == 0 && GX == 7) { dma16c(vz1, pz, lz1 + slot); dma16mc(vz2, pz, lz2 + slot, 0xffffffffull); }
    if (KIND == 1) h_issue_a<HT>(c, h);
    sb();
    if (next && GX != 2) rd(Fn, nsl);
    if (GX != 2) {
    acc[0][0] = mfma32(Fc.a0, Fc.b0, acc[0][0]); acc[1][0] = mfma32(Fc.a0, Fc.b1, acc[1][0]);
    acc[0][1] = mfma32(Fc.a1, Fc.b0, acc[0][1]);
    }
    sb();
    if (KIND == 0 && GX != 1 && GX != 3 && GX != 7) { if (GX == 5) dma16(vz1, pz, lz1 + slot); else dma16c(vz1, pz, lz1 + slot); }
    if (KIND == 1) h_issue_w<HT>(c, h);
    sb();
    if (GX != 2) {
    acc[1][1] = mfma32(Fc.a1, Fc.b1, acc[1][1]);
    acc[0][2] = mfma32(Fc.a2, Fc.b0, acc[0][2]); acc[1][2] = mfma32(Fc.a2, Fc.b1, acc[1][2]);
    }
    sb();
    if (KIND == 0) {
      if (GX != 1 && GX != 3 && GX != 7) {
        if (GX == 5) dma16m(vz2, pz, lz2 + slot, 0xffffffffull);
        else if (GX == 8) { if (w < 4) dma16c(vz8, pz, lz8 + slot); }
        else dma16mc(vz2, pz, lz2 + slot, 0xffffffffull);
      }
      ks = (ks + 1) & 31;
    }
    sb();
    slot = nsl;
  };
  const IC<0> K0; const IC<1> K1; const IC<2> K2;
  constexpr int GW = GX == 3 ? 5 : GX == 4 ? 10 : 15;
  auto gwait = [&]() { if (GX == 8 && w >= 4) wait_vm<10>(); else wait_vm<GW>(); };
  GF F0, F1;
  wait_vm<12>();
  bar();
  STAMP(17);
  if (!isz && c.tid == 0) gstore64(ws + WS_FO + (pi * 8 + pc) * 128, T);
  rd(F0, 0);
  wait_vm<10>(); it(K0, K0, F0, F1, true);
  wait_vm<11>(); it(K0, K0, F1, F0, true);
  wait_vm<12>(); it(K0, K0, F0, F1, true);
  wait_vm<13>(); it(K0, K0, F1, F0, true);
  wait_vm<14>(); it(K0, K0, F0, F1, true);
  wait_vm<15>(); it(K0, K0, F1, F0, true);
  if (isz && c.tid == 0) gstore64(ws + WS_FZ1 + (pi * 8 + pc) * 128, T);
#pragma unroll 1
  for (int s = 6; s < 10; s += 2) { gwait(); it(K0, K0, F0, F1, true); gwait(); it(K0, K0, F1, F0, true); }
  STAMP(21);
  u32x2 fl = aload64c(flofs, ws);
#pragma unroll 1
  for (int s = 10; s < 16; s += 2) { gwait(); it(K0, K0, F0, F1, true); gwait(); it(K0, K0, F1, F0, true); }
  asm volatile("; TIE %0" : "+v"(fl));
  if (!__all(fl.x == c.Tlo && fl.y == c.Thi)) poll_flags(ws + flofs, true, T);
  u32x2 z1v[3];
  z1v[0] = aload64c(z1ofs[0], ws + WS_Z1);
  z1v[1] = aload64c(z1ofs[1], ws + WS_Z1);
  z1v[2] = aload64c(z1ofs[2], ws + WS_Z1);
  unsigned braw;
  asm volatile("global_load_ushort %0, %1, %2" : "=v"(braw) : "v"((tbk * 16 + t) * 2), "s"(uni(c.bsp)) : "memory");
#pragma unroll 1
  for (int s = 16; s < 24; s += 2) { gwait(); it(K0, K0, F0, F1, true); gwait(); it(K0, K0, F1, F0, true); }
  wait_vm<15>(); it(K0, K0, F0, F1, true);
  wait_vm<15>(); it(K1, IC<0>(), F1, F0, true);
  wait_vm<14>(); it(K1, IC<1>(), F0, F1, true);
  wait_vm<13>(); it(K1, IC<2>(), F1, F0, true);
  wait_vm<12>(); it(K1, IC<3>(), F0, F1, true);
  wait_vm<11>(); it(K1, IC<4>(), F1, F0, true);
  wait_vm<10>(); it(K1, IC<5>(), F0, F1, true);
  it(K2, K0, F1, F0, false);
  asm volatile("; TIE %0 %1 %2 %3" : "+v"(z1v[0]), "+v"(z1v[1]), "+v"(z1v[2]), "+v"(braw));
  const float bias = blo(braw);
  STAMP(9);
  {
    const unsigned e01 = 3 * G_SLOT + w * 2048 + lane * 16, e2 = T_EX2 + w * 1024 + lane * 16;
    const unsigned p01 = 3 * G_SLOT + (w ^ 4) * 2048 + lane * 16, p2 = T_EX2 + (w ^ 4) * 1024 + lane * 16;
    f32x4 keep[3], send[3];
#pragma unroll
    for (int j = 0; j < 3; ++j) { keep[j] = kh ? acc[1][j] : acc[0][j]; send[j] = kh ? acc[0][j] : acc[1][j]; }
    *(LAS f32x4*)(c.L + e01) = send[0]; *(LAS f32x4*)(c.L + e01 + 1024) = send[1]; *(LAS f32x4*)(c.L + e2) = send[2];
    bar();
    STAMP(22);
    keep[0] += *(LAS f32x4*)(c.L + p01); keep[1] += *(LAS f32x4*)(c.L + p01 + 1024); keep[2] += *(LAS f32x4*)(c.L + p2);
#pragma unroll
    for (int j = 0; j < 3; ++j) {
      unsigned g0 = pk2(keep[j][0] + bias, keep[j][1] + bias), g1 = pk2(keep[j][2] + bias, keep[j][3] + bias);
      unsigned a0 = z1v[j].x, a1 = z1v[j].y;
      u32x2 u;
      u.x = pk2(blo(a0) * blo(g0), bhi(a0) * bhi(g0));
      u.y = pk2(blo(a1) * blo(g1), bhi(a1) * bhi(g1));
      if (3 * cg + j < nb) __builtin_amdgcn_raw_buffer_store_b64(u, c.rs, WS_U + z1ofs[j], 0, 16);
    }
  }
  STAMP(10);

  const int tb = w & 3, np = w >> 2;
  const unsigned ha = tb * 1024 + lane * 16;
  const int fw = ((t >> 3) & 1) | ((q & 1) << 1);
  const unsigned wl = (8 * q + (t >> 2)) * 128 + (t & 3) * 8;
  const unsigned hw0 = wl + (((2 * np) ^ fw) << 5), hw1 = wl + (((2 * np + 1) ^ fw) << 5);
  f32x4 sa[2] = {z4, z4};
  HF H0, H1;
  h_issue_a<6>(c, h); h_issue_w<6>(c, h);
  h_read<0>(c, H0, ha, hw0, hw1);
  h_it<0, 7, 7, true>(c, h, H0, H1, sa, ha, hw0, hw1);
  h_it<1, -1, 8, true>(c, h, H1, H0, sa, ha, hw0, hw1);
  h_it<2, -1, 9, true>(c, h, H0, H1, sa, ha, hw0, hw1);
  h_it<3, -1, 10, true>(c, h, H1, H0, sa, ha, hw0, hw1);
  wait_vm<7>();
  h_it<4, -1, 11, true>(c, h, H0, H1, sa, ha, hw0, hw1);
  if (c.tid == 0) gstore64(ws + WS_FU + (rt * 8 + gc) * 128, T);
  wait_vm<6>(); h_it<5, -1, 12, true>(c, h, H1, H0, sa, ha, hw0, hw1);
  wait_vm<5>(); h_it<6, -1, 13, true>(c, h, H0, H1, sa, ha, hw0, hw1);
  h_it<7, -1, 14, false>(c, h, H1, H0, sa, ha, hw0, hw1);
  asm volatile("" : "+v"(sa[0]), "+v"(sa[1]));
  STAMP(11);
  h.va = vau;
  if (w == 0) poll_flags(ws + WS_FU + (rt * 8 + (lane & 7)) * 128, lane < 8, T);
  if (epoch_wg && w == 1) {
#pragma unroll
    for (int j = 0; j < 2; ++j) {
      poll_flags(ws + WS_FZ2 + (j * 64 + lane) * 128, true, T);
      poll_flags(ws + WS_FO + (j * 64 + lane) * 128, true, T);
    }
    if (lane == 0) gstore64(ws + WS_EPOCH, T);
  }
  bar();
  STAMP(12);
  h_issue_a<8>(c, h); h_issue_a<9>(c, h); h_issue_a<10>(c, h); h_issue_a<11>(c, h); h_issue_a<12>(c, h); h_issue_a<13>(c, h);
  h_issue_a<14>(c, h);
  const int orow = rt * 64 + tb * 16 + t;
  const unsigned oofs = orow * 1024 + gc * 128 + np * 64 + (q & 1) * 32 + (q >> 1) * 16;
  f32x4 ga[2] = {z4, z4};
  wait_vm<6>();
  bar();
  STAMP(23);
  h_read<8>(c, H0, ha, hw0, hw1);
  wait_vm<5>(); h_it<8, 15, 15, true>(c, h, H0, H1, ga, ha, hw0, hw1);
  wait_vm<6>(); h_it<9, 16, 16, true>(c, h, H1, H0, ga, ha, hw0, hw1);
  wait_vm<7>(); h_it<10, 17, 17, true>(c, h, H0, H1, ga, ha, hw0, hw1);
  wait_vm<8>(); h_it<11, 18, 18, true>(c, h, H1, H0, ga, ha, hw0, hw1);
  wait_vm<9>(); h_it<12, -1, -1, true>(c, h, H0, H1, ga, ha, hw0, hw1);
  u32x4 xres;
  asm volatile("global_load_dwordx4 %0, %1, %2" : "=v"(xres) : "v"(oofs), "s"(uni(c.x)) : "memory");
  wait_vm<9>(); h_it<13, -1, -1, true>(c, h, H1, H0, ga, ha, hw0, hw1);
  wait_vm<7>(); h_it<14, -1, -1, true>(c, h, H0, H1, ga, ha, hw0, hw1);
  wait_vm<5>(); h_it<15, -1, -1, true>(c, h, H1, H0, ga, ha, hw0, hw1);
  wait_vm<3>(); h_it<16, -1, -1, true>(c, h, H0, H1, ga, ha, hw0, hw1);
  wait_vm<1>(); h_it<17, -1, -1, true>(c, h, H1, H0, ga, ha, hw0, hw1);
  h_mma<18, -1, -1>(c, h, H0, ga);
  asm volatile("s_waitcnt vmcnt(0) ; TIE %0" : "+v"(xres) : : "memory");
  STAMP(13);
  {
    u32x2 S[2], G[2];
#pragma unroll
    for (int b = 0; b < 2; ++b) {
      S[b].x = pk2(sa[b][0], sa[b][1]); S[b].y = pk2(sa[b][2], sa[b][3]);
      G[b].x = pk2(ga[b][0], ga[b][1]); G[b].y = pk2(ga[b][2], ga[b][3]);
    }
    u32x4 s8 = pairswap(S[0], S[1]), g8 = pairswap(G[0], G[1]);
    u32x4 o;
#pragma unroll
    for (int j = 0; j < 4; ++j) {
      unsigned su = s8[j], gu = g8[j], xu = xres[j];
      float g0 = blo(gu), g1 = bhi(gu);
      float e0 = g0 * __builtin_amdgcn_rcpf(1.0f + __builtin_amdgcn_exp2f(g0 * -1.4426950408889634f));
      float e1 = g1 * __builtin_amdgcn_rcpf(1.0f + __builtin_amdgcn_exp2f(g1 * -1.4426950408889634f));
      o[j] = pk2(__builtin_fmaf(blo(su), e0, blo(xu)), __builtin_fmaf(bhi(su), e1, bhi(xu)));
    }
    if (OUTSC == 0) *(GAS u32x4*)(size_t)(c.out + oofs) = o;
    else {
      __amdgpu_buffer_rsrc_t ro = __builtin_amdgcn_make_buffer_rsrc((void*)c.out, 0, 2048 * 1024, 0x00020000);
      __builtin_amdgcn_raw_buffer_store_b128(o, ro, oofs, 0, OUTSC == 1 ? 16 : OUTSC == 2 ? 2 : 17);
    }
  }
  STAMP(14); STAMPC(31);
}

__global__ __launch_bounds__(512, 2) void fused_kernel(const char* x, const char* wqkv, const char* wo, const char* win, const char* wsp,
                                                       const char* bsp, const char* wout, char* out, char* ws) {
  extern __shared__ __attribute__((aligned(16))) char smem[];
  Ctx c;
  c.x = x; c.wqkv = wqkv; c.wo = wo; c.win = win; c.wsp = wsp; c.bsp = bsp; c.wout = wout; c.out = out; c.ws = ws;
  c.L = (LAS char*)smem;
  c.lb = (unsigned)(size_t)c.L;
  c.tid = threadIdx.x; c.lane = c.tid & 63; c.w = __builtin_amdgcn_readfirstlane(c.tid >> 6);
  c.rs = __builtin_amdgcn_make_buffer_rsrc((void*)ws, 0, WS_USED, 0x00020000);
  c.Tlo = 0; c.Thi = 0;
  c.bid = blockIdx.x;
  STAMP(0); STAMPC(30);
  const int bid = blockIdx.x, xcd = bid & 7, sl = bid >> 3;
  const int i = (xcd & 1) * 8 + (sl >> 2), rsel = sl & 3, col = 2 * (xcd >> 1) + (rsel & 1);
  const int r16 = (xcd >> 1) * 4 + rsel;
  const int rt = (xcd >> 1) * 8 + (sl >> 2), gc = (xcd & 1) * 4 + (sl & 3);
  const int s0row = i * 128 + r16 * 8 + c.w;
  const G3A g3 = g3_addr(c, rt, sl);
  Z1Keep z1;
  if (rsel < 2) {
    job_a(c, i, col, s0row, g3, rt, rsel, xcd & 1);
#pragma unroll
    for (int j = 0; j < 3; ++j) asm volatile("" : "=v"(z1.acc[0][j]), "=v"(z1.acc[1][j]));
  } else {
    job_z(c, i, col, s0row, g3, z1);
  }
  STAMP(7);
  job_g(c, rt, gc, g3, bid == 0, rsel >= 2, z1, i, col);
}

extern "C" void kernel_launch(void* const* d_in, const int* in_sizes, int n_in, void* d_out, int out_size, void* d_ws, size_t ws_size,
                              hipStream_t stream) {
  (void)in_sizes; (void)n_in; (void)out_size; (void)ws_size;
  (void)hipFuncSetAttribute((const void*)fused_kernel, hipFuncAttributeMaxDynamicSharedMemorySize, LDS_BYTES);
  fused_kernel<<<256, 512, LDS_BYTES, stream>>>((const char*)d_in[0], (const char*)d_in[1], (const char*)d_in[2], (const char*)d_in[4],
                                                (const char*)d_in[5], (const char*)d_in[6], (const char*)d_in[7], (char*)d_out, (char*)d_ws);
}
```

```cpp
#include <hip/hip_runtime.h>
#define EXP 0
#define PF 2
#define GA 1
#define PB 7
#define GX 0
#define ZG 11
#define FE 1
#define SPOS 5
#define NREP 4
#define ROT 0
#define OUTSC 0

typedef unsigned short u16;
typedef unsigned long long u64;
typedef __bf16 bf16x8 __attribute__((ext_vector_type(8)));
typedef __bf16 bf16x2 __attribute__((ext_vector_type(2)));
typedef short s16x4 __attribute__((ext_vector_type(4)));
typedef float f32x4 __attribute__((ext_vector_type(4)));
typedef float f32x2 __attribute__((ext_vector_type(2)));
typedef unsigned u32x4 __attribute__((ext_vector_type(4)));
typedef unsigned u32x2 __attribute__((ext_vector_type(2)));
#define LAS __attribute__((address_space(3)))
#define GAS __attribute__((address_space(1)))
#define DEV __device__ __forceinline__

constexpr unsigned WS_EPOCH = 0;
constexpr unsigned WS_FZ1 = 4096;
constexpr unsigned WS_FO  = WS_FZ1 + 128 * 128;
constexpr unsigned WS_FU  = WS_FO + 128 * 128;
constexpr unsigned WS_XS  = 0x20000;
constexpr unsigned WS_ZS  = 0x30000;
constexpr unsigned WS_FZ2 = 0x80000;
constexpr unsigned WS_Z2N = 0x100000;
constexpr unsigned WS_Z1  = 0x400000;
constexpr unsigned WS_U   = 0x700000;
constexpr unsigned WS_O   = 0xA00000;
constexpr unsigned WS_DBG = 0xD00000;
constexpr unsigned WS_USED = 0xE00000;

constexpr unsigned LDS_BYTES = 160 * 1024;
constexpr unsigned A_SLOT = 43008, A_W = 18432;
constexpr unsigned Z_SLOT = 28672, Z_W = 16384;
constexpr unsigned G_SLOT = 24576;
constexpr unsigned W_STG = 65536;
constexpr unsigned G_EX = 0;
constexpr unsigned T_KX = 139264, T_VX = T_KX + 8192;
constexpr unsigned T_XM = T_KX + 16384;
constexpr unsigned T_XR = T_XM + 640;
constexpr unsigned T_CS = T_XM + 1280;
constexpr unsigned T_ZP = T_CS + 1024;
constexpr unsigned T_ZR = T_ZP + 2048;
constexpr unsigned T_ZB = T_ZR + 512;
constexpr unsigned T_DUMMY = T_ZB + 512;
static_assert(T_DUMMY + 256 <= LDS_BYTES && W_STG + 4 * G_SLOT <= LDS_BYTES && 5 * Z_SLOT <= T_XM, "lds");
constexpr unsigned OOB = 0x10000000;

constexpr unsigned SPIN_CAP = 200000;

#define STAMP1(k) do {} while (0)
#define STAMPC(k) do {} while (0)
#define STAMP(k) do {} while (0)

DEV const char* uni(const char* p) {
  u64 v = (u64)(size_t)p;
  unsigned lo = __builtin_amdgcn_readfirstlane((unsigned)v), hi = __builtin_amdgcn_readfirstlane((unsigned)(v >> 32));
  return (const char*)(size_t)(((u64)hi << 32) | lo);
}
DEV unsigned uni(unsigned v) { return __builtin_amdgcn_readfirstlane(v); }
struct Ctx0 { unsigned sink; };
DEV void dma16(unsigned voff, const char* sbase, unsigned lds) {
  asm volatile("s_mov_b32 m0, %2\n\ts_nop 0\n\tglobal_load_lds_dwordx4 %0, %1" ::"v"(voff), "s"(uni(sbase)), "s"(uni(lds)) : "memory");
}
DEV void dma16c(unsigned voff, const char* sbase, unsigned lds) {
  asm volatile("s_mov_b32 m0, %2\n\ts_nop 0\n\tglobal_load_lds_dwordx4 %0, %1 sc1" ::"v"(voff), "s"(uni(sbase)), "s"(uni(lds)) : "memory");
}
DEV void dma16m(unsigned voff, const char* sbase, unsigned lds, u64 mask) {
  asm volatile("s_mov_b32 m0, %2\n\ts_mov_b64 exec, %3\n\tglobal_load_lds_dwordx4 %0, %1\n\ts_mov_b64 exec, -1"
               ::"v"(voff), "s"(uni(sbase)), "s"(uni(lds)), "s"(mask) : "memory");
}
DEV void dma16mc(unsigned voff, const char* sbase, unsigned lds, u64 mask) {
  asm volatile("s_mov_b32 m0, %2\n\ts_mov_b64 exec, %3\n\tglobal_load_lds_dwordx4 %0, %1 sc1\n\ts_mov_b64 exec, -1"
               ::"v"(voff), "s"(uni(sbase)), "s"(uni(lds)), "s"(mask) : "memory");
}
DEV void touch(const Ctx0& c, const char* p) {
  asm volatile("s_mov_b32 m0, %1\n\ts_nop 0\n\tglobal_load_lds_dword %0, off" ::"v"(p), "s"(uni(c.sink)) : "memory");
}
template <int N> DEV void wait_vm() { asm volatile("s_waitcnt vmcnt(%0)" ::"n"(N) : "memory"); }
DEV void sb() { __builtin_amdgcn_sched_barrier(0); }
DEV void bar0() { asm volatile("s_barrier" ::: "memory"); }
DEV void bar() { asm volatile("s_waitcnt lgkmcnt(0)\n\ts_barrier" ::: "memory"); }

template <int V = 0> DEV f32x4 mfmaX(u32x4 a, u32x4 b, f32x4 c) {
  return __builtin_amdgcn_mfma_f32_16x16x32_bf16(__builtin_bit_cast(bf16x8, a), __builtin_bit_cast(bf16x8, b), c, 0, 0, 0);
}
DEV f32x4 mfma32(u32x4 a, u32x4 b, f32x4 c) {
  return __builtin_amdgcn_mfma_f32_16x16x32_bf16(__builtin_bit_cast(bf16x8, a), __builtin_bit_cast(bf16x8, b), c, 0, 0, 0);
}
DEV f32x4 mfma16(u32x2 a, u32x2 b, f32x4 c) {
  return __builtin_amdgcn_mfma_f32_16x16x16bf16_1k(__builtin_bit_cast(s16x4, a), __builtin_bit_cast(s16x4, b), c, 0, 0, 0);
}
DEV u32x4 lds128(LAS char* p) { return *(LAS u32x4*)p; }
DEV u32x2 lds64(LAS char* p) { return *(LAS u32x2*)p; }
DEV u32x4 trfrag(LAS char* p, int off4) {
  s16x4 a = __builtin_amdgcn_ds_read_tr16_b64_v4i16((LAS s16x4*)p);
  s16x4 b = __builtin_amdgcn_ds_read_tr16_b64_v4i16((LAS s16x4*)(p + off4));
  u32x2 ua = __builtin_bit_cast(u32x2, a), ub = __builtin_bit_cast(u32x2, b);
  u32x4 r = {ua.x, ua.y, ub.x, ub.y};
  return r;
}
DEV unsigned pk2(float a, float b) {
  f32x2 v = {a, b};
  bf16x2 h = __builtin_convertvector(v, bf16x2);
  return __builtin_bit_cast(unsigned, h);
}
DEV float blo(unsigned u) { return __uint_as_float(u << 16); }
DEV float bhi(unsigned u) { return __uint_as_float(u & 0xffff0000u); }
template <int CTRL> DEV float dpp(float v) {
  return __uint_as_float(__builtin_amdgcn_update_dpp(0u, __float_as_uint(v), CTRL, 0xf, 0xf, true));
}
DEV float sum16(float v) {
  v += dpp<0x128>(v); v += dpp<0x124>(v); v += dpp<0x122>(v); v += dpp<0x121>(v);
  return v;
}
DEV float sumq(float v) {
  unsigned u = __float_as_uint(v);
  auto a = __builtin_amdgcn_permlane16_swap(u, u, false, false);
  v = __uint_as_float(a[0]) + __uint_as_float(a[1]);
  u = __float_as_uint(v);
  auto b = __builtin_amdgcn_permlane32_swap(u, u, false, false);
  return __uint_as_float(b[0]) + __uint_as_float(b[1]);
}
DEV float maxq(float v) {
  unsigned u = __float_as_uint(v);
  auto a = __builtin_amdgcn_permlane16_swap(u, u, false, false);
  v = fmaxf(__uint_as_float(a[0]), __uint_as_float(a[1]));
  u = __float_as_uint(v);
  auto b = __builtin_amdgcn_permlane32_swap(u, u, false, false);
  return fmaxf(__uint_as_float(b[0]), __uint_as_float(b[1]));
}
template <bool MX> DEV float op2(float a, float b) { return MX ? fmaxf(a, b) : a + b; }
template <bool MX> DEV void rows4(float (&v)[4]) {
  auto f = [](float x) { return __float_as_uint(x); };
  auto g = [](unsigned x) { return __uint_as_float(x); };
  auto a = __builtin_amdgcn_permlane16_swap(f(v[0]), f(v[1]), false, false);
  auto b = __builtin_amdgcn_permlane16_swap(f(v[2]), f(v[3]), false, false);
  float m1 = op2<MX>(g(a[0]), g(a[1])), m2 = op2<MX>(g(b[0]), g(b[1]));
  auto d = __builtin_amdgcn_permlane32_swap(f(m1), f(m2), false, false);
  unsigned r = f(op2<MX>(g(d[0]), g(d[1])));
  auto e = __builtin_amdgcn_permlane16_swap(r, r, false, false);
  auto ac = __builtin_amdgcn_permlane32_swap(e[0], e[0], false, false);
  auto bd = __builtin_amdgcn_permlane32_swap(e[1], e[1], false, false);
  v[0] = g(ac[0]); v[2] = g(ac[1]); v[1] = g(bd[0]); v[3] = g(bd[1]);
}
DEV u32x4 pairswap(u32x2 b0, u32x2 b1) {
  auto s0 = __builtin_amdgcn_permlane16_swap(b0.x, b1.x, false, false);
  auto s1 = __builtin_amdgcn_permlane16_swap(b0.y, b1.y, false, false);
  u32x4 r = {s0[0], s1[0], s0[1], s1[1]};
  return r;
}
DEV f32x2 fma2(f32x2 a, f32x2 b, f32x2 c) { return __builtin_elementwise_fma(a, b, c); }
DEV f32x2 splat(float v) { f32x2 r = {v, v}; return r; }
DEV f32x2 lo2(f32x4 v) { return __builtin_shufflevector(v, v, 0, 1); }
DEV f32x2 hi2(f32x4 v) { return __builtin_shufflevector(v, v, 2, 3); }
DEV unsigned pk2(f32x2 v) { return __builtin_bit_cast(unsigned, __builtin_convertvector(v, bf16x2)); }
DEV f32x2 unpk(unsigned u) { f32x2 r = {blo(u), bhi(u)}; return r; }
DEV f32x2 gelu2(f32x2 v) {
  f32x2 a = v * fma2(v * v, splat(-0.10294324f), splat(-2.3022082f));
  f32x2 e = {__builtin_amdgcn_exp2f(a.x), __builtin_amdgcn_exp2f(a.y)};
  e = e + 1.0f;
  f32x2 r = {__builtin_amdgcn_rcpf(e.x), __builtin_amdgcn_rcpf(e.y)};
  return v * r;
}
DEV void red16(const float (&a)[2][8], float (&r)[2][2]) {
  float m0, m1, m2, m3, m4, m5, m6, m7;
  asm volatile(
      "s_nop 1\n\t"
      "v_add_f32_dpp %0, %4, %4 row_ror:8 row_mask:0xf bank_mask:0x3\n\t"
      "v_add_f32_dpp %1, %5, %5 row_ror:8 row_mask:0xf bank_mask:0x3\n\t"
      "v_add_f32_dpp %2, %8, %8 row_ror:8 row_mask:0xf bank_mask:0x3\n\t"
      "v_add_f32_dpp %3, %9, %9 row_ror:8 row_mask:0xf bank_mask:0x3\n\t"
      "v_add_f32_dpp %0, %6, %6 row_ror:8 row_mask:0xf bank_mask:0xc\n\t"
      "v_add_f32_dpp %1, %7, %7 row_ror:8 row_mask:0xf bank_mask:0xc\n\t"
      "v_add_f32_dpp %2, %10, %10 row_ror:8 row_mask:0xf bank_mask:0xc\n\t"
      "v_add_f32_dpp %3, %11, %11 row_ror:8 row_mask:0xf bank_mask:0xc"
      : "=&v"(m0), "=&v"(m1), "=&v"(m2), "=&v"(m3)
      : "v"(a[0][0]), "v"(a[0][1]), "v"(a[0][2]), "v"(a[0][3]), "v"(a[0][4]), "v"(a[0][5]), "v"(a[0][6]), "v"(a[0][7]));
  asm volatile(
      "s_nop 1\n\t"
      "v_add_f32_dpp %4, %8, %8 row_ror:8 row_mask:0xf bank_mask:0x3\n\t"
      "v_add_f32_dpp %5, %9, %9 row_ror:8 row_mask:0xf bank_mask:0x3\n\t"
      "v_add_f32_dpp %6, %12, %12 row_ror:8 row_mask:0xf bank_mask:0x3\n\t"
      "v_add_f32_dpp %7, %13, %13 row_ror:8 row_mask:0xf bank_mask:0x3\n\t"
      "v_add_f32_dpp %4, %10, %10 row_ror:8 row_mask:0xf bank_mask:0xc\n\t"
      "v_add_f32_dpp %5, %11, %11 row_ror:8 row_mask:0xf bank_mask:0xc\n\t"
      "v_add_f32_dpp %6, %14, %14 row_ror:8 row_mask:0xf bank_mask:0xc\n\t"
      "v_add_f32_dpp %7, %15, %15 row_ror:8 row_mask:0xf bank_mask:0xc\n\t"
      "v_add_f32_dpp %0, %0, %0 row_ror:12 row_mask:0xf bank_mask:0x5\n\t"
      "v_add_f32_dpp %2, %2, %2 row_ror:12 row_mask:0xf bank_mask:0x5\n\t"
      "v_add_f32_dpp %4, %4, %4 row_ror:12 row_mask:0xf bank_mask:0x5\n\t"
      "v_add_f32_dpp %6, %6, %6 row_ror:12 row_mask:0xf bank_mask:0x5\n\t"
      "v_add_f32_dpp %0, %1, %1 row_ror:4 row_mask:0xf bank_mask:0xa\n\t"
      "v_add_f32_dpp %2, %3, %3 row_ror:4 row_mask:0xf bank_mask:0xa\n\t"
      "v_add_f32_dpp %4, %5, %5 row_ror:4 row_mask:0xf bank_mask:0xa\n\t"
      "v_add_f32_dpp %6, %7, %7 row_ror:4 row_mask:0xf bank_mask:0xa\n\t"
      "v_add_f32_dpp %0, %0, %0 quad_perm:[1,0,3,2] row_mask:0xf bank_mask:0xf\n\t"
      "v_add_f32_dpp %2, %2, %2 quad_perm:[1,0,3,2] row_mask:0xf bank_mask:0xf\n\t"
      "v_add_f32_dpp %4, %4, %4 quad_perm:[1,0,3,2] row_mask:0xf bank_mask:0xf\n\t"
      "v_add_f32_dpp %6, %6, %6 quad_perm:[1,0,3,2] row_mask:0xf bank_mask:0xf\n\t"
      "v_add_f32_dpp %0, %0, %0 quad_perm:[2,3,0,1] row_mask:0xf bank_mask:0xf\n\t"
      "v_add_f32_dpp %2, %2, %2 quad_perm:[2,3,0,1] row_mask:0xf bank_mask:0xf\n\t"
      "v_add_f32_dpp %4, %4, %4 quad_perm:[2,3,0,1] row_mask:0xf bank_mask:0xf\n\t"
      "v_add_f32_dpp %6, %6, %6 quad_perm:[2,3,0,1] row_mask:0xf bank_mask:0xf\n\t"
      "s_nop 1"
      : "+v"(m0), "+v"(m1), "+v"(m2), "+v"(m3), "=&v"(m4), "=&v"(m5), "=&v"(m6), "=&v"(m7)
      : "v"(a[1][0]), "v"(a[1][1]), "v"(a[1][2]), "v"(a[1][3]), "v"(a[1][4]), "v"(a[1][5]), "v"(a[1][6]), "v"(a[1][7]));
  r[0][0] = m0; r[0][1] = m2; r[1][0] = m4; r[1][1] = m6;
}
DEV u64 gload64(const char* p) { return __hip_atomic_load((GAS u64*)(size_t)p, __ATOMIC_RELAXED, __HIP_MEMORY_SCOPE_AGENT); }
DEV void gstore64(const char* p, u64 v) { __hip_atomic_store((GAS u64*)(size_t)p, v, __ATOMIC_RELAXED, __HIP_MEMORY_SCOPE_AGENT); }
DEV void poll_flags(const char* p, bool active, u64 T) {
  for (unsigned it = 0; it < SPIN_CAP; ++it) {
    bool ok = !active || gload64(p) == T;
    if (__all(ok)) break;
    __builtin_amdgcn_s_sleep(1);
  }
}
DEV void settle_one(const u32x4& g, unsigned voff, const char* sbase, unsigned T32, unsigned& v0, unsigned& v1) {
  const bool ok = g.y == T32 && g.w == T32;
  const unsigned all_ok = uni((unsigned)__all(ok));
  unsigned a = g.x, b = g.z, t0, t1, cnt;
  u64 m;
  asm volatile(
      "s_cmp_lg_u32 %6, 0\n\t"
      "s_cbranch_scc1 .Lsettled%=\n\t"
      "s_mov_b32 %4, %10\n\t"
      "s_nop 4\n"
      ".Lagain%=:\n\t"
      "s_sleep 1\n\t"
      "global_load_dword %2, %7, %8 offset:4 sc1\n\t"
      "global_load_dword %3, %7, %8 offset:12 sc1\n\t"
      "s_waitcnt vmcnt(0)\n\t"
      "v_cmp_ne_u32 vcc, %9, %2\n\t"
      "v_cmp_ne_u32 %5, %9, %3\n\t"
      "s_or_b64 vcc, vcc, %5\n\t"
      "s_cmp_eq_u64 vcc, 0\n\t"
      "s_cbranch_scc1 .Lseen%=\n\t"
      "s_sub_u32 %4, %4, 1\n\t"
      "s_cbranch_scc0 .Lagain%=\n"
      ".Lseen%=:\n\t"
      "global_load_dword %0, %7, %8 sc1\n\t"
      "global_load_dword %1, %7, %8 offset:8 sc1\n\t"
      "s_waitcnt vmcnt(0)\n"
      ".Lsettled%=:"
      : "+v"(a), "+v"(b), "=&v"(t0), "=&v"(t1), "=&s"(cnt), "=&s"(m)
      : "s"(all_ok), "v"(voff), "s"(uni(sbase)), "s"(uni(T32)), "n"(SPIN_CAP)
      : "memory", "vcc", "scc");
  v0 = a; v1 = b;
}
DEV void settle_two(const u32x4& g0, const u32x4& g1, unsigned voff, const char* sbase, unsigned T32, float (&v)[4]) {
  const bool ok = g0.y == T32 && g0.w == T32 && g1.y == T32 && g1.w == T32;
  const unsigned all_ok = uni((unsigned)__all(ok));
  unsigned a = g0.x, b = g0.z, d = g1.x, e = g1.z, t0, t1, t2, t3, cnt;
  u64 m;
  asm volatile(
      "s_cmp_lg_u32 %10, 0\n\t"
      "s_cbranch_scc1 .Lsettled%=\n\t"
      "s_mov_b32 %8, %14\n\t"
      "s_nop 4\n"
      ".Lagain%=:\n\t"
      "s_sleep 1\n\t"
      "global_load_dword %4, %11, %12 offset:4 sc1\n\t"
      "global_load_dword %5, %11, %12 offset:12 sc1\n\t"
      "global_load_dword %6, %11, %12 offset:2052 sc1\n\t"
      "global_load_dword %7, %11, %12 offset:2060 sc1\n\t"
      "s_waitcnt vmcnt(0)\n\t"
      "v_cmp_ne_u32 vcc, %13, %4\n\t"
      "v_cmp_ne_u32 %9, %13, %5\n\t"
      "s_or_b64 vcc, vcc, %9\n\t"
      "v_cmp_ne_u32 %9, %13, %6\n\t"
      "s_or_b64 vcc, vcc, %9\n\t"
      "v_cmp_ne_u32 %9, %13, %7\n\t"
      "s_or_b64 vcc, vcc, %9\n\t"
      "s_cmp_eq_u64 vcc, 0\n\t"
      "s_cbranch_scc1 .Lseen%=\n\t"
      "s_sub_u32 %8, %8, 1\n\t"
      "s_cbranch_scc0 .Lagain%=\n"
      ".Lseen%=:\n\t"
      "global_load_dword %0, %11, %12 sc1\n\t"
      "global_load_dword %1, %11, %12 offset:8 sc1\n\t"
      "global_load_dword %2, %11, %12 offset:2048 sc1\n\t"
      "global_load_dword %3, %11, %12 offset:2056 sc1\n\t"
      "s_waitcnt vmcnt(0)\n"
      ".Lsettled%=:"
      : "+v"(a), "+v"(b), "+v"(d), "+v"(e), "=&v"(t0), "=&v"(t1), "=&v"(t2), "=&v"(t3), "=&s"(cnt), "=&s"(m)
      : "s"(all_ok), "v"(voff), "s"(uni(sbase)), "s"(uni(T32)), "n"(SPIN_CAP)
      : "memory", "vcc", "scc");
  v[0] = __uint_as_float(a); v[1] = __uint_as_float(b); v[2] = __uint_as_float(d); v[3] = __uint_as_float(e);
}
DEV void sample_flag(u64& s, unsigned voff, const char* sbase) {
  asm volatile("global_load_dwordx2 %0, %1, %2 sc1" : "=v"(s) : "v"(voff), "s"(uni(sbase)) : "memory");
}
template <int N> DEV bool sampled(u64& s, u64 T) {
  asm volatile("s_waitcnt vmcnt(%1) ; TIE %0" : "+v"(s) : "n"(N) : "memory");
  return __all(s == T);
}
DEV u32x4 aload128c(unsigned voff, const char* sbase) {
  u32x4 r;
  asm volatile("global_load_dwordx4 %0, %1, %2 sc1" : "=v"(r) : "v"(voff), "s"(uni(sbase)) : "memory");
  return r;
}
DEV u32x2 aload64c(unsigned voff, const char* sbase) {
  u32x2 r;
  asm volatile("global_load_dwordx2 %0, %1, %2 sc1" : "=v"(r) : "v"(voff), "s"(uni(sbase)) : "memory");
  return r;
}

template <int V> struct IC { static constexpr int v = V; };
template <int A, int B, class F> DEV void static_for(F&& f) {
  if constexpr (A < B) { f(IC<A>()); static_for<A + 1, B>(f); }
}
struct Ctx {
  const char *x, *wqkv, *wo, *win, *wsp, *bsp, *wout;
  char *out, *ws;
  LAS char* L;
  unsigned lb;
  int tid, lane, w, bid;
  unsigned Tlo, Thi;
  __amdgpu_buffer_rsrc_t rs;
};
DEV u64 tagT(const Ctx& c) { return ((u64)c.Thi << 32) | c.Tlo; }

DEV void s0_issue(const Ctx& c, u32x4& xr, u32x2& ev, int row) {
  ev = aload64c(0, c.ws + WS_EPOCH);
  asm volatile("global_load_dwordx4 %0, %1, %2" : "=v"(xr) : "v"(c.lane * 16), "s"(uni(c.x + (size_t)row * 1024)) : "memory");
}
template <int P> DEV void s0_finish(Ctx& c, u32x4 xr, u32x2 ev, int row) {
  asm volatile("s_waitcnt vmcnt(%2) ; TIE %0 %1" : "+v"(xr), "+v"(ev) : "n"(P) : "memory");
  u64 e = (((u64)(unsigned)__builtin_amdgcn_readfirstlane(ev.y)) << 32) | (unsigned)__builtin_amdgcn_readfirstlane(ev.x);
  e += 1;
  c.Tlo = (unsigned)e; c.Thi = (unsigned)(e >> 32);
  unsigned x0 = xr.x, x1 = xr.y, x2 = xr.z, x3 = xr.w;
  float v[8] = {blo(x0), bhi(x0), blo(x1), bhi(x1), blo(x2), bhi(x2), blo(x3), bhi(x3)};
  float s = ((v[0] + v[1]) + (v[2] + v[3])) + ((v[4] + v[5]) + (v[6] + v[7]));
  s = sumq(sum16(s));
  float m = s * (1.0f / 512.0f), q = 0.f;
#pragma unroll
  for (int j = 0; j < 8; ++j) { float d = v[j] - m; q = __builtin_fmaf(d, d, q); }
  q = sumq(sum16(q));
  float r = __builtin_amdgcn_rsqf(q * (1.0f / 512.0f) + 1e-5f);
  if (c.lane < 2)
    gstore64(c.ws + WS_XS + row * 16 + c.lane * 8, ((u64)c.Tlo << 32) | __float_as_uint(c.lane ? r : m));
}

struct G3A { unsigned vw; const char* bw; unsigned k0; };
DEV G3A g3_addr(const Ctx& c, int rt, int sl) {
  const int r = (c.w & 1) * 32 + (c.lane >> 3);
  G3A a;
  a.vw = r * 4096 + (c.w >> 1) * 1024 + (((c.lane & 7) ^ (c.lane >> 4)) << 4);
  a.k0 = (sl & 1) * 8;
  a.bw = c.wsp + (size_t)rt * 64 * 4096;
  return a;
}
constexpr unsigned w_stage(int u) { return ((u & 2) ? 0 : W_STG) + (u & 1) * 4096; }
template <int U, int H> DEV void w_dma_to(const Ctx& c, const G3A& a, unsigned lds) {
  const char* b = a.bw + (((a.k0 >> 1) + U) & 7) * 128 + H * 32768;
  dma16((H & 1) ? a.vw ^ 64 : a.vw, b, lds);
}
template <int U, int H> DEV void w_dma(const Ctx& c, const G3A& a) {
  w_dma_to<U, H>(c, a, c.lb + w_stage(U) + c.w * 8192 + H * 1024);
}
template <int U> DEV void w_issue(const Ctx& c, const G3A& a) { w_dma<U, 0>(c, a); w_dma<U, 1>(c, a); w_dma<U, 2>(c, a); w_dma<U, 3>(c, a); }
struct WF { u32x4 f[32]; };
DEV unsigned w_lane(const Ctx& c) { const int t = c.lane & 15, q = c.lane >> 4; return t * 128 + ((q ^ (t >> 1)) << 4); }
template <int U> DEV void w_take_from(const Ctx& c, WF& W, unsigned b0, unsigned b1) {
  const unsigned o = w_lane(c);
  W.f[4 * U + 0] = lds128(c.L + b0 + o);
  W.f[4 * U + 1] = lds128(c.L + b1 + o);
  W.f[4 * U + 2] = lds128(c.L + b0 + (o ^ 64));
  W.f[4 * U + 3] = lds128(c.L + b1 + (o ^ 64));
}
template <int U> DEV void w_take(const Ctx& c, WF& W) {
  const unsigned b = w_stage(U) + c.w * 8192;
  w_take_from<U>(c, W, b, b + 2048);
}
DEV void wait_lds() { asm volatile("s_waitcnt lgkmcnt(0)" ::: "memory"); }

DEV void tlb_touch(const Ctx& c) {
  const int n = c.tid;
  const char* p = c.ws + (size_t)n * 65536;
  if (n >= 224) p = c.wsp + (size_t)(n - 224) * 65536;
  if (n >= 352) p = c.out + (size_t)(n - 352) * 65536;
  if (n >= 384) p = c.wo + (size_t)(n - 384) * 65536;
  if (n >= 392) p = c.wout + (size_t)(n - 392) * 65536;
  if (n >= 403) p = c.bsp;
  Ctx0 k = {c.lb + T_DUMMY};
  touch(k, p);
}
DEV void l2_warm(const Ctx& c, int rt, int a16, int half, int xcol) {
  Ctx0 k = {c.lb + T_DUMMY};
  int n = c.w * 64 + c.lane; n = n < 300 ? n : 299;
  int row = a16 * 75 + (n >> 2);
  const char* p = row < 512 ? c.wo + (size_t)row * 1024 : c.wout + (size_t)(row - 512) * 1024;
  if (c.w < 5) touch(k, p + xcol * 512 + (n & 3) * 128);
}

struct AAddr { unsigned vx0, vx1, vh, vw; const char *bx, *bh, *bw; u64 hmask; };
template <int H, int P> DEV void a_dma(const Ctx& c, const AAddr& a, int s, unsigned slot) {
  const unsigned l = c.lb + slot;
  if (H == 0) {
    if (P == 0) dma16(a.vx0, a.bx + s * 128, l + c.w * 2048);
    if (P == 1) dma16(a.vx1, a.bx + s * 128, l + c.w * 2048 + 1024);
    if (P == 2) dma16m(a.vh, a.bh + s * 128, l + 128 * 128 + c.w * 256, a.hmask);
  } else {
    dma16(a.vw, a.bw + (size_t)s * 64 * 3072 + P * 1024, l + A_W + P * 8192 + c.w * 1024);
  }
}
template <int H> DEV void a_half(const Ctx& c, const AAddr& a, int s, unsigned slot) {
  a_dma<H, 0>(c, a, s, slot); a_dma<H, 1>(c, a, s, slot); a_dma<H, 2>(c, a, s, slot);
}

struct AAcc { f32x4 q[2][2], k[2][2], v[2][2], h, qc; };
struct AF { u32x4 x0, x1, hx, qa, qb, ka, kb, va, vb; };
struct AOfs { unsigned xo, xh, wA, wB; };

DEV void a_read(const Ctx& c, AF& F, unsigned slot, int kk, const AOfs& o) {
  LAS char* L = c.L + slot;
  LAS char* pa = L + A_W + o.wA + kk * 4096;
  LAS char* pb = L + A_W + o.wB + kk * 4096;
  F.qa = trfrag(pa, 512);
  F.x0 = lds128(L + (o.xo ^ (kk * 64)));
  F.x1 = lds128(L + (o.xo ^ (kk * 64)) + 2048);
  F.qb = trfrag(pb, 512);
  F.ka = trfrag(pa + 8192, 512); F.kb = trfrag(pb + 8192, 512);
  F.va = trfrag(pa + 16384, 512); F.vb = trfrag(pb + 16384, 512);
  F.hx = lds128(L + (o.xh ^ (kk * 64)));
  sb();
}
template <bool HV, int H, bool NEXT> DEV void a_step(const Ctx& c, AAcc& A, const AF& F, AF& N, unsigned rslot, int kk, const AOfs& o,
                                                     const AAddr& a, int sn, unsigned slot) {
  LAS char* L = c.L + rslot;
  LAS char* pa = L + A_W + o.wA + kk * 4096;
  LAS char* pb = L + A_W + o.wB + kk * 4096;
  LAS char* px = L + (o.xo ^ (kk * 64));
  sb();
  A.q[0][0] = mfmaX(F.qa, F.x0, A.q[0][0]); sb();
  if (NEXT) N.qa = trfrag(pa, 512);
  sb();
  A.q[1][0] = mfmaX(F.qa, F.x1, A.q[1][0]); sb();
  if (NEXT) { N.x0 = lds128(px); N.x1 = lds128(px + 2048); }
  sb();
  A.q[0][1] = mfmaX(F.qb, F.x0, A.q[0][1]); sb();
  if (NEXT) N.qb = trfrag(pb, 512);
  sb();
  A.q[1][1] = mfmaX(F.qb, F.x1, A.q[1][1]); sb();
  if (NEXT) N.ka = trfrag(pa + 8192, 512);
  sb();
  A.k[0][0] = mfmaX(F.ka, F.x0, A.k[0][0]); sb();
  if (NEXT) N.kb = trfrag(pb + 8192, 512);
  sb();
  A.k[1][0] = mfmaX(F.ka, F.x1, A.k[1][0]); sb();
  if (NEXT) N.va = trfrag(pa + 16384, 512);
  sb();
  A.k[0][1] = mfmaX(F.kb, F.x0, A.k[0][1]); sb();
  if (NEXT) N.vb = trfrag(pb + 16384, 512);
  sb();
  A.k[1][1] = mfmaX(F.kb, F.x1, A.k[1][1]); sb();
  if (NEXT) N.hx = lds128(L + (o.xh ^ (kk * 64)));
  sb();
  A.v[0][0] = mfmaX(F.x0, F.va, A.v[0][0]); sb();
  if (H >= 0) a_dma<(H < 0 ? 0 : H), 0>(c, a, sn, slot);
  sb();
  A.v[1][0] = mfmaX(F.x1, F.va, A.v[1][0]);
  A.v[0][1] = mfmaX(F.x0, F.vb, A.v[0][1]); sb();
  if (H >= 0) a_dma<(H < 0 ? 0 : H), 1>(c, a, sn, slot);
  sb();
  A.v[1][1] = mfmaX(F.x1, F.vb, A.v[1][1]);
  if (HV) {
    A.h = mfmaX(F.hx, F.va, A.h);
  } else {
    A.h = mfmaX(F.ka, F.hx, A.h);
  }
  sb();
  if (H >= 0) a_dma<(H < 0 ? 0 : H), 2>(c, a, sn, slot);
  sb();
  if (!HV) A.qc = mfmaX(F.qa, F.hx, A.qc);
  sb();
}

template <bool HV> DEV void a_loop(const Ctx& c, AAcc& A, const AAddr& a, const AOfs& o, u32x4& g, unsigned gofs) {
  AF F0, F1;
  wait_vm<9 + (PF & 1)>();
  bar();
  a_read(c, F0, 0, 0, o);
  unsigned s0 = 0, s1 = A_SLOT, s2 = 2 * A_SLOT;
#pragma unroll 1
  for (int s = 0; s < 5; ++s) {
    a_step<HV, 1, true>(c, A, F0, F1, s0, 1, o, a, s + 2, s2);
    wait_vm<6>();
    bar();
    STAMP1(16 + s);
    a_step<HV, 0, true>(c, A, F1, F0, s1, 0, o, a, s + 3, s0);
    unsigned t = s0; s0 = s1; s1 = s2; s2 = t;
  }
  a_step<HV, 1, true>(c, A, F0, F1, 2 * A_SLOT, 1, o, a, 7, A_SLOT);
  wait_vm<6>();
  bar();
  g = aload128c(gofs, c.ws + WS_XS);
  a_step<HV, -1, true>(c, A, F1, F0, 0, 0, o, a, 0, 0);
  a_step<HV, -1, true>(c, A, F0, F1, 0, 1, o, a, 0, 0);
  asm volatile("s_waitcnt vmcnt(0) ; TIE %0" : "+v"(g) : : "memory");
  bar();
  a_step<HV, -1, true>(c, A, F1, F0, A_SLOT, 0, o, a, 0, 0);
  a_step<HV, -1, true>(c, A, F0, F1, A_SLOT, 1, o, a, 0, 0);
  a_step<HV, -1, false>(c, A, F1, F0, 0, 0, o, a, 0, 0);
}

DEV void job_a(Ctx& c, int i, int hc, int s0row, const G3A& g3, WF& W, int rt, int half, int xcol) {
  u32x4 xr; u32x2 ev;
  s0_issue(c, xr, ev, s0row);
  char* ws = c.ws;
  const int lane = c.lane, w = c.w, rg = w & 3, cg = w >> 2;
  const int t = lane & 15, q = lane >> 4;
  AAddr a;
  {
    int r0 = 16 * w + (lane >> 3);
    a.vx0 = r0 * 1024 + (((lane & 7) ^ ((r0 >> 1) & 7)) << 4);
    int r1 = r0 + 8;
    a.vx1 = r1 * 1024 + (((lane & 7) ^ ((r1 >> 1) & 7)) << 4);
    a.bx = c.x + (size_t)i * 128 * 1024;
    int rh = 2 * w + ((lane >> 3) & 1);
    a.vh = rh * 1024 + (((lane & 7) ^ w) << 4);
    a.bh = c.x + (size_t)(i > 0 ? i * 128 - 16 : 0) * 1024;
    a.hmask = w == 0 ? 0xff00ull : 0xffffull;
    int k = 8 * w + (lane >> 3);
    int f = ((k >> 1) & 1) | (((k >> 3) & 1) << 1);
    a.vw = k * 3072 + (((lane & 7) ^ (f << 1)) << 4);
    a.bw = c.wqkv + hc * 128;
  }
  if (c.tid < 24) {
    u32x4 ones = {0x3f803f80u, 0x3f803f80u, 0x3f803f80u, 0x3f803f80u};
    *(LAS u32x4*)(c.L + (c.tid >> 3) * A_SLOT + 128 * 128 + (c.tid & 7) * 16) = ones;
  }
  a_half<0>(c, a, 0, 0); a_half<1>(c, a, 0, 0);
  if (PB & 4) bar0();
  a_half<0>(c, a, 1, A_SLOT); a_half<1>(c, a, 1, A_SLOT);
  s0_finish<12>(c, xr, ev, s0row);
  STAMP(15);
  a_half<0>(c, a, 2, 2 * A_SLOT);
  if (PF & 1) tlb_touch(c);

  const unsigned xlane = t * 128 + ((q ^ (t >> 1)) << 4);
  const unsigned xo = rg * 32 * 128 + xlane, xh = 128 * 128 + xlane;
  const int f = ((t >> 3) & 1) | ((q & 1) << 1);
  const int hA = 2 * cg + (rg & 1), hB = hA ^ 1;
  const unsigned wl = (8 * q + (t >> 2)) * 128 + (t & 3) * 8;
  const unsigned wA = wl + ((hA ^ f) << 5), wB = wl + ((hB ^ f) << 5);
  const int gj = c.tid < 144 ? c.tid : 143;
  const int gtok = gj < 128 ? i * 128 + gj : (i > 0 ? i * 128 - 16 + (gj - 128) : gj - 128);
  const unsigned gofs = gtok * 16;

  AAcc A;
  const f32x4 z4 = {0.f, 0.f, 0.f, 0.f};
#pragma unroll
  for (int x = 0; x < 2; ++x)
#pragma unroll
    for (int y = 0; y < 2; ++y) { A.q[x][y] = z4; A.k[x][y] = z4; A.v[x][y] = z4; }
  A.h = z4; A.qc = z4;
  u32x4 g = {0, 0, 0, 0};
  const bool hv = rg >= 2;
  const AOfs ao = {xo, xh, wA, wB};
  if (hv) a_loop<true>(c, A, a, ao, g, gofs);
  else a_loop<false>(c, A, a, ao, g, gofs);
  STAMP(2);
  unsigned gm, gr;
  settle_one(g, gofs, c.ws + WS_XS, c.Tlo, gm, gr);
  if (c.tid < 144) {
    *(LAS unsigned*)(c.L + T_XM + c.tid * 4) = gm;
    *(LAS unsigned*)(c.L + T_XR + c.tid * 4) = gr;
  }
  if (!hv) {
    if (t == 0) {
      *(LAS f32x4*)(c.L + T_CS + (64 + hA * 16 + 4 * q) * 4) = A.h;
      *(LAS f32x4*)(c.L + T_CS + (hA * 16 + 4 * q) * 4) = A.qc;
    }
  } else if (lane < 16) {
    *(LAS float*)(c.L + T_CS + (128 + hA * 16 + lane) * 4) = A.h[0];
  }
  bar();
  STAMP(18);
  w_issue<0>(c, g3); w_issue<1>(c, g3);
  sb();
  STAMP(19);
  u32x2 Q[2][2], K[2][2], V[2][2], H;
  {
    f32x4 csq[2], csk[2]; float csv[2];
#pragma unroll
    for (int y = 0; y < 2; ++y) {
      int hh = y ? hB : hA;
      csq[y] = *(LAS f32x4*)(c.L + T_CS + (hh * 16 + 4 * q) * 4);
      csk[y] = *(LAS f32x4*)(c.L + T_CS + (64 + hh * 16 + 4 * q) * 4);
      csv[y] = *(LAS float*)(c.L + T_CS + (128 + hh * 16 + t) * 4);
    }
#pragma unroll
    for (int x = 0; x < 2; ++x) {
      const int tk = rg * 32 + x * 16;
      const float m1 = -*(LAS float*)(c.L + T_XM + (tk + t) * 4), r1 = *(LAS float*)(c.L + T_XR + (tk + t) * 4);
      const f32x4 m4 = *(LAS f32x4*)(c.L + T_XM + (tk + 4 * q) * 4), r4 = *(LAS f32x4*)(c.L + T_XR + (tk + 4 * q) * 4);
      const f32x2 nm = splat(m1), rk = splat(r1), rq = splat(r1 * 0.25f);
#pragma unroll
      for (int y = 0; y < 2; ++y) {
        f32x4 aq = A.q[x][y], ak = A.k[x][y], av = A.v[x][y];
        const f32x2 ncv = splat(-csv[y]);
        Q[x][y].x = pk2(rq * fma2(nm, lo2(csq[y]), lo2(aq)));
        Q[x][y].y = pk2(rq * fma2(nm, hi2(csq[y]), hi2(aq)));
        K[x][y].x = pk2(rk * fma2(nm, lo2(csk[y]), lo2(ak)));
        K[x][y].y = pk2(rk * fma2(nm, hi2(csk[y]), hi2(ak)));
        V[x][y].x = pk2(lo2(r4) * fma2(lo2(m4), ncv, lo2(av)));
        V[x][y].y = pk2(hi2(r4) * fma2(hi2(m4), ncv, hi2(av)));
      }
      sb();
      if (x == 0) w_issue<2>(c, g3); else w_issue<3>(c, g3);
      sb();
    }
    if (!hv) {
      const f32x2 nm = splat(-*(LAS float*)(c.L + T_XM + (128 + t) * 4)), rk = splat(*(LAS float*)(c.L + T_XR + (128 + t) * 4));
      H.x = pk2(rk * fma2(nm, lo2(csk[0]), lo2(A.h)));
      H.y = pk2(rk * fma2(nm, hi2(csk[0]), hi2(A.h)));
    } else {
      const f32x4 m4 = *(LAS f32x4*)(c.L + T_XM + (128 + 4 * q) * 4), r4 = *(LAS f32x4*)(c.L + T_XR + (128 + 4 * q) * 4);
      const f32x2 ncv = splat(-csv[0]);
      H.x = pk2(lo2(r4) * fma2(lo2(m4), ncv, lo2(A.h)));
      H.y = pk2(hi2(r4) * fma2(hi2(m4), ncv, hi2(A.h)));
    }
  }
  const unsigned KX = T_KX, VX = T_VX;
  {
    *(LAS u32x2*)(c.L + (hv ? VX : KX) + hA * 512 + lane * 8) = H;
    if (rg < 3) {
      *(LAS u32x2*)(c.L + KX + ((rg + 1) * 4 + hA) * 512 + lane * 8) = K[1][0];
      *(LAS u32x2*)(c.L + KX + ((rg + 1) * 4 + hB) * 512 + lane * 8) = K[1][1];
      *(LAS u32x2*)(c.L + VX + ((rg + 1) * 4 + hA) * 512 + lane * 8) = V[1][0];
      *(LAS u32x2*)(c.L + VX + ((rg + 1) * 4 + hB) * 512 + lane * 8) = V[1][1];
    }
  }
  if (PF & 2) l2_warm(c, rt, (rt & 7) * 2 + half, half, xcol);
  bar();
  STAMP(20);
  u32x2 Kp[2], Vp[2];
  Kp[0] = lds64(c.L + KX + (rg * 4 + hA) * 512 + lane * 8);
  Kp[1] = lds64(c.L + KX + (rg * 4 + hB) * 512 + lane * 8);
  Vp[0] = lds64(c.L + VX + (rg * 4 + hA) * 512 + lane * 8);
  Vp[1] = lds64(c.L + VX + (rg * 4 + hB) * 512 + lane * 8);
  if (PF & 2) wait_vm<1>(); else wait_vm<0>();
  if ((PF & 2) && w >= 5) wait_vm<0>();
  w_take<0>(c, W); w_take<1>(c, W); w_take<2>(c, W); w_take<3>(c, W);
  wait_lds();
  w_issue<4>(c, g3);
  sb();

  const float NEG = -1e30f, LOG2E = 1.4426950408889634f;
  const bool odd = t & 1;
  const bool cur0 = 4 * q + (t & 1) <= t, cur1 = 4 * q + (t & 1) + 2 <= t;
  const bool first = (i == 0 && rg == 0);
  const float nb0 = (first && !cur0) ? NEG : 0.f, nb1 = (first && !cur1) ? NEG : 0.f;
  const unsigned s0 = odd ? 0x01000c0cu : 0x0c0c0100u, s1 = odd ? 0x03020c0cu : 0x0c0c0302u;
  const unsigned sPx = cur0 ? 0x0c0c0c0cu : s0, sPz = cur0 ? s0 : 0x0c0c0c0cu;
  const unsigned sPy = cur1 ? 0x0c0c0c0cu : s1, sPw = cur1 ? s1 : 0x0c0c0c0cu;
  float e0[2][2], e1[2][2];
#pragma unroll
  for (int x = 0; x < 2; ++x)
#pragma unroll
    for (int y = 0; y < 2; ++y) {
      u32x2 kp = x ? K[0][y] : Kp[y];
      f32x4 sp = mfma16(kp, Q[x][y], z4), sc = mfma16(K[x][y], Q[x][y], z4);
      float c0 = odd ? sc[1] : sc[0], p0 = odd ? sp[1] : sp[0], c1 = odd ? sc[3] : sc[2], p1 = odd ? sp[3] : sp[2];
      float a0 = cur0 ? c0 : p0, a1 = cur1 ? c1 : p1;
      e0[x][y] = x ? a0 * LOG2E : __builtin_fmaf(a0, LOG2E, nb0);
      e1[x][y] = x ? a1 * LOG2E : __builtin_fmaf(a1, LOG2E, nb1);
    }
  sb();
  w_issue<5>(c, g3);
  sb();
  float red[4];
  red[0] = fmaxf(e0[0][0], e1[0][0]); red[1] = fmaxf(e0[0][1], e1[0][1]);
  red[2] = fmaxf(e0[1][0], e1[1][0]); red[3] = fmaxf(e0[1][1], e1[1][1]);
  rows4<true>(red);
#pragma unroll
  for (int k = 0; k < 4; ++k) {
    e0[k >> 1][k & 1] = __builtin_amdgcn_exp2f(e0[k >> 1][k & 1] - red[k]);
    e1[k >> 1][k & 1] = __builtin_amdgcn_exp2f(e1[k >> 1][k & 1] - red[k]);
  }
#pragma unroll
  for (int k = 0; k < 4; ++k) red[k] = e0[k >> 1][k & 1] + e1[k >> 1][k & 1];
  rows4<false>(red);
#pragma unroll
  for (int x = 1; x >= 0; --x) {
    sb();
    if (x == 1) w_issue<6>(c, g3); else w_issue<7>(c, g3);
    sb();
    u32x2 O[2];
#pragma unroll
    for (int y = 0; y < 2; ++y) {
      u32x2 vp = x ? V[0][y] : Vp[y];
      float inv = __builtin_amdgcn_rcpf(red[2 * x + y]);
      unsigned pw = pk2(e0[x][y] * inv, e1[x][y] * inv);
      u32x4 P = {__builtin_amdgcn_perm(0u, pw, sPx), __builtin_amdgcn_perm(0u, pw, sPy), __builtin_amdgcn_perm(0u, pw, sPz),
                 __builtin_amdgcn_perm(0u, pw, sPw)};
      u32x4 VV = {vp.x, vp.y, V[x][y].x, V[x][y].y};
      f32x4 o = mfma32(VV, P, z4);
      O[y].x = pk2(o[0], o[1]); O[y].y = pk2(o[2], o[3]);
    }
    u32x4 fr = pairswap(O[0], O[1]);
    int qp = (((q & 1) << 1) | (q >> 1)) ^ ((rg & 1) << 1);
    unsigned off = WS_O + ((i * 8 + rg * 2 + x) * 16 + 2 * hc + cg) * 1024 + qp * 256 + t * 16;
    __builtin_amdgcn_raw_buffer_store_b128(fr, c.rs, off, 0, 16);
  }
  STAMP(3);
  wait_vm<10>();
  w_take<4>(c, W); w_take<5>(c, W);
}

struct ZAddr { unsigned vx0, vx1, vw; const char *bx, *bw; };
constexpr unsigned z_park(int u, int tb) { return (2 * u + tb) * Z_SLOT; }
template <int S, int H, int P> DEV void z_dma(const Ctx& c, const ZAddr& a) {
  if (S >= 16 || S < 0) return;
  const unsigned l = c.lb + (S % 5) * Z_SLOT;
  const char* bw = a.bw + (size_t)(S & 7) * 64 * 2752 + (S < 8 ? 1376 : 0);
  if (S < 8) {
    if (H == 0) dma16(P ? a.vx1 : a.vx0, a.bx + S * 128, l + c.w * 2048 + P * 1024);
    else dma16m(a.vw, bw + P * 4 * 2752, l + Z_W + c.w * 1536 + P * 768, 0xffffffffffffull);
  } else if (P == 0) {
    dma16m(a.vw, bw + H * 4 * 2752, l + Z_W + c.w * 1536 + H * 768, 0xffffffffffffull);
  }
}
template <int S, int H> DEV void z_half(const Ctx& c, const ZAddr& a) { z_dma<S, H, 0>(c, a); z_dma<S, H, 1>(c, a); }
template <int U> DEV void z_park_issue(const Ctx& c, const G3A& g3) {
  w_dma_to<U, 0>(c, g3, c.lb + z_park(U, 0) + c.w * 2048);
  w_dma_to<U, 1>(c, g3, c.lb + z_park(U, 0) + c.w * 2048 + 1024);
  w_dma_to<U, 2>(c, g3, c.lb + z_park(U, 1) + c.w * 2048);
  w_dma_to<U, 3>(c, g3, c.lb + z_park(U, 1) + c.w * 2048 + 1024);
}
constexpr int z_n(int s) { return s >= 16 ? 0 : (s < 8 ? 4 : 2); }
constexpr int z_wait(int s) { return z_n(s + 1) + z_n(s + 2) + z_n(s + 3); }

struct ZW { u32x4 f[3]; };
template <int S, int KK> DEV void z_read_w(const Ctx& c, ZW& F, const unsigned (&wb)[3]) {
  LAS char* L = c.L + (S % 5) * Z_SLOT;
#pragma unroll
  for (int j = 0; j < 3; ++j) F.f[j] = trfrag(L + Z_W + wb[j] + KK * 6144, 768);
}
template <int S, int KK> DEV void z_read_x(const Ctx& c, u32x4 (&xf)[2][16], unsigned xo) {
  LAS char* L = c.L + (S % 5) * Z_SLOT;
  xf[0][2 * S + KK] = lds128(L + (xo ^ (KK * 64)));
  xf[1][2 * S + KK] = lds128(L + (xo ^ (KK * 64)) + 2048);
}
template <bool SWAP, int J, int RS, int RK, int SN, int H>
DEV void z_step(const Ctx& c, const ZAddr& a, u32x4 (&xf)[2][16], const ZW& F, ZW& N, f32x4 (&acc)[2][3], f32x4& cs, unsigned xo,
                const unsigned (&wb)[3]) {
  const u32x4 ones = {0x3f803f80u, 0x3f803f80u, 0x3f803f80u, 0x3f803f80u};
  constexpr int R = RS < 0 ? 0 : RS, JN = (2 * R + RK) & 15;
  LAS char* L = c.L + (R % 5) * Z_SLOT;
  auto mm = [&](int x, int j) { acc[x][j] = SWAP ? mfmaX(F.f[j], xf[x][J], acc[x][j]) : mfmaX(xf[x][J], F.f[j], acc[x][j]); };
  sb();
  mm(0, 0); sb();
  if (RS >= 0) N.f[0] = trfrag(L + Z_W + wb[0] + RK * 6144, 768);
  sb();
  mm(1, 0); sb();
  if (RS >= 0 && !SWAP) { xf[0][JN] = lds128(L + (xo ^ (RK * 64))); xf[1][JN] = lds128(L + (xo ^ (RK * 64)) + 2048); }
  if (RS >= 0 && SWAP) N.f[1] = trfrag(L + Z_W + wb[1] + RK * 6144, 768);
  sb();
  mm(0, 1); sb();
  if (RS >= 0 && !SWAP) N.f[1] = trfrag(L + Z_W + wb[1] + RK * 6144, 768);
  if (RS >= 0 && SWAP) N.f[2] = trfrag(L + Z_W + wb[2] + RK * 6144, 768);
  sb();
  mm(1, 1); sb();
  if (RS >= 0 && !SWAP) N.f[2] = trfrag(L + Z_W + wb[2] + RK * 6144, 768);
  sb();
  mm(0, 2); sb();
  z_dma<SN, H, 0>(c, a);
  sb();
  mm(1, 2); sb();
  z_dma<SN, H, 1>(c, a);
  sb();
  cs = SWAP ? mfmaX(F.f[0], ones, cs) : mfmaX(ones, F.f[0], cs);
  sb();
}
template <int S> DEV void z_stage_a(const Ctx& c, const ZAddr& a, u32x4 (&xf)[2][16], ZW& F0, ZW& F1, f32x4 (&acc)[2][3], f32x4& cs,
                                    unsigned xo, const unsigned (&wb)[3], u32x4& g, unsigned gofs) {
  z_step<false, 2 * S, S, 1, (S == 0 ? -1 : S + 4), 1>(c, a, xf, F0, F1, acc, cs, xo, wb);
  if (S == 7) asm volatile("s_waitcnt vmcnt(4) ; TIE %0" : "+v"(g) : : "memory");
  else wait_vm<z_wait(S + 1)>();
  bar();
  STAMP1(16 + S);
  if (S == 5) g = aload128c(gofs, c.ws + WS_XS);
  z_step<false, 2 * S + 1, (S < 7 ? S + 1 : -1), 0, S + 5, 0>(c, a, xf, F1, F0, acc, cs, xo, wb);
}
struct ZPub { u32x2 Z2[2][3]; u32x4 gs[2]; unsigned sofs, zoff[3]; const char* sbase; };
constexpr int zb_wait(int S) {
  return S == ZG + 2 ? (ZG == 10 ? 2 : 0)
       : S > ZG + 2 ? (S == 14 ? 3 : 3 + 2)
       : z_wait(S + 1) + (S <= 10 ? 8 : 0) + (S == ZG + 1 ? 2 : 0);
}
template <int S> DEV void z_stage_b(Ctx& c, const ZAddr& a, u32x4 (&xf)[2][16], ZW& F0, ZW& F1, f32x4 (&acc)[2][3], f32x4& cs,
                                    const unsigned (&wb)[3], ZPub& p) {
  const int lane = c.lane, w = c.w, rg = w & 3, t = lane & 15, q = lane >> 4;
  z_step<true, 2 * (S - 8), S, 1, S + 4, 1>(c, a, xf, F0, F1, acc, cs, 0, wb);
  if (S < 15) {
    if (S == ZG + 2) {
      asm volatile("s_waitcnt vmcnt(%2) ; TIE %0 %1" : "+v"(p.gs[0]), "+v"(p.gs[1]) : "n"(zb_wait(S)) : "memory");
      float pv[4];
      settle_two(p.gs[0], p.gs[1], p.sofs, p.sbase, c.Tlo, pv);
      const float sm = sumq(pv[0] + pv[2]);
      const float ss = sumq(pv[1] + pv[3]);
      const float m = sm * (1.0f / 688.0f);
      const float r = __builtin_amdgcn_rsqf(fmaxf(ss * (1.0f / 688.0f) - m * m, 0.f) + 1e-5f);
      if (lane < 16) {
        *(LAS float*)(c.L + T_ZR + (16 * w + lane) * 4) = r;
        *(LAS float*)(c.L + T_ZB + (16 * w + lane) * 4) = -m * r;
      }
    } else {
      wait_vm<zb_wait(S)>();
    }
    bar();
    if (S == ZG) { p.gs[0] = aload128c(p.sofs, p.sbase); p.gs[1] = aload128c(p.sofs, p.sbase + 2048); }
    if (S == ZG + 2) {
      f32x4 r4[2], b4[2];
#pragma unroll
      for (int x = 0; x < 2; ++x) {
        r4[x] = *(LAS f32x4*)(c.L + T_ZR + (rg * 32 + x * 16 + 4 * q) * 4);
        b4[x] = *(LAS f32x4*)(c.L + T_ZB + (rg * 32 + x * 16 + 4 * q) * 4);
      }
#pragma unroll
      for (int j = 0; j < 3; ++j) {
        u32x2 n[2];
#pragma unroll
        for (int x = 0; x < 2; ++x) {
          n[x].x = pk2(fma2(unpk(p.Z2[x][j].x), lo2(r4[x]), lo2(b4[x])));
          n[x].y = pk2(fma2(unpk(p.Z2[x][j].y), hi2(r4[x]), hi2(b4[x])));
        }
        const u32x4 fr = pairswap(n[0], n[1]);
        __builtin_amdgcn_raw_buffer_store_b128(fr, c.rs, p.zoff[j], 0, 16);
      }
      sb();
    }
  }
  z_step<true, 2 * (S - 8) + 1, (S < 15 ? S + 1 : -1), 0, S + 5, 0>(c, a, xf, F1, F0, acc, cs, 0, wb);
}

DEV int fb_start(int c) { return (int)(0x26211C16110C0600ull >> (8 * c)) & 0xff; }
DEV int fb_count(int c) { return 5 + ((0x13 >> c) & 1); }

DEV void job_z(Ctx& c, int i, int zc, int s0row, const G3A& g3, WF& W) {
  u32x4 xr; u32x2 ev;
  s0_issue(c, xr, ev, s0row);
  char* ws = c.ws;
  const int lane = c.lane, w = c.w, rg = w & 3, cg = w >> 2;
  const int t = lane & 15, q = lane >> 4;
  const int fb0 = fb_start(zc), nb = fb_count(zc);
  ZAddr a;
  {
    int r0 = 16 * w + (lane >> 3);
    a.vx0 = r0 * 1024 + (((lane & 7) ^ ((r0 >> 1) & 7)) << 4);
    int r1 = r0 + 8;
    a.vx1 = r1 * 1024 + (((lane & 7) ^ ((r1 >> 1) & 7)) << 4);
    a.bx = c.x + (size_t)i * 128 * 1024;
    int l48 = lane < 48 ? lane : 47;
    int kr = l48 / 12, cc = l48 % 12;
    int b = (cc >> 1) ^ (w & 1);
    int fbg = fb0 + b; fbg = fbg > 42 ? 42 : fbg;
    a.vw = (8 * w + kr) * 2752 + fbg * 32 + (cc & 1) * 16;
    a.bw = c.win;
  }
  z_half<0, 0>(c, a); z_half<0, 1>(c, a);
  if (PB & 4) bar0();
  z_half<1, 0>(c, a); z_half<1, 1>(c, a); z_half<2, 0>(c, a); z_half<2, 1>(c, a);
  s0_finish<12>(c, xr, ev, s0row);
  STAMP(15);
  z_half<3, 0>(c, a); z_half<3, 1>(c, a); z_half<4, 0>(c, a); z_half<4, 1>(c, a);
  if (PF & 1) tlb_touch(c);

  const unsigned xo = rg * 32 * 128 + t * 128 + ((q ^ (t >> 1)) << 4);
  unsigned wb[3]; int lb3[3];
#pragma unroll
  for (int j = 0; j < 3; ++j) {
    int jr = j + (rg == 3 ? 0 : rg); jr = jr >= 3 ? jr - 3 : jr;
    lb3[j] = 3 * cg + jr;
    wb[j] = (8 * q + (t >> 2)) * 192 + ((lb3[j] ^ (q & 1)) << 5) + (t & 3) * 8;
  }
  const int gj = c.tid < 128 ? c.tid : 127;
  const unsigned gofs = (i * 128 + gj) * 16;
  const f32x4 z4 = {0.f, 0.f, 0.f, 0.f};
  u32x4 xf[2][16];
  f32x4 acc[2][3], cs = z4;
#pragma unroll
  for (int j = 0; j < 3; ++j) { acc[0][j] = z4; acc[1][j] = z4; }
  u32x4 g = {0, 0, 0, 0};
  ZW F0, F1;
  wait_vm<16 + (PF & 1)>();
  bar();
  z_read_x<0, 0>(c, xf, xo); z_read_w<0, 0>(c, F0, wb);
  z_stage_a<0>(c, a, xf, F0, F1, acc, cs, xo, wb, g, gofs); z_stage_a<1>(c, a, xf, F0, F1, acc, cs, xo, wb, g, gofs);
  z_stage_a<2>(c, a, xf, F0, F1, acc, cs, xo, wb, g, gofs); z_stage_a<3>(c, a, xf, F0, F1, acc, cs, xo, wb, g, gofs);
  z_stage_a<4>(c, a, xf, F0, F1, acc, cs, xo, wb, g, gofs); z_stage_a<5>(c, a, xf, F0, F1, acc, cs, xo, wb, g, gofs);
  z_stage_a<6>(c, a, xf, F0, F1, acc, cs, xo, wb, g, gofs); z_stage_a<7>(c, a, xf, F0, F1, acc, cs, xo, wb, g, gofs);
  asm volatile("" : "+v"(acc[0][0]), "+v"(acc[0][1]), "+v"(acc[0][2]), "+v"(acc[1][0]), "+v"(acc[1][1]), "+v"(acc[1][2]), "+v"(cs));
  STAMP(2);
  unsigned gm, gr;
  settle_one(g, gofs, c.ws + WS_XS, c.Tlo, gm, gr);
  if (c.tid < 128) {
    *(LAS unsigned*)(c.L + T_XM + c.tid * 4) = gm;
    *(LAS unsigned*)(c.L + T_XR + c.tid * 4) = gr;
  }
  if (rg < 3 && lane < 16) *(LAS float*)(c.L + T_CS + (lb3[0] * 16 + lane) * 4) = cs[0];
  bar();
  if (cg == 0) { z_park_issue<0>(c, g3); z_park_issue<1>(c, g3); }
  sb();
  ZPub pub;
  {
    float ncs[3], on[3];
#pragma unroll
    for (int j = 0; j < 3; ++j) {
      ncs[j] = -*(LAS float*)(c.L + T_CS + (lb3[j] * 16 + t) * 4);
      on[j] = lb3[j] < nb ? 1.0f : 0.0f;
    }
    float st[2][8];
#pragma unroll
    for (int x = 0; x < 2; ++x) {
      const f32x4 m4 = *(LAS f32x4*)(c.L + T_XM + (rg * 32 + x * 16 + 4 * q) * 4);
      const f32x4 r4 = *(LAS f32x4*)(c.L + T_XR + (rg * 32 + x * 16 + 4 * q) * 4);
      f32x2 s1a = {0.f, 0.f}, s1b = s1a, s2a = s1a, s2b = s1a;
#pragma unroll
      for (int j = 0; j < 3; ++j) {
        const f32x4 v = acc[x][j];
        const f32x2 ya = gelu2(lo2(r4) * fma2(lo2(m4), splat(ncs[j]), lo2(v)));
        const f32x2 yb = gelu2(hi2(r4) * fma2(hi2(m4), splat(ncs[j]), hi2(v)));
        pub.Z2[x][j].x = pk2(ya); pub.Z2[x][j].y = pk2(yb);
        const f32x2 ma = ya * on[j], mb = yb * on[j];
        s1a += ma; s1b += mb;
        s2a = fma2(ma, ya, s2a); s2b = fma2(mb, yb, s2b);
      }
      st[x][0] = s1a.x; st[x][1] = s1a.y; st[x][2] = s1b.x; st[x][3] = s1b.y;
      st[x][4] = s2a.x; st[x][5] = s2a.y; st[x][6] = s2b.x; st[x][7] = s2b.y;
    }
    float rs[2][2];
    red16(st, rs);
    if ((t & 3) == 0) {
#pragma unroll
      for (int x = 0; x < 2; ++x) {
        f32x2 o = {rs[x][0], rs[x][1]};
        *(LAS f32x2*)(c.L + T_ZP + (cg * 128 + rg * 32 + x * 16 + 4 * q + (t >> 2)) * 8) = o;
      }
    }
  }
  sb();
  if (cg != 0) { z_park_issue<0>(c, g3); z_park_issue<1>(c, g3); }
  bar();
  if (c.tid < 256) {
    int tok = c.tid >> 1, st = c.tid & 1;
    float v = *(LAS float*)(c.L + T_ZP + tok * 8 + st * 4) + *(LAS float*)(c.L + T_ZP + (128 + tok) * 8 + st * 4);
    gstore64(ws + WS_ZS + ((i * 8 + zc) * 128 + tok) * 16 + st * 8, ((u64)c.Tlo << 32) | __float_as_uint(v));
  }
  STAMP(3);
  cs = z4;
#pragma unroll
  for (int j = 0; j < 3; ++j) { acc[0][j] = z4; acc[1][j] = z4; }
  {
    const u32x4 zero = {0, 0, 0, 0};
    pub.gs[0] = zero; pub.gs[1] = zero;
    pub.sofs = ((2 * q) * 128 + 16 * w + t) * 16;
    pub.sbase = ws + WS_ZS + (size_t)i * 8 * 128 * 16;
    const int qp = ((q & 1) << 1) | (q >> 1);
#pragma unroll
    for (int j = 0; j < 3; ++j)
      pub.zoff[j] = lb3[j] < nb ? WS_Z2N + ((i * 4 + rg) * 44 + fb0 + lb3[j]) * 1024 + qp * 256 + t * 16 : OOB;
  }
  z_read_w<8, 0>(c, F0, wb);
  z_stage_b<8>(c, a, xf, F0, F1, acc, cs, wb, pub);  z_stage_b<9>(c, a, xf, F0, F1, acc, cs, wb, pub);
  z_stage_b<10>(c, a, xf, F0, F1, acc, cs, wb, pub); z_stage_b<11>(c, a, xf, F0, F1, acc, cs, wb, pub);
  z_stage_b<12>(c, a, xf, F0, F1, acc, cs, wb, pub); z_stage_b<13>(c, a, xf, F0, F1, acc, cs, wb, pub);
  z_stage_b<14>(c, a, xf, F0, F1, acc, cs, wb, pub); z_stage_b<15>(c, a, xf, F0, F1, acc, cs, wb, pub);
  asm volatile("" : "+v"(acc[0][0]), "+v"(acc[0][1]), "+v"(acc[0][2]), "+v"(acc[1][0]), "+v"(acc[1][1]), "+v"(acc[1][2]), "+v"(cs));
  STAMP(4);
  w_take_from<0>(c, W, z_park(0, 0) + w * 2048, z_park(0, 1) + w * 2048);
  w_take_from<1>(c, W, z_park(1, 0) + w * 2048, z_park(1, 1) + w * 2048);
  if (rg < 3 && t == 0) *(LAS f32x4*)(c.L + T_CS + 512 + (lb3[0] * 16 + 4 * q) * 4) = cs;
  wait_vm<0>();
  bar();
  if (c.tid < NREP) gstore64(ws + WS_FZ2 + c.tid * 16384 + (zc * 16 + i) * 128, tagT(c));
  STAMP(18);
  w_issue<2>(c, g3); w_issue<3>(c, g3);
  STAMP(19);
  w_issue<4>(c, g3); w_issue<5>(c, g3);
  sb();
  STAMP(5);
  {
    f32x2 nm[2], rs[2];
#pragma unroll
    for (int x = 0; x < 2; ++x) {
      nm[x] = splat(-*(LAS float*)(c.L + T_XM + (rg * 32 + x * 16 + t) * 4));
      rs[x] = splat(*(LAS float*)(c.L + T_XR + (rg * 32 + x * 16 + t) * 4));
    }
#pragma unroll
    for (int j = 0; j < 3; ++j) {
      const f32x4 c4 = *(LAS f32x4*)(c.L + T_CS + 512 + (lb3[j] * 16 + 4 * q) * 4);
      u32x2 n[2];
#pragma unroll
      for (int x = 0; x < 2; ++x) {
        n[x].x = pk2(gelu2(rs[x] * fma2(nm[x], lo2(c4), lo2(acc[x][j]))));
        n[x].y = pk2(gelu2(rs[x] * fma2(nm[x], hi2(c4), hi2(acc[x][j]))));
      }
      const u32x4 fr = pairswap(n[0], n[1]);
      const int fb = fb0 + lb3[j];
      const unsigned off = WS_Z1 + ((i * 8 + rg * 2 + (q & 1)) * 22 + (fb >> 1)) * 1024 + (2 * (fb & 1) + (q >> 1)) * 256 + t * 16;
      __builtin_amdgcn_raw_buffer_store_b128(fr, c.rs, lb3[j] < nb ? off : OOB, 0, 16);
    }
  }
  STAMP(25);
  wait_vm<3>();
  w_take<2>(c, W); w_take<3>(c, W); w_take<4>(c, W); w_take<5>(c, W);
  wait_lds();
  if (w != 0) { w_issue<6>(c, g3); w_issue<7>(c, g3); }
}

struct HAddr { unsigned va, vw; const char *bo, *bu, *bwo, *bwout; };
constexpr unsigned h_slot(int T) { return (T & 7) < 4 ? 90112 + (T & 7) * 16384 : (T & 7) == 4 ? 73728 : ((T & 7) - 5) * 16384; }
template <int T> DEV void h_issue_w(const Ctx& c, const HAddr& a) {
  const unsigned l = c.lb + h_slot(T) + 8192 + c.w * 1024;
  if (T < 8) dma16(a.vw, a.bwo + (size_t)T * 64 * 1024, l);
  else if (T < 18) dma16(a.vw, a.bwout + (size_t)(T - 8) * 64 * 1024, l);
  else {
    int k = 8 * c.w + (c.lane >> 3);
    unsigned v = k > 47 ? a.vw - (k - 47) * 1024 : a.vw;
    dma16(v, a.bwout + (size_t)640 * 1024, l);
  }
}
template <int T> DEV void h_issue_a(const Ctx& c, const HAddr& a) {
  const unsigned l = c.lb + h_slot(T) + c.w * 1024;
  if (T < 8) dma16c(a.va, a.bo + T * 2048, l);
  else dma16c(a.va, a.bu + (T - 8) * 2048, l);
}
struct HF { u32x4 b[2], f0[2], f1[2]; };
template <int T> DEV void h_read(const Ctx& c, HF& F, unsigned ao, unsigned w0, unsigned w1) {
  LAS char* L = c.L + h_slot(T);
#pragma unroll
  for (int kk = 0; kk < 2; ++kk) {
    F.b[kk] = lds128(L + ao + kk * 4096);
    F.f0[kk] = trfrag(L + 8192 + w0 + kk * 4096, 512);
    F.f1[kk] = trfrag(L + 8192 + w1 + kk * 4096, 512);
  }
  sb();
}
template <int T, int IA, int IW> DEV void h_mma(const Ctx& c, const HAddr& h, const HF& F, f32x4 (&acc)[2]) {
  if (IA >= 0) h_issue_a<(IA < 0 ? 0 : IA)>(c, h);
  sb();
  acc[0] = mfma32(F.f0[0], F.b[0], acc[0]);
  acc[1] = mfma32(F.f1[0], F.b[0], acc[1]);
  sb();
  if (IW >= 0) h_issue_w<(IW < 0 ? 0 : IW)>(c, h);
  sb();
  u32x4 b = F.b[1];
  if (T == 18) {
    if (c.lane >= 32) { u32x4 z = {0, 0, 0, 0}; b = z; }
  }
  acc[0] = mfma32(F.f0[1], b, acc[0]);
  acc[1] = mfma32(F.f1[1], b, acc[1]);
  sb();
}
template <int T, int IA, int IW, bool NEXT> DEV void h_it(const Ctx& c, const HAddr& h, HF& Fc, HF& Fn, f32x4 (&acc)[2],
                                                         unsigned ao, unsigned w0, unsigned w1) {
  bar0();
  if (NEXT) h_read<T + 1>(c, Fn, ao, w0, w1);
  h_mma<T, IA, IW>(c, h, Fc, acc);
}

struct GF { u32x4 a[6]; };

constexpr int g_z(int s) { return s <= 11 ? 3 : 0; }
constexpr int g_ex(int s) { return s == 5 ? 1 : s == 9 ? 4 : (s >= 12 && s <= 14) ? 2 : 0; }
constexpr int g_wait(int s) {
  return g_ex(s - 3) + g_z(s - 2) + g_ex(s - 2) + g_z(s - 1) + g_ex(s - 1);
}
constexpr unsigned g_slot(int s) { return s % 5 < 4 ? W_STG + (s % 5) * G_SLOT : 0; }
DEV void dma16c3(unsigned voff, const char* sbase, unsigned lds) {
  asm volatile("s_mov_b32 m0, %2\n\ts_nop 0\n\tglobal_load_lds_dwordx4 %0, %1 sc1\n\tglobal_load_lds_dwordx4 %0, %1 offset:1024 sc1\n\t"
               "global_load_lds_dwordx4 %0, %1 offset:2048 sc1" ::"v"(voff), "s"(uni(sbase)), "s"(uni(lds)) : "memory");
}

DEV void dma16c3m(unsigned voff, const char* sbase, unsigned lds, u64 mask) {
  asm volatile("s_mov_b32 m0, %2\n\ts_nop 0\n\tglobal_load_lds_dwordx4 %0, %1 sc1\n\tglobal_load_lds_dwordx4 %0, %1 offset:1024 sc1\n\t"
               "s_mov_b64 exec, %3\n\tglobal_load_lds_dwordx4 %0, %1 offset:2048 sc1\n\ts_mov_b64 exec, -1"
               ::"v"(voff), "s"(uni(sbase)), "s"(uni(lds)), "s"(mask) : "memory");
}

template <bool isz> DEV void job_g(Ctx& c, int rt, int gc, const G3A& g3, WF& W, bool epoch_wg, int pi, int pc) {
  char* ws = c.ws;
  const int lane = c.lane, w = c.w;
  const int t = lane & 15, q = lane >> 4;
  const int rg = w & 1, kq = w >> 1;
  const int fb0 = fb_start(gc), nb = fb_count(gc);
  const u64 T = tagT(c);
  if (w == 0) poll_flags(ws + WS_FZ2 + (rt & (NREP - 1)) * 16384 + (gc * 16 + (lane & 15)) * 128, lane < 16, T);
  bar();
  STAMP(8);
  if (isz && w == 0) { w_issue<6>(c, g3); w_issue<7>(c, g3); }
  const char* zb = ws + WS_Z2N + (size_t)fb0 * 1024;
  const unsigned vz = (kq * 16 * 44 + 3 * rg) * 1024 + lane * 16;
  const unsigned lz = c.lb + w * 3072;
  const u64 m3 = rg ? 1ull : ~0ull;
  auto z_stage = [&](auto ss, auto nn) {
    constexpr int S = decltype(ss)::v;
    if (decltype(nn)::v == 6) dma16c3(vz, zb + ((g3.k0 + S) & 15) * 45056, lz + g_slot(S));
    else dma16c3m(vz, zb + ((g3.k0 + S) & 15) * 45056, lz + g_slot(S), m3);
  };
  constexpr IC<(isz ? 5 : 6)> nbk;
  z_stage(IC<0>(), nbk);
  if (PB & 1) bar0();
  z_stage(IC<1>(), nbk);
  if (PB & 1) bar0();
  z_stage(IC<2>(), nbk); z_stage(IC<3>(), nbk);
  STAMP(24);
  HAddr h;
  {
    h.va = ((w & 3) * 16 + (w >> 2)) * 1024 + lane * 16;
    int k = 8 * w + (lane >> 3);
    int f = ((k >> 1) & 1) | (((k >> 3) & 1) << 1);
    h.vw = k * 1024 + (((lane & 7) ^ (f << 1)) << 4);
    h.bo = ws + WS_O + (size_t)rt * 4 * 16 * 1024;
    h.bu = ws + WS_U + (size_t)rt * 4 * 22 * 1024;
    h.bwo = c.wo + gc * 128;
    h.bwout = c.wout + gc * 128;
  }
  const unsigned vau = ((w & 3) * 22 + (w >> 2)) * 1024 + lane * 16;
  const unsigned ao_ = kq * 6144 + lane * 16;
  const f32x4 z4 = {0.f, 0.f, 0.f, 0.f};
  f32x4 acc[2][6];
#pragma unroll
  for (int j = 0; j < 6; ++j) { acc[0][j] = z4; acc[1][j] = z4; }
  const int tbk = rt * 4 + rg * 2 + (kq & 1), ch = kq >> 1;
  const unsigned flofs = lane < 8 ? WS_FO + ((rt >> 1) * 8 + lane) * 128 : WS_FZ1 + ((rt >> 1) * 8 + gc) * 128;
  unsigned z1ofs[3];
#pragma unroll
  for (int j = 0; j < 3; ++j) {
    int fb = fb0 + 3 * ch + j; fb = fb > 42 ? 42 : fb;
    z1ofs[j] = (tbk * 22 + (fb >> 1)) * 1024 + (2 * (fb & 1) + (q >> 1)) * 256 + t * 16 + (q & 1) * 8;
  }
  GF F;
  u32x2 fl, z1v[3];
  unsigned braw;
  wait_vm<9>();
  w_take<6>(c, W); w_take<7>(c, W);
  bar();
  STAMP(17);
  if (c.tid == 0) gstore64(ws + (isz ? WS_FZ1 : WS_FO) + (pi * 8 + pc) * 128, T);
#pragma unroll
  for (int j = 0; j < 6; ++j) F.a[j] = lds128(c.L + g_slot(0) + ao_ + j * 1024);
  sb();
  auto it = [&](auto ss, auto nn) {
    constexpr int S = decltype(ss)::v, NB = decltype(nn)::v;
    if (S < 15) {
      wait_vm<g_wait(S)>();
      asm volatile("s_barrier" ::: "memory");
    } else {
      bar();
    }
    auto req = [&]() {
      if (S <= 11) z_stage(IC<(S <= 11 ? S + 4 : 0)>(), nn);
      if (S >= 12 && S <= 14) { h_issue_a<(S >= 12 ? S - 12 : 0)>(c, h); h_issue_w<(S >= 12 ? S - 12 : 0)>(c, h); }
    };
    if (GA == 0 || (GA == 1 && (w & 4))) req();
    sb();
    LAS char* Ln = c.L + g_slot(S + 1) + ao_;
#pragma unroll
    for (int j = 0; j < NB; ++j) {
      if (GX != 2) {
        acc[0][j] = mfma32(F.a[j], W.f[2 * S], acc[0][j]);
        acc[1][j] = mfma32(F.a[j], W.f[2 * S + 1], acc[1][j]);
      }
      sb();
      if (S < 15 && GX != 2) F.a[j] = lds128(Ln + j * 1024);
      sb();
    }
    if (GA == 2 || (GA == 1 && !(w & 4))) req();
    sb();
    if (S == 5) { STAMP(21); fl = aload64c(flofs, ws); }
    if (S == 9) {
      asm volatile("; TIE %0" : "+v"(fl));
      if (!__all(fl.x == c.Tlo && fl.y == c.Thi)) poll_flags(ws + flofs, true, T);
      z1v[0] = aload64c(z1ofs[0], ws + WS_Z1);
      z1v[1] = aload64c(z1ofs[1], ws + WS_Z1);
      z1v[2] = aload64c(z1ofs[2], ws + WS_Z1);
      asm volatile("global_load_ushort %0, %1, %2" : "=v"(braw) : "v"((tbk * 16 + t) * 2), "s"(uni(c.bsp)) : "memory");
    }
    sb();
  };
  static_for<0, 16>([&](auto ss) { it(ss, IC<(isz ? 5 : 6)>()); });
  asm volatile("; TIE %0 %1 %2 %3" : "+v"(z1v[0]), "+v"(z1v[1]), "+v"(z1v[2]), "+v"(braw));
  const float bias = blo(braw);
  STAMP(9);
  {
    f32x4 keep[3];
#pragma unroll
    for (int g = 0; g < 4; ++g) {
      if (g != kq) {
        const unsigned e = G_EX + (rg + 2 * g) * 9216 + (((kq - g) & 3) - 1) * 3072 + lane * 16;
#pragma unroll
        for (int j = 0; j < 3; ++j) *(LAS f32x4*)(c.L + e + j * 1024) = acc[g & 1][3 * (g >> 1) + j];
      }
    }
#pragma unroll
    for (int j = 0; j < 3; ++j) {
      const f32x4 k0 = (kq & 1) ? acc[1][j] : acc[0][j], k1 = (kq & 1) ? acc[1][3 + j] : acc[0][3 + j];
      keep[j] = ch ? k1 : k0;
    }
    wait_vm<4>();
    bar();
    STAMP(22);
    const unsigned p = G_EX + w * 9216 + lane * 16;
#pragma unroll
    for (int j = 0; j < 3; ++j) {
      const f32x4 v1 = *(LAS f32x4*)(c.L + p + j * 1024), v2 = *(LAS f32x4*)(c.L + p + 3072 + j * 1024),
                  v3 = *(LAS f32x4*)(c.L + p + 6144 + j * 1024);
      keep[j] = (keep[j] + v1) + (v2 + v3);
    }
#pragma unroll
    for (int j = 0; j < 3; ++j) {
      unsigned g0 = pk2(keep[j][0] + bias, keep[j][1] + bias), g1 = pk2(keep[j][2] + bias, keep[j][3] + bias);
      unsigned a0 = z1v[j].x, a1 = z1v[j].y;
      u32x2 u;
      u.x = pk2(blo(a0) * blo(g0), bhi(a0) * bhi(g0));
      u.y = pk2(blo(a1) * blo(g1), bhi(a1) * bhi(g1));
      __builtin_amdgcn_raw_buffer_store_b64(u, c.rs, 3 * ch + j < nb ? WS_U + z1ofs[j] : OOB, 0, 16);
    }
  }
  STAMP(10);

  const int tb = w & 3, np = w >> 2;
  const unsigned ha = tb * 1024 + lane * 16;
  const int fw = ((t >> 3) & 1) | ((q & 1) << 1);
  const unsigned wl = (8 * q + (t >> 2)) * 128 + (t & 3) * 8;
  const unsigned hw0 = wl + (((2 * np) ^ fw) << 5), hw1 = wl + (((2 * np + 1) ^ fw) << 5);
  f32x4 sa[2] = {z4, z4};
  HF H0, H1;
  h_issue_a<3>(c, h); h_issue_w<3>(c, h); h_issue_a<4>(c, h); h_issue_w<4>(c, h);
  h_read<0>(c, H0, ha, hw0, hw1);
  if (FE == 3) {
    wait_vm<4>();
    bar0();
    if (c.tid == 0) gstore64(ws + WS_FU + (rt * 8 + gc) * 128, T);
    h_read<1>(c, H1, ha, hw0, hw1);
    h_mma<0, 5, 5>(c, h, H0, sa);
  } else {
    wait_vm<9>();  h_it<0, 5, 5, true>(c, h, H0, H1, sa, ha, hw0, hw1);
  }
  h_issue_a<6>(c, h); h_issue_w<6>(c, h);
  if (FE == 1) {
    wait_vm<8>();
    bar0();
    if (c.tid == 0) gstore64(ws + WS_FU + (rt * 8 + gc) * 128, T);
    h_read<2>(c, H0, ha, hw0, hw1);
    h_mma<1, 7, 7>(c, h, H1, sa);
  } else {
    wait_vm<11>(); h_it<1, 7, 7, true>(c, h, H1, H0, sa, ha, hw0, hw1);
  }
  h_issue_w<8>(c, h);
  wait_vm<9>();
  if (FE == 2) {
    bar0();
    if (c.tid == 0) gstore64(ws + WS_FU + (rt * 8 + gc) * 128, T);
    h_read<3>(c, H1, ha, hw0, hw1);
    h_mma<2, -1, 9>(c, h, H0, sa);
  } else {
    h_it<2, -1, 9, true>(c, h, H0, H1, sa, ha, hw0, hw1);
    if (FE == 0 && c.tid == 0) gstore64(ws + WS_FU + (rt * 8 + gc) * 128, T);
  }
  wait_vm<8>(); h_it<3, -1, 10, true>(c, h, H1, H0, sa, ha, hw0, hw1);
  u64 fu = 0;
  const unsigned fuofs = WS_FU + (rt * 8 + w) * 128;
  wait_vm<7>(); h_it<4, -1, 11, true>(c, h, H0, H1, sa, ha, hw0, hw1);
  if (SPOS == 4) sample_flag(fu, fuofs, ws);
  wait_vm<6 + (SPOS == 4)>(); h_it<5, -1, 12, true>(c, h, H1, H0, sa, ha, hw0, hw1);
  if (SPOS == 5) sample_flag(fu, fuofs, ws);
  wait_vm<5 + (SPOS == 4 || SPOS == 5)>(); h_it<6, -1, 13, true>(c, h, H0, H1, sa, ha, hw0, hw1);
  if (SPOS == 6) sample_flag(fu, fuofs, ws);
  h_it<7, -1, 14, false>(c, h, H1, H0, sa, ha, hw0, hw1);
  asm volatile("" : "+v"(sa[0]), "+v"(sa[1]));
  STAMP(11);
  h.va = vau;
  if (SPOS == 0) {
    if (w == 0) poll_flags(ws + WS_FU + (rt * 8 + (lane & 7)) * 128, lane < 8, T);
  } else if (!sampled<7 - SPOS>(fu, T)) poll_flags(ws + fuofs, true, T);
  if (epoch_wg && w == 1) {
#pragma unroll
    for (int j = 0; j < 2; ++j) {
      poll_flags(ws + WS_FZ2 + (j * 64 + lane) * 128, true, T);
      poll_flags(ws + WS_FO + (j * 64 + lane) * 128, true, T);
    }
    if (lane == 0) gstore64(ws + WS_EPOCH, T);
  }
  bar();
  STAMP(12);
  h_issue_a<8>(c, h); h_issue_a<9>(c, h);
  if (PB & 2) bar0();
  h_issue_a<10>(c, h); h_issue_a<11>(c, h); h_issue_a<12>(c, h); h_issue_a<13>(c, h);
  h_issue_a<14>(c, h);
  const int orow = rt * 64 + tb * 16 + t;
  const unsigned oofs = orow * 1024 + gc * 128 + np * 64 + (q & 1) * 32 + (q >> 1) * 16;
  f32x4 ga[2] = {z4, z4};
  wait_vm<6>();
  bar();
  STAMP(23);
  h_read<8>(c, H0, ha, hw0, hw1);
  wait_vm<5>(); h_it<8, 15, 15, true>(c, h, H0, H1, ga, ha, hw0, hw1);
  wait_vm<6>(); h_it<9, 16, 16, true>(c, h, H1, H0, ga, ha, hw0, hw1);
  wait_vm<7>(); h_it<10, 17, 17, true>(c, h, H0, H1, ga, ha, hw0, hw1);
  wait_vm<8>(); h_it<11, 18, 18, true>(c, h, H1, H0, ga, ha, hw0, hw1);
  wait_vm<9>(); h_it<12, -1, -1, true>(c, h, H0, H1, ga, ha, hw0, hw1);
  u32x4 xres;
  asm volatile("global_load_dwordx4 %0, %1, %2" : "=v"(xres) : "v"(oofs), "s"(uni(c.x)) : "memory");
  wait_vm<9>(); h_it<13, -1, -1, true>(c, h, H1, H0, ga, ha, hw0, hw1);
  wait_vm<7>(); h_it<14, -1, -1, true>(c, h, H0, H1, ga, ha, hw0, hw1);
  wait_vm<5>(); h_it<15, -1, -1, true>(c, h, H1, H0, ga, ha, hw0, hw1);
  wait_vm<3>(); h_it<16, -1, -1, true>(c, h, H0, H1, ga, ha, hw0, hw1);
  wait_vm<1>(); h_it<17, -1, -1, true>(c, h, H1, H0, ga, ha, hw0, hw1);
  h_mma<18, -1, -1>(c, h, H0, ga);
  asm volatile("s_waitcnt vmcnt(0) ; TIE %0" : "+v"(xres) : : "memory");
  STAMP(13);
  {
    u32x2 S[2], G[2];
#pragma unroll
    for (int b = 0; b < 2; ++b) {
      S[b].x = pk2(sa[b][0], sa[b][1]); S[b].y = pk2(sa[b][2], sa[b][3]);
      G[b].x = pk2(ga[b][0], ga[b][1]); G[b].y = pk2(ga[b][2], ga[b][3]);
    }
    u32x4 s8 = pairswap(S[0], S[1]), g8 = pairswap(G[0], G[1]);
    u32x4 o;
#pragma unroll
    for (int j = 0; j < 4; ++j) {
      unsigned su = s8[j], gu = g8[j], xu = xres[j];
      float g0 = blo(gu), g1 = bhi(gu);
      float e0 = g0 * __builtin_amdgcn_rcpf(1.0f + __builtin_amdgcn_exp2f(g0 * -1.4426950408889634f));
      float e1 = g1 * __builtin_amdgcn_rcpf(1.0f + __builtin_amdgcn_exp2f(g1 * -1.4426950408889634f));
      o[j] = pk2(__builtin_fmaf(blo(su), e0, blo(xu)), __builtin_fmaf(bhi(su), e1, bhi(xu)));
    }
    if (OUTSC == 0) *(GAS u32x4*)(size_t)(c.out + oofs) = o;
    else {
      __amdgpu_buffer_rsrc_t ro = __builtin_amdgcn_make_buffer_rsrc((void*)c.out, 0, 2048 * 1024, 0x00020000);
      __builtin_amdgcn_raw_buffer_store_b128(o, ro, oofs, 0, OUTSC == 1 ? 16 : OUTSC == 2 ? 2 : 17);
    }
  }
  STAMP(14); STAMPC(31);
}

__global__ __launch_bounds__(512, 2) void fused_kernel(const char* x, const char* wqkv, const char* wo, const char* win, const char* wsp,
                                                       const char* bsp, const char* wout, char* out, char* ws) {
  extern __shared__ __attribute__((aligned(16))) char smem[];
  Ctx c;
  c.x = x; c.wqkv = wqkv; c.wo = wo; c.win = win; c.wsp = wsp; c.bsp = bsp; c.wout = wout; c.out = out; c.ws = ws;
  c.L = (LAS char*)smem;
  c.lb = (unsigned)(size_t)c.L;
  c.tid = threadIdx.x; c.lane = c.tid & 63; c.w = __builtin_amdgcn_readfirstlane(c.tid >> 6);
  c.rs = __builtin_amdgcn_make_buffer_rsrc((void*)ws, 0, WS_USED, 0x00020000);
  c.Tlo = 0; c.Thi = 0;
  c.bid = blockIdx.x;
  STAMP(0); STAMPC(30);
  const int bid = blockIdx.x, xcd = bid & 7, sl = bid >> 3;
  const int i = (xcd & 1) * 8 + (sl >> 2), rsel = sl & 3, col = 2 * (xcd >> 1) + (rsel & 1);
  const int r16 = (xcd >> 1) * 4 + rsel;
  const int rt = (xcd >> 1) * 8 + (sl >> 2), gc = (xcd & 1) * 4 + (sl & 3);
  const int s0row = i * 128 + r16 * 8 + c.w;
  const G3A g3 = g3_addr(c, rt, sl);
  WF W;
  if (c.w == 7) {
    u64 pc;
    asm volatile("s_getpc_b64 %0" : "=s"(pc));
    Ctx0 k = {c.lb + T_DUMMY};
    touch(k, (const char*)(size_t)pc + (sl % 6) * 8192 + c.lane * 128);
  }
  if (rsel < 2) {
    job_a(c, i, col, s0row, g3, W, rt, rsel, xcd & 1);
    STAMP(7);
    job_g<false>(c, rt, gc, g3, W, bid == 0, i, col);
  } else {
    job_z(c, i, col, s0row, g3, W);
    STAMP(7);
    job_g<true>(c, rt, gc, g3, W, false, i, col);
  }
}

extern "C" void kernel_launch(void* const* d_in, const int* in_sizes, int n_in, void* d_out, int out_size, void* d_ws, size_t ws_size,
                              hipStream_t stream) {
  (void)in_sizes; (void)n_in; (void)out_size; (void)ws_size;
  (void)hipFuncSetAttribute((const void*)fused_kernel, hipFuncAttributeMaxDynamicSharedMemorySize, LDS_BYTES);
  fused_kernel<<<256, 512, LDS_BYTES, stream>>>((const char*)d_in[0], (const char*)d_in[1], (const char*)d_in[2], (const char*)d_in[4],
                                                (const char*)d_in[5], (const char*)d_in[6], (const char*)d_in[7], (char*)d_out, (char*)d_ws);
}
```
